```python
import torch

def _softmax(x, axis=-1):
    return torch.softmax(x, dim=axis)
def _gelu(x):
    return torch.nn.functional.gelu(x, approximate="tanh")
def _silu(x):
    return torch.nn.functional.silu(x)
def _sigmoid(x):
    return torch.sigmoid(x)
def _layernorm(x, axis=-1, eps=1e-5):
    m = x.mean(dim=axis, keepdim=True)
    v = x.var(dim=axis, keepdim=True, unbiased=False)
    return (x - m) / torch.sqrt(v + eps)
def _scatter_add(tgt, idx, src, axis=0):
    return tgt.clone().index_add_(axis, idx, src)
def _segment_sum(src, seg, n):
    out = torch.zeros((n,) + src.shape[1:], dtype=src.dtype)
    return out.index_add_(0, seg, src)
def _pad(x, pw):
    flat = tuple(v for pair in reversed(pw) for v in pair)
    return torch.nn.functional.pad(x, flat)

kernel_name = 'standalone__mha_softmax__near_ridge'

def setup_inputs():

    _w = (lambda a: (lambda t: t.to(torch.bfloat16) if t.is_floating_point() else t)(torch.as_tensor(a)))
    B, H, Sq, Sk, D = 8, 32, 736, 512, 128
    return {
        "q": _w((lambda *s: torch.randn(*s, dtype=torch.bfloat16) * 0.8)(B, H, Sq, D)),
        "k": _w((lambda *s: torch.randn(*s, dtype=torch.bfloat16) * 0.8)(B, H, Sk, D)),
        "v": _w((lambda *s: torch.randn(*s, dtype=torch.bfloat16) * 0.05)(B, H, Sk, D)),
    }

def reference(q, k, v):
    scale = q.shape[-1] ** -0.5
    scores = torch.einsum("bhqd,bhkd->bhqk", q, k) * scale
    p = _softmax(scores, axis=-1)
    return torch.einsum("bhqk,bhkd->bhqd", p, v)
```

```cpp
#include <hip/hip_runtime.h>
#include <hip/hip_bf16.h>
#include <cstdio>

using bf16 = __hip_bfloat16;
constexpr int   D = 128, NW = 8, QBLK = 32, KVBLK = 64;
constexpr float SCALE = 0.088388347648318440f;
#define ATTN_THR 8.f
constexpr float THR = ATTN_THR;
#define ATTN_LAYOUT 0
constexpr int LAYOUT = ATTN_LAYOUT;
#define ATTN_SDEPTH 2
constexpr int SDEPTH = ATTN_SDEPTH;
static_assert(SDEPTH == 1 || SDEPTH == 2, "SDEPTH is 1 or 2");
#define ATTN_B 8
#define ATTN_HQ 32
#define ATTN_HKV 32
#define ATTN_S 736
#define ATTN_SKV 512
constexpr int B = ATTN_B, HQ = ATTN_HQ, HKV = ATTN_HKV, S = ATTN_S, SKV = ATTN_SKV;
constexpr int LDQ = LAYOUT ? HQ * D : D, LDK = LAYOUT ? HKV * D : D, LDO = LDQ;
static_assert(SKV % (2 * KVBLK) == 0 && HQ % HKV == 0, "S % 256 == 0, SKV % 128 == 0 (even tile count), HQ % HKV == 0");
constexpr size_t SHM_V = KVBLK * D * 2, SHM_K = KVBLK * D * 2, SHM_ATTN = 2 * SHM_V + 2 * SHM_K + NW * 64 * 4;

using bf16x8 = __attribute__((ext_vector_type(8))) short;
using s16x4  = __attribute__((ext_vector_type(4))) short;
using f32x16 = __attribute__((ext_vector_type(16))) float;
using f32x8  = __attribute__((ext_vector_type(8))) float;
using u32x4  = __attribute__((ext_vector_type(4))) unsigned;
#define KSWZ(row, colB) ((row) * 256 + ((colB) ^ (((row) & 7) << 4)))
#define SBAR() __builtin_amdgcn_sched_barrier(0)
__device__ __forceinline__ int crow(int r, int hi) { return (r & 3) + 8 * (r >> 2) + 4 * hi; }
__device__ __forceinline__ unsigned cvtpk(float lo, float hi) {
  unsigned r; asm volatile("v_cvt_pk_bf16_f32 %0, %1, %2" : "=v"(r) : "v"(lo), "v"(hi)); return r;
}
template <typename TIn> struct Stage;
template <> struct Stage<bf16>  { using T = bf16x8;
  __device__ static __forceinline__ T ld8(const bf16* p) { return *reinterpret_cast<const bf16x8*>(p); }
  __device__ static __forceinline__ bf16x8 tobf(T x) { return x; } };
template <> struct Stage<float> { using T = f32x8;
  __device__ static __forceinline__ T ld8(const float* p) { return *reinterpret_cast<const f32x8*>(p); }
  __device__ static __forceinline__ bf16x8 tobf(T x) {
    u32x4 w = {cvtpk(x[0], x[1]), cvtpk(x[2], x[3]), cvtpk(x[4], x[5]), cvtpk(x[6], x[7])}; return *reinterpret_cast<bf16x8*>(&w); } };

__device__ __forceinline__ void partialSM(f32x16& p0, f32x16& p1, float& m_reg, float& mn, float& alpha) {
  constexpr float C = SCALE * 1.4426950408889634f;
  float pmax = p0[0]; for (int r = 1; r < 16; ++r) pmax = fmaxf(pmax, p0[r]); for (int r = 0; r < 16; ++r) pmax = fmaxf(pmax, p1[r]);
  { auto rr = __builtin_amdgcn_permlane32_swap(__float_as_uint(pmax), __float_as_uint(pmax), false, false);
    pmax = fmaxf(__uint_as_float(rr[0]), __uint_as_float(rr[1])); }
  if (__builtin_expect(__all(pmax - m_reg <= THR / SCALE), 1)) { mn = m_reg; alpha = 1.f; }
  else { mn = fmaxf(m_reg, pmax); alpha = __builtin_amdgcn_exp2f((m_reg - mn) * C); m_reg = mn; }
  float mnC = -mn * C;
  for (int r = 0; r < 16; ++r) p0[r] = fmaf(p0[r], C, mnC); for (int r = 0; r < 16; ++r) p1[r] = fmaf(p1[r], C, mnC);
  for (int r = 0; r < 16; ++r) p0[r] = __builtin_amdgcn_exp2f(p0[r]);
}
__device__ __forceinline__ void finishSM(f32x16& p0, f32x16& p1, float alpha, float& l_reg, bf16x8& pa0, bf16x8& pa1, bf16x8& pa2, bf16x8& pa3) {
  for (int r = 0; r < 16; ++r) p1[r] = __builtin_amdgcn_exp2f(p1[r]);
  float ps = 0; for (int r = 0; r < 16; ++r) ps += p0[r]; for (int r = 0; r < 16; ++r) ps += p1[r];
  { auto rr = __builtin_amdgcn_permlane32_swap(__float_as_uint(ps), __float_as_uint(ps), false, false);
    ps = __uint_as_float(rr[0]) + __uint_as_float(rr[1]); }
  l_reg = l_reg * alpha + ps;
#define PK4(P, BASE, OUT) do { unsigned a0 = cvtpk(P[BASE + 0], P[BASE + 1]), a1 = cvtpk(P[BASE + 2], P[BASE + 3]);   \
    unsigned b0 = cvtpk(P[BASE + 4], P[BASE + 5]), b1 = cvtpk(P[BASE + 6], P[BASE + 7]);                              \
    auto r0 = __builtin_amdgcn_permlane32_swap(a0, b0, false, false); auto r1 = __builtin_amdgcn_permlane32_swap(a1, b1, false, false); \
    u32x4 w = {r0[0], r1[0], r0[1], r1[1]}; OUT = *reinterpret_cast<bf16x8*>(&w); } while (0)
  PK4(p0, 0, pa0); PK4(p0, 8, pa1); PK4(p1, 0, pa2); PK4(p1, 8, pa3);
#undef PK4
}
__device__ __forceinline__ void qkt(f32x16& p0, f32x16& p1, const bf16* Ks, const bf16x8* qr, int r32, int hi) {
  p0 = f32x16{}; p1 = f32x16{};
  for (int d0 = 0; d0 < 8; ++d0) { int cb = (d0 * 16 + hi * 8) * 2;
    bf16x8 b0 = *reinterpret_cast<const bf16x8*>((const char*)Ks + KSWZ(r32, cb));
    bf16x8 b1 = *reinterpret_cast<const bf16x8*>((const char*)Ks + KSWZ(32 + r32, cb));
    p0 = __builtin_amdgcn_mfma_f32_32x32x16_bf16(b0, qr[d0], p0, 0, 0, 0);
    p1 = __builtin_amdgcn_mfma_f32_32x32x16_bf16(b1, qr[d0], p1, 0, 0, 0); }
}
__device__ __forceinline__ int v_st(int k, int c) { const int kk = (k & ~0xC) | ((k & 4) << 1) | ((k & 8) >> 1); return ((kk >> 3) * 4 + (c >> 5)) * 512 + ((kk & 7) * 32 + (c & 31)) * 2; }
__device__ __forceinline__ int v_rd_base(int lane) { return ((lane & 3) << 3) | (((lane >> 2) & 3) << 6) | (((lane >> 4) & 1) << 5) | (((lane >> 5) & 1) << 8); }
constexpr int v_rd_off(int d0, int ks, int half) { return d0 * 512 + ks * 4096 + half * 2048; }
template <int OFF> __device__ __forceinline__ s16x4 tr_read(int vb) {
  s16x4 r; asm volatile("ds_read_b64_tr_b16 %0, %1 offset:%2" : "=&v"(r) : "v"(vb), "i"(OFF) : "memory"); return r;
}
template <int D0> __device__ __forceinline__ void pv_one(f32x16& od, int vb, bf16x8 pa0, bf16x8 pa1, bf16x8 pa2, bf16x8 pa3) {
  const s16x4 l0 = tr_read<v_rd_off(D0, 0, 0)>(vb), h0 = tr_read<v_rd_off(D0, 0, 1)>(vb), l1 = tr_read<v_rd_off(D0, 1, 0)>(vb), h1 = tr_read<v_rd_off(D0, 1, 1)>(vb);
  const s16x4 l2 = tr_read<v_rd_off(D0, 2, 0)>(vb), h2 = tr_read<v_rd_off(D0, 2, 1)>(vb), l3 = tr_read<v_rd_off(D0, 3, 0)>(vb), h3 = tr_read<v_rd_off(D0, 3, 1)>(vb);
  asm volatile("s_waitcnt lgkmcnt(0)" ::: "memory"); SBAR();
#define PK(L, H) (bf16x8){L[0], L[1], L[2], L[3], H[0], H[1], H[2], H[3]}
  od = __builtin_amdgcn_mfma_f32_32x32x16_bf16(pa0, PK(l0, h0), od, 0, 0, 0);
  od = __builtin_amdgcn_mfma_f32_32x32x16_bf16(pa1, PK(l1, h1), od, 0, 0, 0);
  od = __builtin_amdgcn_mfma_f32_32x32x16_bf16(pa2, PK(l2, h2), od, 0, 0, 0);
  od = __builtin_amdgcn_mfma_f32_32x32x16_bf16(pa3, PK(l3, h3), od, 0, 0, 0);
#undef PK
}
__device__ __forceinline__ void pv_d0(f32x16* o, int vb, bf16x8 pa0, bf16x8 pa1, bf16x8 pa2, bf16x8 pa3) {
  pv_one<0>(o[0], vb, pa0, pa1, pa2, pa3); pv_one<1>(o[1], vb, pa0, pa1, pa2, pa3); pv_one<2>(o[2], vb, pa0, pa1, pa2, pa3); pv_one<3>(o[3], vb, pa0, pa1, pa2, pa3);
}

template <typename TQ>
__device__ __forceinline__ void attn_dense_body(const TQ* __restrict__ Qb, const bf16* __restrict__ Kh, const bf16* __restrict__ Vh,
                                                bf16* __restrict__ Ob, int seq, char* lds, int nrows) {
  using St = Stage<bf16>; using SQ = Stage<TQ>;
  const int tid = threadIdx.x, wid = tid >> 6, lane = tid & 63, r32 = lane & 31, hi = lane >> 5;
  bf16* V_lds = (bf16*)lds; bf16* K_lds = (bf16*)(lds + 2 * SHM_V);
  float* ws = (float*)(lds + 2 * SHM_V + 2 * SHM_K) + wid * 64; float* li_l = ws; float* al_l = ws + 32;
  float m_reg = -1e30f, l_reg = 0; f32x16 o[4] = {}; bf16x8 qr[8];
  const bool wok = wid * QBLK < nrows; const TQ* Qw = Qb + (long)((wok ? wid * QBLK : 0) + r32) * LDQ + hi * 8;
#pragma unroll
  for (int d0 = 0; d0 < 8; ++d0) qr[d0] = SQ::tobf(SQ::ld8(Qw + d0 * 16));
  const int sr = tid >> 4, sc = (tid & 15) * 8, vst0 = v_st(sr, sc), vst1 = v_st(32 + sr, sc);
  const int vb0 = (int)(uintptr_t)V_lds + v_rd_base(lane);
  struct { typename St::T vs0, vs1, ks0, ks1; } sr_[SDEPTH];
#define SLOAD(i, k0) do { sr_[i].vs0 = St::ld8(&Vh[(long)((k0) + sr) * LDK + sc]); sr_[i].vs1 = St::ld8(&Vh[(long)((k0) + 32 + sr) * LDK + sc]); \
    sr_[i].ks0 = St::ld8(&Kh[(long)((k0) + sr) * LDK + sc]); sr_[i].ks1 = St::ld8(&Kh[(long)((k0) + 32 + sr) * LDK + sc]); } while (0)
#define SWRITE(b, i) do { *(bf16x8*)((char*)V_lds + (b) * SHM_V + vst0) = St::tobf(sr_[i].vs0);          \
    *(bf16x8*)((char*)V_lds + (b) * SHM_V + vst1) = St::tobf(sr_[i].vs1); int kc = sc * 2;               \
    *(bf16x8*)((char*)K_lds + (b) * SHM_K + KSWZ(sr, kc)) = St::tobf(sr_[i].ks0);                       \
    *(bf16x8*)((char*)K_lds + (b) * SHM_K + KSWZ(32 + sr, kc)) = St::tobf(sr_[i].ks1); } while (0)
#define SWAIT() do { if constexpr (SDEPTH == 2) asm volatile("s_waitcnt vmcnt(4)" ::: "memory"); else asm volatile("s_waitcnt vmcnt(0)" ::: "memory"); } while (0)
#define RESC(a) do { if (__any((a) < 1.f)) { if (hi == 0) al_l[r32] = (a); asm volatile("s_waitcnt lgkmcnt(0)" ::: "memory"); \
    for (int d = 0; d < 4; ++d) for (int r = 0; r < 16; ++r) o[d][r] *= al_l[crow(r, hi)]; } } while (0)
  f32x16 pA0, pA1, pB0, pB1; float mnA, mnB, alA, alB; bf16x8 pa0, pa1, pa2, pa3; const int NT = seq / KVBLK;
  constexpr int SE = 0, SO = SDEPTH - 1;
  SLOAD(SE, 0); asm volatile("s_waitcnt vmcnt(0)" ::: "memory"); SWRITE(0, SE); __syncthreads();
  qkt(pA0, pA1, K_lds, qr, r32, hi); partialSM(pA0, pA1, m_reg, mnA, alA);
  SLOAD(SO, KVBLK); if constexpr (SDEPTH == 2) { if (2 < NT) SLOAD(SE, 2 * KVBLK); }
  SWAIT(); SWRITE(1, SO); __syncthreads();
  for (int j = 1; j + 1 < NT; j += 2) {
    SBAR(); qkt(pB0, pB1, (bf16*)((char*)K_lds + SHM_K), qr, r32, hi);
    finishSM(pA0, pA1, alA, l_reg, pa0, pa1, pa2, pa3); SBAR();
    SLOAD(SO, (j + SDEPTH) * KVBLK); SBAR();
    pv_d0(o, vb0, pa0, pa1, pa2, pa3); partialSM(pB0, pB1, m_reg, mnB, alB);
    __syncthreads(); SWAIT(); SWRITE(0, SE);
    RESC(alB); __syncthreads();
    SBAR(); qkt(pA0, pA1, K_lds, qr, r32, hi);
    finishSM(pB0, pB1, alB, l_reg, pa0, pa1, pa2, pa3); SBAR();
    if (SDEPTH == 1 || j + 3 < NT) SLOAD(SE, (j + 1 + SDEPTH) * KVBLK); SBAR();
    pv_d0(o, vb0 + (int)SHM_V, pa0, pa1, pa2, pa3); partialSM(pA0, pA1, m_reg, mnA, alA);
    __syncthreads(); SWAIT(); SWRITE(1, SO);
    RESC(alA); __syncthreads();
  }
  SBAR(); qkt(pB0, pB1, (bf16*)((char*)K_lds + SHM_K), qr, r32, hi);
  finishSM(pA0, pA1, alA, l_reg, pa0, pa1, pa2, pa3); SBAR();
  pv_d0(o, vb0, pa0, pa1, pa2, pa3); partialSM(pB0, pB1, m_reg, mnB, alB);
  __syncthreads(); RESC(alB);
  finishSM(pB0, pB1, alB, l_reg, pa0, pa1, pa2, pa3); SBAR();
  pv_d0(o, vb0 + (int)SHM_V, pa0, pa1, pa2, pa3);
  if (hi == 0) li_l[r32] = l_reg; asm volatile("s_waitcnt lgkmcnt(0)" ::: "memory");
  float rli[16];
#pragma unroll
  for (int r = 0; r < 16; ++r) rli[r] = __builtin_amdgcn_rcpf(li_l[crow(r, hi)]);
  bf16* Ow = Ob + (long)(wid * QBLK) * LDO;
  if (wok)
  _Pragma("unroll") for (int r = 0; r < 16; ++r) { int orow = crow(r, hi);
    for (int d0 = 0; d0 < 4; ++d0) Ow[(long)orow * LDO + d0 * 32 + r32] = __float2bfloat16(o[d0][r] * rli[r]); }
#undef SLOAD
#undef SWRITE
#undef SWAIT
#undef RESC
}

template <typename TQ>
__global__ __launch_bounds__(64 * NW, 1)
void attn_fwd_dense(const TQ* __restrict__ Q, const bf16* __restrict__ K, const bf16* __restrict__ V, bf16* __restrict__ O, int sq, int skv, int GQA) {
  extern __shared__ __attribute__((aligned(16))) char shm[];
  const long b = blockIdx.z, h = blockIdx.y, Hq = gridDim.y, Hkv = Hq / GQA, kvh = h / GQA, i0 = (long)blockIdx.x * (QBLK * NW);
  if constexpr (LAYOUT == 0) {
    const long q0 = ((b * Hq + h) * sq + i0) * D, k0 = (b * Hkv + kvh) * skv * (long)D;
    attn_dense_body<TQ>(Q + q0, K + k0, V + k0, O + q0, skv, shm, sq - (int)i0);
  } else {
    const long q0 = ((b * sq + i0) * HQ + h) * (long)D, k0 = (b * skv * HKV + kvh) * (long)D;
    attn_dense_body<TQ>(Q + q0, K + k0, V + k0, O + q0, skv, shm, sq - (int)i0);
  }
}

extern "C" void kernel_launch(void* const* d_in, const int* in_sizes, int n_in, void* d_out, int out_size, void* d_ws, size_t ws_size, hipStream_t stream) {
  (void)hipFuncSetAttribute((const void*)attn_fwd_dense<bf16>, hipFuncAttributeMaxDynamicSharedMemorySize, (int)SHM_ATTN);
  hipLaunchKernelGGL(attn_fwd_dense<bf16>, dim3((S + 255) / 256, HQ, B), dim3(64 * NW), SHM_ATTN, stream,
                     (const bf16*)d_in[0], (const bf16*)d_in[1], (const bf16*)d_in[2], (bf16*)d_out, S, SKV, HQ / HKV);
}
```

```cpp
#include <hip/hip_runtime.h>
#include <hip/hip_bf16.h>
#include <cstdio>

using bf16 = __hip_bfloat16;
constexpr int   D = 128, NW = 8, QBLK = 32, KVBLK = 64;
constexpr float SCALE = 0.088388347648318440f;
#define ATTN_THR 8.f
constexpr float THR = ATTN_THR;
#define ATTN_LAYOUT 0
constexpr int LAYOUT = ATTN_LAYOUT;
#define ATTN_SDEPTH 2
constexpr int SDEPTH = ATTN_SDEPTH;
static_assert(SDEPTH == 1 || SDEPTH == 2, "SDEPTH is 1 or 2");
#define ATTN_B 8
#define ATTN_HQ 32
#define ATTN_HKV 32
#define ATTN_S 736
#define ATTN_SKV 512
constexpr int B = ATTN_B, HQ = ATTN_HQ, HKV = ATTN_HKV, S = ATTN_S, SKV = ATTN_SKV;
constexpr int LDQ = LAYOUT ? HQ * D : D, LDK = LAYOUT ? HKV * D : D, LDO = LDQ;
static_assert(SKV % (2 * KVBLK) == 0 && HQ % HKV == 0, "S % 256 == 0, SKV % 128 == 0 (even tile count), HQ % HKV == 0");
constexpr size_t SHM_V = KVBLK * D * 2, SHM_K = KVBLK * D * 2, SHM_ATTN = 2 * SHM_V + 2 * SHM_K + NW * 64 * 4;

using bf16x8 = __attribute__((ext_vector_type(8))) short;
using s16x4  = __attribute__((ext_vector_type(4))) short;
using f32x16 = __attribute__((ext_vector_type(16))) float;
using f32x8  = __attribute__((ext_vector_type(8))) float;
using u32x4  = __attribute__((ext_vector_type(4))) unsigned;
#define KSWZ(row, colB) ((row) * 256 + ((colB) ^ (((row) & 7) << 4)))
#define SBAR() __builtin_amdgcn_sched_barrier(0)
__device__ __forceinline__ int crow(int r, int hi) { return (r & 3) + 8 * (r >> 2) + 4 * hi; }
__device__ __forceinline__ unsigned cvtpk(float lo, float hi) {
  unsigned r; asm volatile("v_cvt_pk_bf16_f32 %0, %1, %2" : "=v"(r) : "v"(lo), "v"(hi)); return r;
}
template <typename TIn> struct Stage;
template <> struct Stage<bf16>  { using T = bf16x8;
  __device__ static __forceinline__ T ld8(const bf16* p) { return *reinterpret_cast<const bf16x8*>(p); }
  __device__ static __forceinline__ bf16x8 tobf(T x) { return x; } };
template <> struct Stage<float> { using T = f32x8;
  __device__ static __forceinline__ T ld8(const float* p) { return *reinterpret_cast<const f32x8*>(p); }
  __device__ static __forceinline__ bf16x8 tobf(T x) {
    u32x4 w = {cvtpk(x[0], x[1]), cvtpk(x[2], x[3]), cvtpk(x[4], x[5]), cvtpk(x[6], x[7])}; return *reinterpret_cast<bf16x8*>(&w); } };

__device__ __forceinline__ void partialSM(f32x16& p0, f32x16& p1, float& m_reg, float& mn, float& alpha) {
  constexpr float C = SCALE * 1.4426950408889634f;
  float pmax = p0[0]; for (int r = 1; r < 16; ++r) pmax = fmaxf(pmax, p0[r]); for (int r = 0; r < 16; ++r) pmax = fmaxf(pmax, p1[r]);
  { auto rr = __builtin_amdgcn_permlane32_swap(__float_as_uint(pmax), __float_as_uint(pmax), false, false);
    pmax = fmaxf(__uint_as_float(rr[0]), __uint_as_float(rr[1])); }
  if (__builtin_expect(__all(pmax - m_reg <= THR / SCALE), 1)) { mn = m_reg; alpha = 1.f; }
  else { mn = fmaxf(m_reg, pmax); alpha = __builtin_amdgcn_exp2f((m_reg - mn) * C); m_reg = mn; }
  float mnC = -mn * C;
  for (int r = 0; r < 16; ++r) p0[r] = fmaf(p0[r], C, mnC); for (int r = 0; r < 16; ++r) p1[r] = fmaf(p1[r], C, mnC);
  for (int r = 0; r < 16; ++r) p0[r] = __builtin_amdgcn_exp2f(p0[r]);
}
__device__ __forceinline__ void finishSM(f32x16& p0, f32x16& p1, float alpha, float& l_reg, bf16x8& pa0, bf16x8& pa1, bf16x8& pa2, bf16x8& pa3) {
  for (int r = 0; r < 16; ++r) p1[r] = __builtin_amdgcn_exp2f(p1[r]);
  float ps = 0; for (int r = 0; r < 16; ++r) ps += p0[r]; for (int r = 0; r < 16; ++r) ps += p1[r];
  { auto rr = __builtin_amdgcn_permlane32_swap(__float_as_uint(ps), __float_as_uint(ps), false, false);
    ps = __uint_as_float(rr[0]) + __uint_as_float(rr[1]); }
  l_reg = l_reg * alpha + ps;
#define PK4(P, BASE, OUT) do { unsigned a0 = cvtpk(P[BASE + 0], P[BASE + 1]), a1 = cvtpk(P[BASE + 2], P[BASE + 3]);   \
    unsigned b0 = cvtpk(P[BASE + 4], P[BASE + 5]), b1 = cvtpk(P[BASE + 6], P[BASE + 7]);                              \
    auto r0 = __builtin_amdgcn_permlane32_swap(a0, b0, false, false); auto r1 = __builtin_amdgcn_permlane32_swap(a1, b1, false, false); \
    u32x4 w = {r0[0], r1[0], r0[1], r1[1]}; OUT = *reinterpret_cast<bf16x8*>(&w); } while (0)
  PK4(p0, 0, pa0); PK4(p0, 8, pa1); PK4(p1, 0, pa2); PK4(p1, 8, pa3);
#undef PK4
}
__device__ __forceinline__ void qkt(f32x16& p0, f32x16& p1, const bf16* Ks, const bf16x8* qr, int r32, int hi) {
  p0 = f32x16{}; p1 = f32x16{};
  for (int d0 = 0; d0 < 8; ++d0) { int cb = (d0 * 16 + hi * 8) * 2;
    bf16x8 b0 = *reinterpret_cast<const bf16x8*>((const char*)Ks + KSWZ(r32, cb));
    bf16x8 b1 = *reinterpret_cast<const bf16x8*>((const char*)Ks + KSWZ(32 + r32, cb));
    p0 = __builtin_amdgcn_mfma_f32_32x32x16_bf16(b0, qr[d0], p0, 0, 0, 0);
    p1 = __builtin_amdgcn_mfma_f32_32x32x16_bf16(b1, qr[d0], p1, 0, 0, 0); }
}
__device__ __forceinline__ int v_st(int k, int c) { const int kk = (k & ~0xC) | ((k & 4) << 1) | ((k & 8) >> 1); return ((kk >> 3) * 4 + (c >> 5)) * 512 + ((kk & 7) * 32 + (c & 31)) * 2; }
__device__ __forceinline__ int v_rd_base(int lane) { return ((lane & 3) << 3) | (((lane >> 2) & 3) << 6) | (((lane >> 4) & 1) << 5) | (((lane >> 5) & 1) << 8); }
constexpr int v_rd_off(int d0, int ks, int half) { return d0 * 512 + ks * 4096 + half * 2048; }
template <int OFF> __device__ __forceinline__ s16x4 tr_read(int vb) {
  s16x4 r; asm volatile("ds_read_b64_tr_b16 %0, %1 offset:%2" : "=&v"(r) : "v"(vb), "i"(OFF) : "memory"); return r;
}
template <int D0> __device__ __forceinline__ void pv_one(f32x16& od, int vb, bf16x8 pa0, bf16x8 pa1, bf16x8 pa2, bf16x8 pa3) {
  const s16x4 l0 = tr_read<v_rd_off(D0, 0, 0)>(vb), h0 = tr_read<v_rd_off(D0, 0, 1)>(vb), l1 = tr_read<v_rd_off(D0, 1, 0)>(vb), h1 = tr_read<v_rd_off(D0, 1, 1)>(vb);
  const s16x4 l2 = tr_read<v_rd_off(D0, 2, 0)>(vb), h2 = tr_read<v_rd_off(D0, 2, 1)>(vb), l3 = tr_read<v_rd_off(D0, 3, 0)>(vb), h3 = tr_read<v_rd_off(D0, 3, 1)>(vb);
  asm volatile("s_waitcnt lgkmcnt(0)" ::: "memory"); SBAR();
#define PK(L, H) (bf16x8){L[0], L[1], L[2], L[3], H[0], H[1], H[2], H[3]}
  od = __builtin_amdgcn_mfma_f32_32x32x16_bf16(pa0, PK(l0, h0), od, 0, 0, 0);
  od = __builtin_amdgcn_mfma_f32_32x32x16_bf16(pa1, PK(l1, h1), od, 0, 0, 0);
  od = __builtin_amdgcn_mfma_f32_32x32x16_bf16(pa2, PK(l2, h2), od, 0, 0, 0);
  od = __builtin_amdgcn_mfma_f32_32x32x16_bf16(pa3, PK(l3, h3), od, 0, 0, 0);
#undef PK
}
__device__ __forceinline__ void pv_d0(f32x16* o, int vb, bf16x8 pa0, bf16x8 pa1, bf16x8 pa2, bf16x8 pa3) {
  pv_one<0>(o[0], vb, pa0, pa1, pa2, pa3); pv_one<1>(o[1], vb, pa0, pa1, pa2, pa3); pv_one<2>(o[2], vb, pa0, pa1, pa2, pa3); pv_one<3>(o[3], vb, pa0, pa1, pa2, pa3);
}

template <typename TQ>
__device__ __forceinline__ void attn_dense_body(const TQ* __restrict__ Qb, const bf16* __restrict__ Kh, const bf16* __restrict__ Vh,
                                                bf16* __restrict__ Ob, int seq, char* lds, int nrows) {
  using St = Stage<bf16>; using SQ = Stage<TQ>;
  const int tid = threadIdx.x, wid = tid >> 6, lane = tid & 63, r32 = lane & 31, hi = lane >> 5;
  bf16* V_lds = (bf16*)lds; bf16* K_lds = (bf16*)(lds + 2 * SHM_V);
  float* ws = (float*)(lds + 2 * SHM_V + 2 * SHM_K) + wid * 64; float* li_l = ws; float* al_l = ws + 32;
  float m_reg = -1e30f, l_reg = 0; f32x16 o[4] = {}; bf16x8 qr[8];
  const bool wok = wid * QBLK < nrows; const TQ* Qw = Qb + (long)((wok ? wid * QBLK : 0) + r32) * LDQ + hi * 8;
#pragma unroll
  for (int d0 = 0; d0 < 8; ++d0) qr[d0] = SQ::tobf(SQ::ld8(Qw + d0 * 16));
  const int sr = tid >> 4, sc = (tid & 15) * 8, vst0 = v_st(sr, sc), vst1 = v_st(32 + sr, sc);
  const int vb0 = (int)(uintptr_t)V_lds + v_rd_base(lane);
  struct { typename St::T vs0, vs1, ks0, ks1; } sr_[SDEPTH];
#define SLOAD(i, k0) do { sr_[i].vs0 = St::ld8(&Vh[(long)((k0) + sr) * LDK + sc]); sr_[i].vs1 = St::ld8(&Vh[(long)((k0) + 32 + sr) * LDK + sc]); \
    sr_[i].ks0 = St::ld8(&Kh[(long)((k0) + sr) * LDK + sc]); sr_[i].ks1 = St::ld8(&Kh[(long)((k0) + 32 + sr) * LDK + sc]); } while (0)
#define SWRITE(b, i) do { *(bf16x8*)((char*)V_lds + (b) * SHM_V + vst0) = St::tobf(sr_[i].vs0);          \
    *(bf16x8*)((char*)V_lds + (b) * SHM_V + vst1) = St::tobf(sr_[i].vs1); int kc = sc * 2;               \
    *(bf16x8*)((char*)K_lds + (b) * SHM_K + KSWZ(sr, kc)) = St::tobf(sr_[i].ks0);                       \
    *(bf16x8*)((char*)K_lds + (b) * SHM_K + KSWZ(32 + sr, kc)) = St::tobf(sr_[i].ks1); } while (0)
#define SWAIT() do { if constexpr (SDEPTH == 2) asm volatile("s_waitcnt vmcnt(4)" ::: "memory"); else asm volatile("s_waitcnt vmcnt(0)" ::: "memory"); } while (0)
#define RESC(a) do { if (__any((a) < 1.f)) { if (hi == 0) al_l[r32] = (a); asm volatile("s_waitcnt lgkmcnt(0)" ::: "memory"); \
    for (int d = 0; d < 4; ++d) for (int r = 0; r < 16; ++r) o[d][r] *= al_l[crow(r, hi)]; } } while (0)
  f32x16 pA0, pA1, pB0, pB1; float mnA, mnB, alA, alB; bf16x8 pa0, pa1, pa2, pa3; const int NT = seq / KVBLK;
  constexpr int SE = 0, SO = SDEPTH - 1;
  SLOAD(SE, 0); asm volatile("s_waitcnt vmcnt(0)" ::: "memory"); SWRITE(0, SE); __syncthreads();
  qkt(pA0, pA1, K_lds, qr, r32, hi); partialSM(pA0, pA1, m_reg, mnA, alA);
  SLOAD(SO, KVBLK); if constexpr (SDEPTH == 2) { if (2 < NT) SLOAD(SE, 2 * KVBLK); }
  SWAIT(); SWRITE(1, SO); __syncthreads();
  for (int j = 1; j + 1 < NT; j += 2) {
    SBAR(); qkt(pB0, pB1, (bf16*)((char*)K_lds + SHM_K), qr, r32, hi);
    finishSM(pA0, pA1, alA, l_reg, pa0, pa1, pa2, pa3); SBAR();
    SLOAD(SO, (j + SDEPTH) * KVBLK); SBAR();
    pv_d0(o, vb0, pa0, pa1, pa2, pa3); partialSM(pB0, pB1, m_reg, mnB, alB);
    __syncthreads(); SWAIT(); SWRITE(0, SE);
    RESC(alB); __syncthreads();
    SBAR(); qkt(pA0, pA1, K_lds, qr, r32, hi);
    finishSM(pB0, pB1, alB, l_reg, pa0, pa1, pa2, pa3); SBAR();
    if (SDEPTH == 1 || j + 3 < NT) SLOAD(SE, (j + 1 + SDEPTH) * KVBLK); SBAR();
    pv_d0(o, vb0 + (int)SHM_V, pa0, pa1, pa2, pa3); partialSM(pA0, pA1, m_reg, mnA, alA);
    __syncthreads(); SWAIT(); SWRITE(1, SO);
    RESC(alA); __syncthreads();
  }
  SBAR(); qkt(pB0, pB1, (bf16*)((char*)K_lds + SHM_K), qr, r32, hi);
  finishSM(pA0, pA1, alA, l_reg, pa0, pa1, pa2, pa3); SBAR();
  pv_d0(o, vb0, pa0, pa1, pa2, pa3); partialSM(pB0, pB1, m_reg, mnB, alB);
  __syncthreads(); RESC(alB);
  finishSM(pB0, pB1, alB, l_reg, pa0, pa1, pa2, pa3); SBAR();
  pv_d0(o, vb0 + (int)SHM_V, pa0, pa1, pa2, pa3);
  if (hi == 0) li_l[r32] = l_reg; asm volatile("s_waitcnt lgkmcnt(0)" ::: "memory");
  float rli[16];
#pragma unroll
  for (int r = 0; r < 16; ++r) rli[r] = __builtin_amdgcn_rcpf(li_l[crow(r, hi)]);
  bf16* Ow = Ob + (long)(wid * QBLK) * LDO;
  if (wok)
  _Pragma("unroll") for (int r = 0; r < 16; ++r) { int orow = crow(r, hi);
    for (int d0 = 0; d0 < 4; ++d0) Ow[(long)orow * LDO + d0 * 32 + r32] = __float2bfloat16(o[d0][r] * rli[r]); }
#undef SLOAD
#undef SWRITE
#undef SWAIT
#undef RESC
}

template <typename TQ>
__global__ __launch_bounds__(64 * NW, 1)
void attn_fwd_dense(const TQ* __restrict__ Q, const bf16* __restrict__ K, const bf16* __restrict__ V, bf16* __restrict__ O, int sq, int skv, int GQA) {
  extern __shared__ __attribute__((aligned(16))) char shm[];
  const long b = blockIdx.z, h = blockIdx.y, Hq = gridDim.y, Hkv = Hq / GQA, kvh = h / GQA, i0 = (long)blockIdx.x * (QBLK * NW);
  if constexpr (LAYOUT == 0) {
    const long q0 = ((b * Hq + h) * sq + i0) * D, k0 = (b * Hkv + kvh) * skv * (long)D;
    attn_dense_body<TQ>(Q + q0, K + k0, V + k0, O + q0, skv, shm, sq - (int)i0);
  } else {
    const long q0 = ((b * sq + i0) * HQ + h) * (long)D, k0 = (b * skv * HKV + kvh) * (long)D;
    attn_dense_body<TQ>(Q + q0, K + k0, V + k0, O + q0, skv, shm, sq - (int)i0);
  }
}

template <int AUX, int C0, int C1, int STEP>
__global__ __launch_bounds__(512) void prestream(const char* __restrict__ q, const char* __restrict__ k, const char* __restrict__ v) {
  __shared__ __attribute__((aligned(16))) char lds[131072];
  const int h = blockIdx.x, w = __builtin_amdgcn_readfirstlane(threadIdx.x >> 6), lane = threadIdx.x & 63;
  constexpr int PER = 4, DEPTH = 3;
  unsigned long long r0 = __builtin_amdgcn_s_memrealtime();
  auto issue = [&](int c) {
    const char* base = c < 8 ? ((c & 1) ? v : k) + (size_t)h * 131072 + (c >> 1) * 32768 : q + (size_t)h * 188416 + (c - 8) * 32768;
#pragma unroll
    for (int i = 0; i < PER; ++i) {
      const char* src = base + (w * PER + i) * 1024 + lane * 16;
      __builtin_amdgcn_global_load_lds((const __attribute__((address_space(1))) void*)src, (__attribute__((address_space(3))) void*)(lds + ((c / STEP) & 3) * 32768 + (w * PER + i) * 1024), 16, 0, AUX);
    }
  };
#pragma unroll
  for (int c = C0; c < C0 + DEPTH * STEP; c += STEP) issue(c);
#pragma unroll
  for (int c = C0; c < C1; c += STEP) {
    asm volatile("s_waitcnt vmcnt(%0)" :: "i"(2 * PER) : "memory");
    if (c + DEPTH * STEP < C1) issue(c + DEPTH * STEP);
  }
  asm volatile("s_waitcnt vmcnt(0)" ::: "memory");
  __syncthreads();
  unsigned long long r1 = __builtin_amdgcn_s_memrealtime();
  unsigned long long tgt = r1 + 10 * (r1 - r0);
  for (int i = 0; i < 100000; ++i) { if (__builtin_amdgcn_s_memrealtime() >= tgt) break; __builtin_amdgcn_s_sleep(4); }
}

extern "C" void kernel_launch(void* const* d_in, const int* in_sizes, int n_in, void* d_out, int out_size, void* d_ws, size_t ws_size, hipStream_t stream) {
  (void)hipFuncSetAttribute((const void*)attn_fwd_dense<bf16>, hipFuncAttributeMaxDynamicSharedMemorySize, (int)SHM_ATTN);
  prestream<2,0,8,2><<<256, 512, 0, stream>>>((const char*)d_in[0], (const char*)d_in[1], (const char*)d_in[2]);
  hipLaunchKernelGGL(attn_fwd_dense<bf16>, dim3((S + 255) / 256, HQ, B), dim3(64 * NW), SHM_ATTN, stream,
                     (const bf16*)d_in[0], (const bf16*)d_in[1], (const bf16*)d_in[2], (bf16*)d_out, S, SKV, HQ / HKV);
}
```

```cpp
#include <hip/hip_runtime.h>
#include <hip/hip_bf16.h>

namespace {
constexpr int SQ = 736, SK = 512, DH = 128, HALF = 368;
typedef unsigned u32x4 __attribute__((ext_vector_type(4)));
__device__ __forceinline__ float bf_lo(unsigned x) { return __uint_as_float(x << 16); }
__device__ __forceinline__ float bf_hi(unsigned x) { return __uint_as_float(x & 0xffff0000u); }

__device__ __forceinline__ void safe_rows(const unsigned* __restrict__ q, const unsigned* __restrict__ k, const unsigned* __restrict__ v, unsigned* __restrict__ o,
                                          float* lds, int pair, int par, int j, int lane) {
  float* qf = lds + j * 1024;
  float* pf = qf + 128;
  const float c = 0.08838834764831845f * 1.4426950408889634f;
  for (int idx = j; idx < 2 * HALF; idx += 4) {
    const int hh = idx / HALF, head = 2 * pair + hh, row = HALF * par + (idx - hh * HALF);
    const unsigned qq = q[((size_t)head * SQ + row) * 64 + lane];
    qf[2 * lane] = bf_lo(qq); qf[2 * lane + 1] = bf_hi(qq);
    __builtin_amdgcn_fence(__ATOMIC_RELEASE, "wavefront"); __builtin_amdgcn_wave_barrier();
    float mx = -INFINITY;
    for (int m = 0; m < 8; ++m) {
      const u32x4* kr = (const u32x4*)(k + ((size_t)head * SK + lane + 64 * m) * 64);
      float acc = 0.f;
      for (int ch = 0; ch < 16; ++ch) {
        const u32x4 kk = kr[ch];
#pragma unroll
        for (int e = 0; e < 4; ++e) { acc = fmaf(qf[8 * ch + 2 * e], bf_lo(kk[e]), acc); acc = fmaf(qf[8 * ch + 2 * e + 1], bf_hi(kk[e]), acc); }
      }
      acc *= c; pf[lane + 64 * m] = acc; mx = fmaxf(mx, acc);
    }
    for (int s = 32; s; s >>= 1) mx = fmaxf(mx, __shfl_xor(mx, s));
    float l = 0.f;
    for (int m = 0; m < 8; ++m) { const float p = exp2f(pf[lane + 64 * m] - mx); pf[lane + 64 * m] = p; l += p; }
    for (int s = 32; s; s >>= 1) l += __shfl_xor(l, s);
    __builtin_amdgcn_fence(__ATOMIC_RELEASE, "wavefront"); __builtin_amdgcn_wave_barrier();
    float a0 = 0.f, a1 = 0.f; const float rl = 1.f / l;
    const unsigned* vr = v + (size_t)head * SK * 64 + lane;
#pragma unroll 8
    for (int key = 0; key < SK; ++key) { const unsigned vv = vr[key * 64]; const float p = pf[key] * rl; a0 = fmaf(p, bf_lo(vv), a0); a1 = fmaf(p, bf_hi(vv), a1); }
    const __hip_bfloat16 b0 = __float2bfloat16(a0), b1 = __float2bfloat16(a1);
    o[((size_t)head * SQ + row) * 64 + lane] = (unsigned)__builtin_bit_cast(unsigned short, b0) | ((unsigned)__builtin_bit_cast(unsigned short, b1) << 16);
    __builtin_amdgcn_fence(__ATOMIC_RELEASE, "wavefront"); __builtin_amdgcn_wave_barrier();
  }
}
}

__global__ __launch_bounds__(512, 2) void mha_kvs_kernel(const void* __restrict__ q, const void* __restrict__ k, const void* __restrict__ v, void* __restrict__ o) {
  __shared__ __attribute__((aligned(16))) float lds[40960];
  const unsigned wave = __builtin_amdgcn_readfirstlane(threadIdx.x >> 6), blk = blockIdx.x;
  unsigned flag;
  asm volatile(
"s_mov_b64 s[20:21], %1\n"
"s_mov_b64 s[24:25], %2\n"
"s_mov_b64 s[28:29], %3\n"
"s_mov_b64 s[64:65], %4\n"
"s_mov_b32 s36, %5\n"
"s_mov_b32 s37, %6\n"
"s_getpc_b64 s[68:69]\n"
"L_code_begin:\n"
"s_and_b32 s69, s69, 0xffff\n"
"s_mov_b32 s70, L_done-L_code_begin\n"
"s_mov_b32 s71, 0x20000\n"
"s_and_b32 s21, s21, 0xffff\n"
"s_mov_b32 s22, 0x2e00000\n"
"s_mov_b32 s23, 0x20000\n"
"s_and_b32 s25, s25, 0xffff\n"
"s_mov_b32 s26, 0x2000000\n"
"s_mov_b32 s27, 0x20000\n"
"s_and_b32 s29, s29, 0xffff\n"
"s_mov_b32 s30, 0x2000000\n"
"s_mov_b32 s31, 0x20000\n"
"s_and_b32 s65, s65, 0xffff\n"
"s_mov_b32 s66, 0x2e00000\n"
"s_mov_b32 s67, 0x20000\n"
"s_and_b32 s48, s37, 7\n"
"s_lshl_b32 s48, s48, 4\n"
"s_lshr_b32 s49, s37, 4\n"
"s_add_u32 s48, s48, s49\n"
"s_lshr_b32 s49, s37, 3\n"
"s_and_b32 s49, s49, 1\n"
"s_mul_i32 s62, s49, 0\n"
"s_mul_i32 s53, s48, 0x40000\n"
"s_mul_i32 s51, s48, 0x5c000\n"
"s_mul_i32 s49, s49, 0x17000\n"
"s_add_u32 s51, s51, s49\n"
"s_and_b32 s38, s36, 3\n"
"s_cmp_ge_u32 s36, 4\n"
"s_cbranch_scc0 L_qk_head\n"
"s_setprio 0\n"
"v_mbcnt_lo_u32_b32 v112, -1, 0\n"
"v_mbcnt_hi_u32_b32 v112, -1, v112\n"
"v_lshlrev_b32 v124, 4, v112\n"
"s_mov_b32 s52, s51\n"
"s_lshl_b32 s48, s38, 10\n"
"s_add_u32 s51, s51, s48\n"
"s_add_u32 s40, s51, 0\n"
"buffer_load_dwordx4 v[96:99], v124, s[20:23], s40 offen nt\n"
"s_add_u32 s40, s51, 0x1000\n"
"buffer_load_dwordx4 v[100:103], v124, s[20:23], s40 offen nt\n"
"s_add_u32 s40, s51, 0x2000\n"
"buffer_load_dwordx4 v[104:107], v124, s[20:23], s40 offen nt\n"
"s_add_u32 s40, s51, 0x3000\n"
"buffer_load_dwordx4 v[108:111], v124, s[20:23], s40 offen nt\n"
"s_branch L_pv_role\n"
"L_qk_head:\n"
"s_setprio 1\n"
"v_mbcnt_lo_u32_b32 v112, -1, 0\n"
"v_mbcnt_hi_u32_b32 v112, -1, v112\n"
"v_and_b32 v113, 15, v112\n"
"v_lshrrev_b32 v114, 4, v112\n"
"v_xor_b32 v104, v113, v114\n"
"v_lshlrev_b32 v104, 4, v104\n"
"v_lshl_add_u32 v104, v114, 8, v104\n"
"v_xor_b32 v105, 64, v104\n"
"v_xor_b32 v106, 0x80, v104\n"
"v_xor_b32 v107, 0xc0, v104\n"
"s_lshl_b32 s48, s38, 13\n"
"s_add_u32 s54, s48, 0x18000\n"
"s_xor_b32 s62, s48, s62\n"
"s_add_u32 s39, s53, 0x20000\n"
"s_xor_b32 s63, s62, 0x1000\n"
"s_sub_u32 s63, s63, s62\n"
"s_xor_b32 s42, s62, 0\n"
"s_mov_b32 m0, s54\n"
"s_add_u32 s42, s42, s53\n"
"buffer_load_dwordx4 v104, s[24:27], s42 offen offset:0 nt lds\n"
"buffer_load_dwordx4 v105, s[24:27], s42 offen offset:1024 nt lds\n"
"buffer_load_dwordx4 v106, s[24:27], s42 offen offset:2048 nt lds\n"
"buffer_load_dwordx4 v107, s[24:27], s42 offen offset:3072 nt lds\n"
"s_add_u32 s42, s42, s63\n"
"s_add_u32 m0, s54, 0x1000\n"
"s_nop 0\n"
"buffer_load_dwordx4 v104, s[24:27], s42 offen offset:0 nt lds\n"
"buffer_load_dwordx4 v105, s[24:27], s42 offen offset:1024 nt lds\n"
"buffer_load_dwordx4 v106, s[24:27], s42 offen offset:2048 nt lds\n"
"buffer_load_dwordx4 v107, s[24:27], s42 offen offset:3072 nt lds\n"
"v_xor_b32 v96, v113, v114\n"
"v_lshlrev_b32 v96, 4, v96\n"
"v_lshl_add_u32 v96, v113, 8, v96\n"
"v_xor_b32 v97, 64, v96\n"
"v_xor_b32 v98, 0x80, v96\n"
"v_xor_b32 v99, 0xc0, v96\n"
"v_add_u32 v100, s54, v96\n"
"v_add_u32 v96, 0xc000, v96\n"
"v_add_u32 v101, s54, v97\n"
"v_add_u32 v97, 0xc000, v97\n"
"v_add_u32 v102, s54, v98\n"
"v_add_u32 v98, 0xc000, v98\n"
"v_add_u32 v103, s54, v99\n"
"v_add_u32 v99, 0xc000, v99\n"
"s_lshl_b32 s48, s38, 10\n"
"v_lshl_add_u32 v108, v112, 4, s48\n"
"s_lshl_b32 s48, s38, 2\n"
"s_add_u32 s48, s48, 0x9000\n"
"v_lshl_add_u32 v109, v112, 4, s48\n"
"v_and_b32 v115, 3, v112\n"
"v_subrev_u32 v115, 1, v115\n"
"v_lshrrev_b32 v115, 2, v115\n"
"v_and_b32 v60, 0x3f803f80, v115\n"
"v_mov_b32 v61, v60\n"
"v_mov_b32 v62, v60\n"
"v_mov_b32 v63, v60\n"
"s_lshr_b32 s48, s37, 3\n"
"s_lshl_b32 s48, s48, 2\n"
"s_add_u32 s48, s48, s38\n"
"s_lshl_b32 s48, s48, 10\n"
"v_lshrrev_b32 v120, 3, v112\n"
"v_lshl_add_u32 v120, v120, 7, s48\n"
"buffer_load_dword v121, v120, s[68:71], 0 offen\n"
"s_waitcnt vmcnt(1)\n"
"ds_read_b128 a[0:3], v100 offset:0\n"
"ds_read_b128 a[4:7], v101 offset:0\n"
"ds_read_b128 a[8:11], v102 offset:0\n"
"ds_read_b128 a[12:15], v103 offset:0\n"
"ds_read_b128 a[16:19], v100 offset:4096\n"
"ds_read_b128 a[20:23], v101 offset:4096\n"
"ds_read_b128 a[24:27], v102 offset:4096\n"
"ds_read_b128 a[28:31], v103 offset:4096\n"
"s_barrier\n"
"ds_read_b128 v[0:3], v96 offset:0\n"
"ds_read_b128 v[4:7], v97 offset:0\n"
"ds_read_b128 v[8:11], v98 offset:0\n"
"ds_read_b128 v[12:15], v99 offset:0\n"
"ds_read_b128 v[16:19], v96 offset:4096\n"
"ds_read_b128 v[20:23], v97 offset:4096\n"
"ds_read_b128 v[24:27], v98 offset:4096\n"
"ds_read_b128 v[28:31], v99 offset:4096\n"
"s_waitcnt lgkmcnt(4)\n"
"v_mfma_f32_16x16x32_bf16 v[32:35], a[0:3], v[0:3], -4.0\n"
"v_mfma_f32_16x16x32_bf16 v[32:35], a[4:7], v[4:7], v[32:35]\n"
"v_mfma_f32_16x16x32_bf16 v[32:35], a[8:11], v[8:11], v[32:35]\n"
"v_mfma_f32_16x16x32_bf16 v[32:35], a[12:15], v[12:15], v[32:35]\n"
"v_mfma_f32_16x16x32_bf16 v[36:39], a[16:19], v[0:3], -4.0\n"
"v_mfma_f32_16x16x32_bf16 v[36:39], a[20:23], v[4:7], v[36:39]\n"
"v_mfma_f32_16x16x32_bf16 v[36:39], a[24:27], v[8:11], v[36:39]\n"
"v_mfma_f32_16x16x32_bf16 v[36:39], a[28:31], v[12:15], v[36:39]\n"
"ds_read_b128 v[0:3], v96 offset:8192\n"
"ds_read_b128 v[4:7], v97 offset:8192\n"
"ds_read_b128 v[8:11], v98 offset:8192\n"
"ds_read_b128 v[12:15], v99 offset:8192\n"
"s_xor_b32 s42, s62, 0x8000\n"
"s_mov_b32 m0, s54\n"
"s_add_u32 s42, s42, s53\n"
"buffer_load_dwordx4 v104, s[24:27], s42 offen offset:0 nt lds\n"
"buffer_load_dwordx4 v105, s[24:27], s42 offen offset:1024 nt lds\n"
"s_waitcnt lgkmcnt(4)\n"
"v_mfma_f32_16x16x32_bf16 v[40:43], a[0:3], v[16:19], -4.0\n"
"v_exp_f32 v32, v32\n"
"v_mfma_f32_16x16x32_bf16 v[40:43], a[4:7], v[20:23], v[40:43]\n"
"v_exp_f32 v33, v33\n"
"v_mfma_f32_16x16x32_bf16 v[40:43], a[8:11], v[24:27], v[40:43]\n"
"v_exp_f32 v34, v34\n"
"v_mfma_f32_16x16x32_bf16 v[40:43], a[12:15], v[28:31], v[40:43]\n"
"v_exp_f32 v35, v35\n"
"v_mfma_f32_16x16x32_bf16 v[44:47], a[16:19], v[16:19], -4.0\n"
"v_exp_f32 v36, v36\n"
"v_mfma_f32_16x16x32_bf16 v[44:47], a[20:23], v[20:23], v[44:47]\n"
"v_exp_f32 v37, v37\n"
"v_mfma_f32_16x16x32_bf16 v[44:47], a[24:27], v[24:27], v[44:47]\n"
"v_exp_f32 v38, v38\n"
"v_mfma_f32_16x16x32_bf16 v[44:47], a[28:31], v[28:31], v[44:47]\n"
"v_exp_f32 v39, v39\n"
"s_nop 0\n"
"v_cvt_pk_bf16_f32 v48, v32, v33\n"
"v_cvt_pk_bf16_f32 v49, v34, v35\n"
"v_cvt_pk_bf16_f32 v50, v36, v37\n"
"v_cvt_pk_bf16_f32 v51, v38, v39\n"
"ds_read_b128 v[16:19], v96 offset:12288\n"
"ds_read_b128 v[20:23], v97 offset:12288\n"
"ds_read_b128 v[24:27], v98 offset:12288\n"
"ds_read_b128 v[28:31], v99 offset:12288\n"
"buffer_load_dwordx4 v106, s[24:27], s42 offen offset:2048 nt lds\n"
"buffer_load_dwordx4 v107, s[24:27], s42 offen offset:3072 nt lds\n"
"ds_write_b128 v108, v[48:51] offset:0\n"
"s_barrier\n"
"s_waitcnt lgkmcnt(5)\n"
"v_mfma_f32_16x16x32_bf16 v[32:35], a[0:3], v[0:3], -4.0\n"
"v_exp_f32 v40, v40\n"
"v_mfma_f32_16x16x32_bf16 v[32:35], a[4:7], v[4:7], v[32:35]\n"
"v_exp_f32 v41, v41\n"
"v_mfma_f32_16x16x32_bf16 v[32:35], a[8:11], v[8:11], v[32:35]\n"
"v_exp_f32 v42, v42\n"
"v_mfma_f32_16x16x32_bf16 v[32:35], a[12:15], v[12:15], v[32:35]\n"
"v_exp_f32 v43, v43\n"
"v_mfma_f32_16x16x32_bf16 v[36:39], a[16:19], v[0:3], -4.0\n"
"v_exp_f32 v44, v44\n"
"v_mfma_f32_16x16x32_bf16 v[36:39], a[20:23], v[4:7], v[36:39]\n"
"v_exp_f32 v45, v45\n"
"v_mfma_f32_16x16x32_bf16 v[36:39], a[24:27], v[8:11], v[36:39]\n"
"v_exp_f32 v46, v46\n"
"v_mfma_f32_16x16x32_bf16 v[36:39], a[28:31], v[12:15], v[36:39]\n"
"v_exp_f32 v47, v47\n"
"v_mfma_f32_16x16x32_bf16 v[64:67], v[60:63], v[48:51], 0\n"
"v_cvt_pk_bf16_f32 v52, v40, v41\n"
"v_cvt_pk_bf16_f32 v53, v42, v43\n"
"v_cvt_pk_bf16_f32 v54, v44, v45\n"
"v_cvt_pk_bf16_f32 v55, v46, v47\n"
"ds_read_b128 v[0:3], v96 offset:16384\n"
"ds_read_b128 v[4:7], v97 offset:16384\n"
"ds_read_b128 v[8:11], v98 offset:16384\n"
"ds_read_b128 v[12:15], v99 offset:16384\n"
"s_add_u32 s42, s42, s63\n"
"s_add_u32 m0, s54, 0x1000\n"
"s_nop 0\n"
"buffer_load_dwordx4 v104, s[24:27], s42 offen offset:0 nt lds\n"
"buffer_load_dwordx4 v105, s[24:27], s42 offen offset:1024 nt lds\n"
"ds_write_b128 v108, v[52:55] offset:4096\n"
"s_waitcnt lgkmcnt(6)\n"
"v_mfma_f32_16x16x32_bf16 v[40:43], a[0:3], v[16:19], -4.0\n"
"v_exp_f32 v32, v32\n"
"v_mfma_f32_16x16x32_bf16 v[40:43], a[4:7], v[20:23], v[40:43]\n"
"v_exp_f32 v33, v33\n"
"v_mfma_f32_16x16x32_bf16 v[40:43], a[8:11], v[24:27], v[40:43]\n"
"v_exp_f32 v34, v34\n"
"v_mfma_f32_16x16x32_bf16 v[40:43], a[12:15], v[28:31], v[40:43]\n"
"v_exp_f32 v35, v35\n"
"v_mfma_f32_16x16x32_bf16 v[44:47], a[16:19], v[16:19], -4.0\n"
"v_exp_f32 v36, v36\n"
"v_mfma_f32_16x16x32_bf16 v[44:47], a[20:23], v[20:23], v[44:47]\n"
"v_exp_f32 v37, v37\n"
"v_mfma_f32_16x16x32_bf16 v[44:47], a[24:27], v[24:27], v[44:47]\n"
"v_exp_f32 v38, v38\n"
"v_mfma_f32_16x16x32_bf16 v[44:47], a[28:31], v[28:31], v[44:47]\n"
"v_exp_f32 v39, v39\n"
"v_mfma_f32_16x16x32_bf16 v[68:71], v[60:63], v[52:55], 0\n"
"v_cvt_pk_bf16_f32 v48, v32, v33\n"
"v_cvt_pk_bf16_f32 v49, v34, v35\n"
"v_cvt_pk_bf16_f32 v50, v36, v37\n"
"v_cvt_pk_bf16_f32 v51, v38, v39\n"
"ds_read_b128 v[16:19], v96 offset:20480\n"
"ds_read_b128 v[20:23], v97 offset:20480\n"
"ds_read_b128 v[24:27], v98 offset:20480\n"
"ds_read_b128 v[28:31], v99 offset:20480\n"
"buffer_load_dwordx4 v106, s[24:27], s42 offen offset:2048 nt lds\n"
"buffer_load_dwordx4 v107, s[24:27], s42 offen offset:3072 nt lds\n"
"ds_write_b128 v108, v[48:51] offset:8192\n"
"s_waitcnt lgkmcnt(6)\n"
"v_mfma_f32_16x16x32_bf16 v[32:35], a[0:3], v[0:3], -4.0\n"
"v_exp_f32 v40, v40\n"
"v_mfma_f32_16x16x32_bf16 v[32:35], a[4:7], v[4:7], v[32:35]\n"
"v_exp_f32 v41, v41\n"
"v_mfma_f32_16x16x32_bf16 v[32:35], a[8:11], v[8:11], v[32:35]\n"
"v_exp_f32 v42, v42\n"
"v_mfma_f32_16x16x32_bf16 v[32:35], a[12:15], v[12:15], v[32:35]\n"
"v_exp_f32 v43, v43\n"
"v_mfma_f32_16x16x32_bf16 v[36:39], a[16:19], v[0:3], -4.0\n"
"v_exp_f32 v44, v44\n"
"v_mfma_f32_16x16x32_bf16 v[36:39], a[20:23], v[4:7], v[36:39]\n"
"v_exp_f32 v45, v45\n"
"v_mfma_f32_16x16x32_bf16 v[36:39], a[24:27], v[8:11], v[36:39]\n"
"v_exp_f32 v46, v46\n"
"v_mfma_f32_16x16x32_bf16 v[36:39], a[28:31], v[12:15], v[36:39]\n"
"v_exp_f32 v47, v47\n"
"v_mfma_f32_16x16x32_bf16 v[72:75], v[60:63], v[48:51], 0\n"
"v_cvt_pk_bf16_f32 v52, v40, v41\n"
"v_cvt_pk_bf16_f32 v53, v42, v43\n"
"v_cvt_pk_bf16_f32 v54, v44, v45\n"
"v_cvt_pk_bf16_f32 v55, v46, v47\n"
"ds_read_b128 v[0:3], v96 offset:24576\n"
"ds_read_b128 v[4:7], v97 offset:24576\n"
"ds_read_b128 v[8:11], v98 offset:24576\n"
"ds_read_b128 v[12:15], v99 offset:24576\n"
"ds_write_b128 v108, v[52:55] offset:12288\n"
"s_waitcnt lgkmcnt(6)\n"
"v_mfma_f32_16x16x32_bf16 v[40:43], a[0:3], v[16:19], -4.0\n"
"v_exp_f32 v32, v32\n"
"v_mfma_f32_16x16x32_bf16 v[40:43], a[4:7], v[20:23], v[40:43]\n"
"v_exp_f32 v33, v33\n"
"v_mfma_f32_16x16x32_bf16 v[40:43], a[8:11], v[24:27], v[40:43]\n"
"v_exp_f32 v34, v34\n"
"v_mfma_f32_16x16x32_bf16 v[40:43], a[12:15], v[28:31], v[40:43]\n"
"v_exp_f32 v35, v35\n"
"v_mfma_f32_16x16x32_bf16 v[44:47], a[16:19], v[16:19], -4.0\n"
"v_exp_f32 v36, v36\n"
"v_mfma_f32_16x16x32_bf16 v[44:47], a[20:23], v[20:23], v[44:47]\n"
"v_exp_f32 v37, v37\n"
"v_mfma_f32_16x16x32_bf16 v[44:47], a[24:27], v[24:27], v[44:47]\n"
"v_exp_f32 v38, v38\n"
"v_mfma_f32_16x16x32_bf16 v[44:47], a[28:31], v[28:31], v[44:47]\n"
"v_exp_f32 v39, v39\n"
"v_mfma_f32_16x16x32_bf16 v[76:79], v[60:63], v[52:55], 0\n"
"v_cvt_pk_bf16_f32 v48, v32, v33\n"
"v_cvt_pk_bf16_f32 v49, v34, v35\n"
"v_cvt_pk_bf16_f32 v50, v36, v37\n"
"v_cvt_pk_bf16_f32 v51, v38, v39\n"
"ds_read_b128 v[16:19], v96 offset:28672\n"
"ds_read_b128 v[20:23], v97 offset:28672\n"
"ds_read_b128 v[24:27], v98 offset:28672\n"
"ds_read_b128 v[28:31], v99 offset:28672\n"
"ds_write_b128 v108, v[48:51] offset:16384\n"
"s_waitcnt lgkmcnt(6)\n"
"v_mfma_f32_16x16x32_bf16 v[32:35], a[0:3], v[0:3], -4.0\n"
"v_exp_f32 v40, v40\n"
"v_mfma_f32_16x16x32_bf16 v[32:35], a[4:7], v[4:7], v[32:35]\n"
"v_exp_f32 v41, v41\n"
"v_mfma_f32_16x16x32_bf16 v[32:35], a[8:11], v[8:11], v[32:35]\n"
"v_exp_f32 v42, v42\n"
"v_mfma_f32_16x16x32_bf16 v[32:35], a[12:15], v[12:15], v[32:35]\n"
"v_exp_f32 v43, v43\n"
"v_mfma_f32_16x16x32_bf16 v[36:39], a[16:19], v[0:3], -4.0\n"
"v_exp_f32 v44, v44\n"
"v_mfma_f32_16x16x32_bf16 v[36:39], a[20:23], v[4:7], v[36:39]\n"
"v_exp_f32 v45, v45\n"
"v_mfma_f32_16x16x32_bf16 v[36:39], a[24:27], v[8:11], v[36:39]\n"
"v_exp_f32 v46, v46\n"
"v_mfma_f32_16x16x32_bf16 v[36:39], a[28:31], v[12:15], v[36:39]\n"
"v_exp_f32 v47, v47\n"
"v_mfma_f32_16x16x32_bf16 v[80:83], v[60:63], v[48:51], 0\n"
"v_cvt_pk_bf16_f32 v52, v40, v41\n"
"v_cvt_pk_bf16_f32 v53, v42, v43\n"
"v_cvt_pk_bf16_f32 v54, v44, v45\n"
"v_cvt_pk_bf16_f32 v55, v46, v47\n"
"ds_read_b128 v[0:3], v96 offset:0\n"
"ds_read_b128 v[4:7], v97 offset:0\n"
"ds_read_b128 v[8:11], v98 offset:0\n"
"ds_read_b128 v[12:15], v99 offset:0\n"
"ds_write_b128 v108, v[52:55] offset:20480\n"
"s_waitcnt lgkmcnt(6)\n"
"v_mfma_f32_16x16x32_bf16 v[40:43], a[0:3], v[16:19], -4.0\n"
"v_exp_f32 v32, v32\n"
"v_mfma_f32_16x16x32_bf16 v[40:43], a[4:7], v[20:23], v[40:43]\n"
"v_exp_f32 v33, v33\n"
"v_mfma_f32_16x16x32_bf16 v[40:43], a[8:11], v[24:27], v[40:43]\n"
"v_exp_f32 v34, v34\n"
"v_mfma_f32_16x16x32_bf16 v[40:43], a[12:15], v[28:31], v[40:43]\n"
"v_exp_f32 v35, v35\n"
"v_mfma_f32_16x16x32_bf16 v[44:47], a[16:19], v[16:19], -4.0\n"
"v_exp_f32 v36, v36\n"
"v_mfma_f32_16x16x32_bf16 v[44:47], a[20:23], v[20:23], v[44:47]\n"
"v_exp_f32 v37, v37\n"
"v_mfma_f32_16x16x32_bf16 v[44:47], a[24:27], v[24:27], v[44:47]\n"
"v_exp_f32 v38, v38\n"
"v_mfma_f32_16x16x32_bf16 v[44:47], a[28:31], v[28:31], v[44:47]\n"
"v_exp_f32 v39, v39\n"
"v_mfma_f32_16x16x32_bf16 v[84:87], v[60:63], v[52:55], 0\n"
"v_cvt_pk_bf16_f32 v48, v32, v33\n"
"v_cvt_pk_bf16_f32 v49, v34, v35\n"
"v_cvt_pk_bf16_f32 v50, v36, v37\n"
"v_cvt_pk_bf16_f32 v51, v38, v39\n"
"ds_read_b128 v[16:19], v96 offset:4096\n"
"ds_read_b128 v[20:23], v97 offset:4096\n"
"ds_read_b128 v[24:27], v98 offset:4096\n"
"ds_read_b128 v[28:31], v99 offset:4096\n"
"ds_write_b128 v108, v[48:51] offset:24576\n"
"v_exp_f32 v40, v40\n"
"v_exp_f32 v41, v41\n"
"v_exp_f32 v42, v42\n"
"v_exp_f32 v43, v43\n"
"v_exp_f32 v44, v44\n"
"v_exp_f32 v45, v45\n"
"v_exp_f32 v46, v46\n"
"v_exp_f32 v47, v47\n"
"v_cvt_pk_bf16_f32 v52, v40, v41\n"
"v_cvt_pk_bf16_f32 v53, v42, v43\n"
"v_cvt_pk_bf16_f32 v54, v44, v45\n"
"v_cvt_pk_bf16_f32 v55, v46, v47\n"
"ds_write_b128 v108, v[52:55] offset:28672\n"
"s_waitcnt lgkmcnt(0)\n"
"s_barrier\n"
"s_waitcnt vmcnt(4)\n"
"ds_read_b128 a[32:35], v100 offset:0\n"
"ds_read_b128 a[36:39], v101 offset:0\n"
"ds_read_b128 a[40:43], v102 offset:0\n"
"ds_read_b128 a[44:47], v103 offset:0\n"
"s_waitcnt vmcnt(0)\n"
"ds_read_b128 a[48:51], v100 offset:4096\n"
"ds_read_b128 a[52:55], v101 offset:4096\n"
"ds_read_b128 a[56:59], v102 offset:4096\n"
"ds_read_b128 a[60:63], v103 offset:4096\n"
"s_waitcnt lgkmcnt(7)\n"
"v_mfma_f32_16x16x32_bf16 v[32:35], a[32:35], v[0:3], -4.0\n"
"s_waitcnt lgkmcnt(6)\n"
"v_mfma_f32_16x16x32_bf16 v[32:35], a[36:39], v[4:7], v[32:35]\n"
"s_waitcnt lgkmcnt(5)\n"
"v_mfma_f32_16x16x32_bf16 v[32:35], a[40:43], v[8:11], v[32:35]\n"
"s_waitcnt lgkmcnt(4)\n"
"v_mfma_f32_16x16x32_bf16 v[32:35], a[44:47], v[12:15], v[32:35]\n"
"s_waitcnt lgkmcnt(3)\n"
"v_mfma_f32_16x16x32_bf16 v[36:39], a[48:51], v[0:3], -4.0\n"
"s_waitcnt lgkmcnt(2)\n"
"v_mfma_f32_16x16x32_bf16 v[36:39], a[52:55], v[4:7], v[36:39]\n"
"s_waitcnt lgkmcnt(1)\n"
"v_mfma_f32_16x16x32_bf16 v[36:39], a[56:59], v[8:11], v[36:39]\n"
"s_waitcnt lgkmcnt(0)\n"
"v_mfma_f32_16x16x32_bf16 v[36:39], a[60:63], v[12:15], v[36:39]\n"
"v_mfma_f32_16x16x32_bf16 v[88:91], v[60:63], v[48:51], 0\n"
"ds_read_b128 v[0:3], v96 offset:8192\n"
"ds_read_b128 v[4:7], v97 offset:8192\n"
"ds_read_b128 v[8:11], v98 offset:8192\n"
"ds_read_b128 v[12:15], v99 offset:8192\n"
"s_xor_b32 s42, s62, 0x10000\n"
"s_mov_b32 m0, s54\n"
"s_add_u32 s42, s42, s53\n"
"buffer_load_dwordx4 v104, s[24:27], s42 offen offset:0 nt lds\n"
"buffer_load_dwordx4 v105, s[24:27], s42 offen offset:1024 nt lds\n"
"buffer_load_dwordx4 v106, s[24:27], s42 offen offset:2048 nt lds\n"
"buffer_load_dwordx4 v107, s[24:27], s42 offen offset:3072 nt lds\n"
"v_mfma_f32_16x16x32_bf16 v[40:43], a[32:35], v[16:19], -4.0\n"
"v_exp_f32 v32, v32\n"
"v_mfma_f32_16x16x32_bf16 v[40:43], a[36:39], v[20:23], v[40:43]\n"
"v_exp_f32 v33, v33\n"
"v_mfma_f32_16x16x32_bf16 v[40:43], a[40:43], v[24:27], v[40:43]\n"
"v_exp_f32 v34, v34\n"
"v_mfma_f32_16x16x32_bf16 v[40:43], a[44:47], v[28:31], v[40:43]\n"
"v_exp_f32 v35, v35\n"
"v_mfma_f32_16x16x32_bf16 v[44:47], a[48:51], v[16:19], -4.0\n"
"v_exp_f32 v36, v36\n"
"v_mfma_f32_16x16x32_bf16 v[44:47], a[52:55], v[20:23], v[44:47]\n"
"v_exp_f32 v37, v37\n"
"v_mfma_f32_16x16x32_bf16 v[44:47], a[56:59], v[24:27], v[44:47]\n"
"v_exp_f32 v38, v38\n"
"v_mfma_f32_16x16x32_bf16 v[44:47], a[60:63], v[28:31], v[44:47]\n"
"v_exp_f32 v39, v39\n"
"v_mfma_f32_16x16x32_bf16 v[92:95], v[60:63], v[52:55], 0\n"
"v_cvt_pk_bf16_f32 v48, v32, v33\n"
"v_cvt_pk_bf16_f32 v49, v34, v35\n"
"v_cvt_pk_bf16_f32 v50, v36, v37\n"
"v_cvt_pk_bf16_f32 v51, v38, v39\n"
"ds_read_b128 v[16:19], v96 offset:12288\n"
"ds_read_b128 v[20:23], v97 offset:12288\n"
"ds_read_b128 v[24:27], v98 offset:12288\n"
"ds_read_b128 v[28:31], v99 offset:12288\n"
"s_add_u32 s42, s42, s63\n"
"s_add_u32 m0, s54, 0x1000\n"
"s_nop 0\n"
"buffer_load_dwordx4 v104, s[24:27], s42 offen offset:0 nt lds\n"
"ds_write_b128 v108, v[48:51] offset:32768\n"
"s_barrier\n"
"s_waitcnt lgkmcnt(5)\n"
"v_mfma_f32_16x16x32_bf16 v[32:35], a[32:35], v[0:3], -4.0\n"
"v_exp_f32 v40, v40\n"
"v_mfma_f32_16x16x32_bf16 v[32:35], a[36:39], v[4:7], v[32:35]\n"
"v_exp_f32 v41, v41\n"
"v_mfma_f32_16x16x32_bf16 v[32:35], a[40:43], v[8:11], v[32:35]\n"
"v_exp_f32 v42, v42\n"
"v_mfma_f32_16x16x32_bf16 v[32:35], a[44:47], v[12:15], v[32:35]\n"
"v_exp_f32 v43, v43\n"
"v_mfma_f32_16x16x32_bf16 v[36:39], a[48:51], v[0:3], -4.0\n"
"v_exp_f32 v44, v44\n"
"v_mfma_f32_16x16x32_bf16 v[36:39], a[52:55], v[4:7], v[36:39]\n"
"v_exp_f32 v45, v45\n"
"v_mfma_f32_16x16x32_bf16 v[36:39], a[56:59], v[8:11], v[36:39]\n"
"v_exp_f32 v46, v46\n"
"v_mfma_f32_16x16x32_bf16 v[36:39], a[60:63], v[12:15], v[36:39]\n"
"v_exp_f32 v47, v47\n"
"v_mfma_f32_16x16x32_bf16 v[64:67], v[60:63], v[48:51], v[64:67]\n"
"v_cvt_pk_bf16_f32 v52, v40, v41\n"
"v_cvt_pk_bf16_f32 v53, v42, v43\n"
"v_cvt_pk_bf16_f32 v54, v44, v45\n"
"v_cvt_pk_bf16_f32 v55, v46, v47\n"
"ds_read_b128 v[0:3], v96 offset:16384\n"
"ds_read_b128 v[4:7], v97 offset:16384\n"
"ds_read_b128 v[8:11], v98 offset:16384\n"
"ds_read_b128 v[12:15], v99 offset:16384\n"
"buffer_load_dwordx4 v105, s[24:27], s42 offen offset:1024 nt lds\n"
"buffer_load_dwordx4 v106, s[24:27], s42 offen offset:2048 nt lds\n"
"ds_write_b128 v108, v[52:55] offset:0\n"
"s_waitcnt lgkmcnt(6)\n"
"v_mfma_f32_16x16x32_bf16 v[40:43], a[32:35], v[16:19], -4.0\n"
"v_exp_f32 v32, v32\n"
"v_mfma_f32_16x16x32_bf16 v[40:43], a[36:39], v[20:23], v[40:43]\n"
"v_exp_f32 v33, v33\n"
"v_mfma_f32_16x16x32_bf16 v[40:43], a[40:43], v[24:27], v[40:43]\n"
"v_exp_f32 v34, v34\n"
"v_mfma_f32_16x16x32_bf16 v[40:43], a[44:47], v[28:31], v[40:43]\n"
"v_exp_f32 v35, v35\n"
"v_mfma_f32_16x16x32_bf16 v[44:47], a[48:51], v[16:19], -4.0\n"
"v_exp_f32 v36, v36\n"
"v_mfma_f32_16x16x32_bf16 v[44:47], a[52:55], v[20:23], v[44:47]\n"
"v_exp_f32 v37, v37\n"
"v_mfma_f32_16x16x32_bf16 v[44:47], a[56:59], v[24:27], v[44:47]\n"
"v_exp_f32 v38, v38\n"
"v_mfma_f32_16x16x32_bf16 v[44:47], a[60:63], v[28:31], v[44:47]\n"
"v_exp_f32 v39, v39\n"
"v_mfma_f32_16x16x32_bf16 v[68:71], v[60:63], v[52:55], v[68:71]\n"
"v_cvt_pk_bf16_f32 v48, v32, v33\n"
"v_cvt_pk_bf16_f32 v49, v34, v35\n"
"v_cvt_pk_bf16_f32 v50, v36, v37\n"
"v_cvt_pk_bf16_f32 v51, v38, v39\n"
"ds_read_b128 v[16:19], v96 offset:20480\n"
"ds_read_b128 v[20:23], v97 offset:20480\n"
"ds_read_b128 v[24:27], v98 offset:20480\n"
"ds_read_b128 v[28:31], v99 offset:20480\n"
"buffer_load_dwordx4 v107, s[24:27], s42 offen offset:3072 nt lds\n"
"ds_write_b128 v108, v[48:51] offset:4096\n"
"s_waitcnt lgkmcnt(6)\n"
"v_mfma_f32_16x16x32_bf16 v[32:35], a[32:35], v[0:3], -4.0\n"
"v_exp_f32 v40, v40\n"
"v_mfma_f32_16x16x32_bf16 v[32:35], a[36:39], v[4:7], v[32:35]\n"
"v_exp_f32 v41, v41\n"
"v_mfma_f32_16x16x32_bf16 v[32:35], a[40:43], v[8:11], v[32:35]\n"
"v_exp_f32 v42, v42\n"
"v_mfma_f32_16x16x32_bf16 v[32:35], a[44:47], v[12:15], v[32:35]\n"
"v_exp_f32 v43, v43\n"
"v_mfma_f32_16x16x32_bf16 v[36:39], a[48:51], v[0:3], -4.0\n"
"v_exp_f32 v44, v44\n"
"v_mfma_f32_16x16x32_bf16 v[36:39], a[52:55], v[4:7], v[36:39]\n"
"v_exp_f32 v45, v45\n"
"v_mfma_f32_16x16x32_bf16 v[36:39], a[56:59], v[8:11], v[36:39]\n"
"v_exp_f32 v46, v46\n"
"v_mfma_f32_16x16x32_bf16 v[36:39], a[60:63], v[12:15], v[36:39]\n"
"v_exp_f32 v47, v47\n"
"v_mfma_f32_16x16x32_bf16 v[72:75], v[60:63], v[48:51], v[72:75]\n"
"v_cvt_pk_bf16_f32 v52, v40, v41\n"
"v_cvt_pk_bf16_f32 v53, v42, v43\n"
"v_cvt_pk_bf16_f32 v54, v44, v45\n"
"v_cvt_pk_bf16_f32 v55, v46, v47\n"
"ds_read_b128 v[0:3], v96 offset:24576\n"
"ds_read_b128 v[4:7], v97 offset:24576\n"
"ds_read_b128 v[8:11], v98 offset:24576\n"
"ds_read_b128 v[12:15], v99 offset:24576\n"
"ds_write_b128 v108, v[52:55] offset:8192\n"
"s_waitcnt lgkmcnt(6)\n"
"v_mfma_f32_16x16x32_bf16 v[40:43], a[32:35], v[16:19], -4.0\n"
"v_exp_f32 v32, v32\n"
"v_mfma_f32_16x16x32_bf16 v[40:43], a[36:39], v[20:23], v[40:43]\n"
"v_exp_f32 v33, v33\n"
"v_mfma_f32_16x16x32_bf16 v[40:43], a[40:43], v[24:27], v[40:43]\n"
"v_exp_f32 v34, v34\n"
"v_mfma_f32_16x16x32_bf16 v[40:43], a[44:47], v[28:31], v[40:43]\n"
"v_exp_f32 v35, v35\n"
"v_mfma_f32_16x16x32_bf16 v[44:47], a[48:51], v[16:19], -4.0\n"
"v_exp_f32 v36, v36\n"
"v_mfma_f32_16x16x32_bf16 v[44:47], a[52:55], v[20:23], v[44:47]\n"
"v_exp_f32 v37, v37\n"
"v_mfma_f32_16x16x32_bf16 v[44:47], a[56:59], v[24:27], v[44:47]\n"
"v_exp_f32 v38, v38\n"
"v_mfma_f32_16x16x32_bf16 v[44:47], a[60:63], v[28:31], v[44:47]\n"
"v_exp_f32 v39, v39\n"
"v_mfma_f32_16x16x32_bf16 v[76:79], v[60:63], v[52:55], v[76:79]\n"
"v_cvt_pk_bf16_f32 v48, v32, v33\n"
"v_cvt_pk_bf16_f32 v49, v34, v35\n"
"v_cvt_pk_bf16_f32 v50, v36, v37\n"
"v_cvt_pk_bf16_f32 v51, v38, v39\n"
"ds_read_b128 v[16:19], v96 offset:28672\n"
"ds_read_b128 v[20:23], v97 offset:28672\n"
"ds_read_b128 v[24:27], v98 offset:28672\n"
"ds_read_b128 v[28:31], v99 offset:28672\n"
"ds_write_b128 v108, v[48:51] offset:12288\n"
"s_waitcnt lgkmcnt(5)\n"
"s_barrier\n"
"v_mfma_f32_16x16x32_bf16 v[32:35], a[32:35], v[0:3], -4.0\n"
"v_exp_f32 v40, v40\n"
"v_mfma_f32_16x16x32_bf16 v[32:35], a[36:39], v[4:7], v[32:35]\n"
"v_exp_f32 v41, v41\n"
"v_mfma_f32_16x16x32_bf16 v[32:35], a[40:43], v[8:11], v[32:35]\n"
"v_exp_f32 v42, v42\n"
"v_mfma_f32_16x16x32_bf16 v[32:35], a[44:47], v[12:15], v[32:35]\n"
"v_exp_f32 v43, v43\n"
"v_mfma_f32_16x16x32_bf16 v[36:39], a[48:51], v[0:3], -4.0\n"
"v_exp_f32 v44, v44\n"
"v_mfma_f32_16x16x32_bf16 v[36:39], a[52:55], v[4:7], v[36:39]\n"
"v_exp_f32 v45, v45\n"
"v_mfma_f32_16x16x32_bf16 v[36:39], a[56:59], v[8:11], v[36:39]\n"
"v_exp_f32 v46, v46\n"
"v_mfma_f32_16x16x32_bf16 v[36:39], a[60:63], v[12:15], v[36:39]\n"
"v_exp_f32 v47, v47\n"
"v_mfma_f32_16x16x32_bf16 v[80:83], v[60:63], v[48:51], v[80:83]\n"
"v_cvt_pk_bf16_f32 v52, v40, v41\n"
"v_cvt_pk_bf16_f32 v53, v42, v43\n"
"v_cvt_pk_bf16_f32 v54, v44, v45\n"
"v_cvt_pk_bf16_f32 v55, v46, v47\n"
"ds_read_b128 v[0:3], v96 offset:0\n"
"ds_read_b128 v[4:7], v97 offset:0\n"
"ds_read_b128 v[8:11], v98 offset:0\n"
"ds_read_b128 v[12:15], v99 offset:0\n"
"s_waitcnt vmcnt(4)\n"
"ds_read_b128 a[64:67], v100 offset:0\n"
"ds_read_b128 a[68:71], v101 offset:0\n"
"ds_read_b128 a[72:75], v102 offset:0\n"
"ds_read_b128 a[76:79], v103 offset:0\n"
"ds_write_b128 v108, v[52:55] offset:16384\n"
"s_waitcnt lgkmcnt(10)\n"
"v_mfma_f32_16x16x32_bf16 v[40:43], a[32:35], v[16:19], -4.0\n"
"v_exp_f32 v32, v32\n"
"v_mfma_f32_16x16x32_bf16 v[40:43], a[36:39], v[20:23], v[40:43]\n"
"v_exp_f32 v33, v33\n"
"v_mfma_f32_16x16x32_bf16 v[40:43], a[40:43], v[24:27], v[40:43]\n"
"v_exp_f32 v34, v34\n"
"v_mfma_f32_16x16x32_bf16 v[40:43], a[44:47], v[28:31], v[40:43]\n"
"v_exp_f32 v35, v35\n"
"v_mfma_f32_16x16x32_bf16 v[44:47], a[48:51], v[16:19], -4.0\n"
"v_exp_f32 v36, v36\n"
"v_mfma_f32_16x16x32_bf16 v[44:47], a[52:55], v[20:23], v[44:47]\n"
"v_exp_f32 v37, v37\n"
"v_mfma_f32_16x16x32_bf16 v[44:47], a[56:59], v[24:27], v[44:47]\n"
"v_exp_f32 v38, v38\n"
"v_mfma_f32_16x16x32_bf16 v[44:47], a[60:63], v[28:31], v[44:47]\n"
"v_exp_f32 v39, v39\n"
"v_mfma_f32_16x16x32_bf16 v[84:87], v[60:63], v[52:55], v[84:87]\n"
"v_cvt_pk_bf16_f32 v48, v32, v33\n"
"v_cvt_pk_bf16_f32 v49, v34, v35\n"
"v_cvt_pk_bf16_f32 v50, v36, v37\n"
"v_cvt_pk_bf16_f32 v51, v38, v39\n"
"ds_read_b128 v[16:19], v96 offset:4096\n"
"ds_read_b128 v[20:23], v97 offset:4096\n"
"ds_read_b128 v[24:27], v98 offset:4096\n"
"ds_read_b128 v[28:31], v99 offset:4096\n"
"s_waitcnt vmcnt(0)\n"
"ds_read_b128 a[80:83], v100 offset:4096\n"
"ds_read_b128 a[84:87], v101 offset:4096\n"
"ds_read_b128 a[88:91], v102 offset:4096\n"
"ds_read_b128 a[92:95], v103 offset:4096\n"
"s_waitcnt lgkmcnt(9)\n"
"s_xor_b32 s42, s62, 0x18000\n"
"s_mov_b32 m0, s54\n"
"s_add_u32 s42, s42, s53\n"
"buffer_load_dwordx4 v104, s[24:27], s42 offen offset:0 nt lds\n"
"buffer_load_dwordx4 v105, s[24:27], s42 offen offset:1024 nt lds\n"
"ds_write_b128 v108, v[48:51] offset:20480\n"
"v_mfma_f32_16x16x32_bf16 v[32:35], a[64:67], v[0:3], -4.0\n"
"v_exp_f32 v40, v40\n"
"v_mfma_f32_16x16x32_bf16 v[32:35], a[68:71], v[4:7], v[32:35]\n"
"v_exp_f32 v41, v41\n"
"v_mfma_f32_16x16x32_bf16 v[32:35], a[72:75], v[8:11], v[32:35]\n"
"v_exp_f32 v42, v42\n"
"v_mfma_f32_16x16x32_bf16 v[32:35], a[76:79], v[12:15], v[32:35]\n"
"v_exp_f32 v43, v43\n"
"s_waitcnt lgkmcnt(4)\n"
"v_mfma_f32_16x16x32_bf16 v[36:39], a[80:83], v[0:3], -4.0\n"
"v_exp_f32 v44, v44\n"
"s_waitcnt lgkmcnt(3)\n"
"v_mfma_f32_16x16x32_bf16 v[36:39], a[84:87], v[4:7], v[36:39]\n"
"v_exp_f32 v45, v45\n"
"s_waitcnt lgkmcnt(2)\n"
"v_mfma_f32_16x16x32_bf16 v[36:39], a[88:91], v[8:11], v[36:39]\n"
"v_exp_f32 v46, v46\n"
"s_waitcnt lgkmcnt(1)\n"
"v_mfma_f32_16x16x32_bf16 v[36:39], a[92:95], v[12:15], v[36:39]\n"
"v_exp_f32 v47, v47\n"
"v_mfma_f32_16x16x32_bf16 v[88:91], v[60:63], v[48:51], v[88:91]\n"
"v_cvt_pk_bf16_f32 v52, v40, v41\n"
"v_cvt_pk_bf16_f32 v53, v42, v43\n"
"v_cvt_pk_bf16_f32 v54, v44, v45\n"
"v_cvt_pk_bf16_f32 v55, v46, v47\n"
"ds_read_b128 v[0:3], v96 offset:8192\n"
"ds_read_b128 v[4:7], v97 offset:8192\n"
"ds_read_b128 v[8:11], v98 offset:8192\n"
"ds_read_b128 v[12:15], v99 offset:8192\n"
"buffer_load_dwordx4 v106, s[24:27], s42 offen offset:2048 nt lds\n"
"buffer_load_dwordx4 v107, s[24:27], s42 offen offset:3072 nt lds\n"
"s_add_u32 s42, s42, s63\n"
"s_add_u32 m0, s54, 0x1000\n"
"s_nop 0\n"
"buffer_load_dwordx4 v104, s[24:27], s42 offen offset:0 nt lds\n"
"ds_write_b128 v108, v[52:55] offset:24576\n"
"v_mfma_f32_16x16x32_bf16 v[40:43], a[64:67], v[16:19], -4.0\n"
"v_exp_f32 v32, v32\n"
"v_mfma_f32_16x16x32_bf16 v[40:43], a[68:71], v[20:23], v[40:43]\n"
"v_exp_f32 v33, v33\n"
"v_mfma_f32_16x16x32_bf16 v[40:43], a[72:75], v[24:27], v[40:43]\n"
"v_exp_f32 v34, v34\n"
"v_mfma_f32_16x16x32_bf16 v[40:43], a[76:79], v[28:31], v[40:43]\n"
"v_exp_f32 v35, v35\n"
"v_mfma_f32_16x16x32_bf16 v[44:47], a[80:83], v[16:19], -4.0\n"
"v_exp_f32 v36, v36\n"
"v_mfma_f32_16x16x32_bf16 v[44:47], a[84:87], v[20:23], v[44:47]\n"
"v_exp_f32 v37, v37\n"
"v_mfma_f32_16x16x32_bf16 v[44:47], a[88:91], v[24:27], v[44:47]\n"
"v_exp_f32 v38, v38\n"
"v_mfma_f32_16x16x32_bf16 v[44:47], a[92:95], v[28:31], v[44:47]\n"
"v_exp_f32 v39, v39\n"
"v_mfma_f32_16x16x32_bf16 v[92:95], v[60:63], v[52:55], v[92:95]\n"
"v_cvt_pk_bf16_f32 v48, v32, v33\n"
"v_cvt_pk_bf16_f32 v49, v34, v35\n"
"v_cvt_pk_bf16_f32 v50, v36, v37\n"
"v_cvt_pk_bf16_f32 v51, v38, v39\n"
"ds_read_b128 v[16:19], v96 offset:12288\n"
"ds_read_b128 v[20:23], v97 offset:12288\n"
"ds_read_b128 v[24:27], v98 offset:12288\n"
"ds_read_b128 v[28:31], v99 offset:12288\n"
"ds_write_b128 v108, v[48:51] offset:28672\n"
"s_waitcnt lgkmcnt(5)\n"
"s_barrier\n"
"v_mfma_f32_16x16x32_bf16 v[32:35], a[64:67], v[0:3], -4.0\n"
"v_exp_f32 v40, v40\n"
"v_mfma_f32_16x16x32_bf16 v[32:35], a[68:71], v[4:7], v[32:35]\n"
"v_exp_f32 v41, v41\n"
"v_mfma_f32_16x16x32_bf16 v[32:35], a[72:75], v[8:11], v[32:35]\n"
"v_exp_f32 v42, v42\n"
"v_mfma_f32_16x16x32_bf16 v[32:35], a[76:79], v[12:15], v[32:35]\n"
"v_exp_f32 v43, v43\n"
"v_mfma_f32_16x16x32_bf16 v[36:39], a[80:83], v[0:3], -4.0\n"
"v_exp_f32 v44, v44\n"
"v_mfma_f32_16x16x32_bf16 v[36:39], a[84:87], v[4:7], v[36:39]\n"
"v_exp_f32 v45, v45\n"
"v_mfma_f32_16x16x32_bf16 v[36:39], a[88:91], v[8:11], v[36:39]\n"
"v_exp_f32 v46, v46\n"
"v_mfma_f32_16x16x32_bf16 v[36:39], a[92:95], v[12:15], v[36:39]\n"
"v_exp_f32 v47, v47\n"
"v_mfma_f32_16x16x32_bf16 v[64:67], v[60:63], v[48:51], v[64:67]\n"
"v_cvt_pk_bf16_f32 v52, v40, v41\n"
"v_cvt_pk_bf16_f32 v53, v42, v43\n"
"v_cvt_pk_bf16_f32 v54, v44, v45\n"
"v_cvt_pk_bf16_f32 v55, v46, v47\n"
"ds_read_b128 v[0:3], v96 offset:16384\n"
"ds_read_b128 v[4:7], v97 offset:16384\n"
"ds_read_b128 v[8:11], v98 offset:16384\n"
"ds_read_b128 v[12:15], v99 offset:16384\n"
"buffer_load_dwordx4 v105, s[24:27], s42 offen offset:1024 nt lds\n"
"buffer_load_dwordx4 v106, s[24:27], s42 offen offset:2048 nt lds\n"
"ds_write_b128 v108, v[52:55] offset:32768\n"
"s_waitcnt lgkmcnt(6)\n"
"v_mfma_f32_16x16x32_bf16 v[40:43], a[64:67], v[16:19], -4.0\n"
"v_exp_f32 v32, v32\n"
"v_mfma_f32_16x16x32_bf16 v[40:43], a[68:71], v[20:23], v[40:43]\n"
"v_exp_f32 v33, v33\n"
"v_mfma_f32_16x16x32_bf16 v[40:43], a[72:75], v[24:27], v[40:43]\n"
"v_exp_f32 v34, v34\n"
"v_mfma_f32_16x16x32_bf16 v[40:43], a[76:79], v[28:31], v[40:43]\n"
"v_exp_f32 v35, v35\n"
"v_mfma_f32_16x16x32_bf16 v[44:47], a[80:83], v[16:19], -4.0\n"
"v_exp_f32 v36, v36\n"
"v_mfma_f32_16x16x32_bf16 v[44:47], a[84:87], v[20:23], v[44:47]\n"
"v_exp_f32 v37, v37\n"
"v_mfma_f32_16x16x32_bf16 v[44:47], a[88:91], v[24:27], v[44:47]\n"
"v_exp_f32 v38, v38\n"
"v_mfma_f32_16x16x32_bf16 v[44:47], a[92:95], v[28:31], v[44:47]\n"
"v_exp_f32 v39, v39\n"
"v_mfma_f32_16x16x32_bf16 v[68:71], v[60:63], v[52:55], v[68:71]\n"
"v_cvt_pk_bf16_f32 v48, v32, v33\n"
"v_cvt_pk_bf16_f32 v49, v34, v35\n"
"v_cvt_pk_bf16_f32 v50, v36, v37\n"
"v_cvt_pk_bf16_f32 v51, v38, v39\n"
"ds_read_b128 v[16:19], v96 offset:20480\n"
"ds_read_b128 v[20:23], v97 offset:20480\n"
"ds_read_b128 v[24:27], v98 offset:20480\n"
"ds_read_b128 v[28:31], v99 offset:20480\n"
"buffer_load_dwordx4 v107, s[24:27], s42 offen offset:3072 nt lds\n"
"ds_write_b128 v108, v[48:51] offset:0\n"
"s_waitcnt lgkmcnt(6)\n"
"v_mfma_f32_16x16x32_bf16 v[32:35], a[64:67], v[0:3], -4.0\n"
"v_exp_f32 v40, v40\n"
"v_mfma_f32_16x16x32_bf16 v[32:35], a[68:71], v[4:7], v[32:35]\n"
"v_exp_f32 v41, v41\n"
"v_mfma_f32_16x16x32_bf16 v[32:35], a[72:75], v[8:11], v[32:35]\n"
"v_exp_f32 v42, v42\n"
"v_mfma_f32_16x16x32_bf16 v[32:35], a[76:79], v[12:15], v[32:35]\n"
"v_exp_f32 v43, v43\n"
"v_mfma_f32_16x16x32_bf16 v[36:39], a[80:83], v[0:3], -4.0\n"
"v_exp_f32 v44, v44\n"
"v_mfma_f32_16x16x32_bf16 v[36:39], a[84:87], v[4:7], v[36:39]\n"
"v_exp_f32 v45, v45\n"
"v_mfma_f32_16x16x32_bf16 v[36:39], a[88:91], v[8:11], v[36:39]\n"
"v_exp_f32 v46, v46\n"
"v_mfma_f32_16x16x32_bf16 v[36:39], a[92:95], v[12:15], v[36:39]\n"
"v_exp_f32 v47, v47\n"
"v_mfma_f32_16x16x32_bf16 v[72:75], v[60:63], v[48:51], v[72:75]\n"
"v_cvt_pk_bf16_f32 v52, v40, v41\n"
"v_cvt_pk_bf16_f32 v53, v42, v43\n"
"v_cvt_pk_bf16_f32 v54, v44, v45\n"
"v_cvt_pk_bf16_f32 v55, v46, v47\n"
"ds_read_b128 v[0:3], v96 offset:24576\n"
"ds_read_b128 v[4:7], v97 offset:24576\n"
"ds_read_b128 v[8:11], v98 offset:24576\n"
"ds_read_b128 v[12:15], v99 offset:24576\n"
"ds_write_b128 v108, v[52:55] offset:4096\n"
"s_waitcnt lgkmcnt(6)\n"
"v_mfma_f32_16x16x32_bf16 v[40:43], a[64:67], v[16:19], -4.0\n"
"v_exp_f32 v32, v32\n"
"v_mfma_f32_16x16x32_bf16 v[40:43], a[68:71], v[20:23], v[40:43]\n"
"v_exp_f32 v33, v33\n"
"v_mfma_f32_16x16x32_bf16 v[40:43], a[72:75], v[24:27], v[40:43]\n"
"v_exp_f32 v34, v34\n"
"v_mfma_f32_16x16x32_bf16 v[40:43], a[76:79], v[28:31], v[40:43]\n"
"v_exp_f32 v35, v35\n"
"v_mfma_f32_16x16x32_bf16 v[44:47], a[80:83], v[16:19], -4.0\n"
"v_exp_f32 v36, v36\n"
"v_mfma_f32_16x16x32_bf16 v[44:47], a[84:87], v[20:23], v[44:47]\n"
"v_exp_f32 v37, v37\n"
"v_mfma_f32_16x16x32_bf16 v[44:47], a[88:91], v[24:27], v[44:47]\n"
"v_exp_f32 v38, v38\n"
"v_mfma_f32_16x16x32_bf16 v[44:47], a[92:95], v[28:31], v[44:47]\n"
"v_exp_f32 v39, v39\n"
"v_mfma_f32_16x16x32_bf16 v[76:79], v[60:63], v[52:55], v[76:79]\n"
"v_cvt_pk_bf16_f32 v48, v32, v33\n"
"v_cvt_pk_bf16_f32 v49, v34, v35\n"
"v_cvt_pk_bf16_f32 v50, v36, v37\n"
"v_cvt_pk_bf16_f32 v51, v38, v39\n"
"ds_read_b128 v[16:19], v96 offset:28672\n"
"ds_read_b128 v[20:23], v97 offset:28672\n"
"ds_read_b128 v[24:27], v98 offset:28672\n"
"ds_read_b128 v[28:31], v99 offset:28672\n"
"ds_write_b128 v108, v[48:51] offset:8192\n"
"s_waitcnt lgkmcnt(5)\n"
"s_barrier\n"
"v_mfma_f32_16x16x32_bf16 v[32:35], a[64:67], v[0:3], -4.0\n"
"v_exp_f32 v40, v40\n"
"v_mfma_f32_16x16x32_bf16 v[32:35], a[68:71], v[4:7], v[32:35]\n"
"v_exp_f32 v41, v41\n"
"v_mfma_f32_16x16x32_bf16 v[32:35], a[72:75], v[8:11], v[32:35]\n"
"v_exp_f32 v42, v42\n"
"v_mfma_f32_16x16x32_bf16 v[32:35], a[76:79], v[12:15], v[32:35]\n"
"v_exp_f32 v43, v43\n"
"v_mfma_f32_16x16x32_bf16 v[36:39], a[80:83], v[0:3], -4.0\n"
"v_exp_f32 v44, v44\n"
"v_mfma_f32_16x16x32_bf16 v[36:39], a[84:87], v[4:7], v[36:39]\n"
"v_exp_f32 v45, v45\n"
"v_mfma_f32_16x16x32_bf16 v[36:39], a[88:91], v[8:11], v[36:39]\n"
"v_exp_f32 v46, v46\n"
"v_mfma_f32_16x16x32_bf16 v[36:39], a[92:95], v[12:15], v[36:39]\n"
"v_exp_f32 v47, v47\n"
"v_mfma_f32_16x16x32_bf16 v[80:83], v[60:63], v[48:51], v[80:83]\n"
"v_cvt_pk_bf16_f32 v52, v40, v41\n"
"v_cvt_pk_bf16_f32 v53, v42, v43\n"
"v_cvt_pk_bf16_f32 v54, v44, v45\n"
"v_cvt_pk_bf16_f32 v55, v46, v47\n"
"ds_read_b128 v[0:3], v96 offset:0\n"
"ds_read_b128 v[4:7], v97 offset:0\n"
"ds_read_b128 v[8:11], v98 offset:0\n"
"ds_read_b128 v[12:15], v99 offset:0\n"
"s_waitcnt vmcnt(4)\n"
"ds_read_b128 a[96:99], v100 offset:0\n"
"ds_read_b128 a[100:103], v101 offset:0\n"
"ds_read_b128 a[104:107], v102 offset:0\n"
"ds_read_b128 a[108:111], v103 offset:0\n"
"ds_write_b128 v108, v[52:55] offset:12288\n"
"s_waitcnt lgkmcnt(10)\n"
"v_mfma_f32_16x16x32_bf16 v[40:43], a[64:67], v[16:19], -4.0\n"
"v_exp_f32 v32, v32\n"
"v_mfma_f32_16x16x32_bf16 v[40:43], a[68:71], v[20:23], v[40:43]\n"
"v_exp_f32 v33, v33\n"
"v_mfma_f32_16x16x32_bf16 v[40:43], a[72:75], v[24:27], v[40:43]\n"
"v_exp_f32 v34, v34\n"
"v_mfma_f32_16x16x32_bf16 v[40:43], a[76:79], v[28:31], v[40:43]\n"
"v_exp_f32 v35, v35\n"
"v_mfma_f32_16x16x32_bf16 v[44:47], a[80:83], v[16:19], -4.0\n"
"v_exp_f32 v36, v36\n"
"v_mfma_f32_16x16x32_bf16 v[44:47], a[84:87], v[20:23], v[44:47]\n"
"v_exp_f32 v37, v37\n"
"v_mfma_f32_16x16x32_bf16 v[44:47], a[88:91], v[24:27], v[44:47]\n"
"v_exp_f32 v38, v38\n"
"v_mfma_f32_16x16x32_bf16 v[44:47], a[92:95], v[28:31], v[44:47]\n"
"v_exp_f32 v39, v39\n"
"v_mfma_f32_16x16x32_bf16 v[84:87], v[60:63], v[52:55], v[84:87]\n"
"v_cvt_pk_bf16_f32 v48, v32, v33\n"
"v_cvt_pk_bf16_f32 v49, v34, v35\n"
"v_cvt_pk_bf16_f32 v50, v36, v37\n"
"v_cvt_pk_bf16_f32 v51, v38, v39\n"
"ds_read_b128 v[16:19], v96 offset:4096\n"
"ds_read_b128 v[20:23], v97 offset:4096\n"
"ds_read_b128 v[24:27], v98 offset:4096\n"
"ds_read_b128 v[28:31], v99 offset:4096\n"
"s_waitcnt vmcnt(0)\n"
"ds_read_b128 a[112:115], v100 offset:4096\n"
"ds_read_b128 a[116:119], v101 offset:4096\n"
"ds_read_b128 a[120:123], v102 offset:4096\n"
"ds_read_b128 a[124:127], v103 offset:4096\n"
"ds_write_b128 v108, v[48:51] offset:16384\n"
"s_waitcnt lgkmcnt(13)\n"
"v_mfma_f32_16x16x32_bf16 v[32:35], a[96:99], v[0:3], -4.0\n"
"v_exp_f32 v40, v40\n"
"s_waitcnt lgkmcnt(12)\n"
"v_mfma_f32_16x16x32_bf16 v[32:35], a[100:103], v[4:7], v[32:35]\n"
"v_exp_f32 v41, v41\n"
"s_waitcnt lgkmcnt(11)\n"
"v_mfma_f32_16x16x32_bf16 v[32:35], a[104:107], v[8:11], v[32:35]\n"
"v_exp_f32 v42, v42\n"
"s_waitcnt lgkmcnt(10)\n"
"v_mfma_f32_16x16x32_bf16 v[32:35], a[108:111], v[12:15], v[32:35]\n"
"v_exp_f32 v43, v43\n"
"s_waitcnt lgkmcnt(4)\n"
"v_mfma_f32_16x16x32_bf16 v[36:39], a[112:115], v[0:3], -4.0\n"
"v_exp_f32 v44, v44\n"
"s_waitcnt lgkmcnt(3)\n"
"v_mfma_f32_16x16x32_bf16 v[36:39], a[116:119], v[4:7], v[36:39]\n"
"v_exp_f32 v45, v45\n"
"s_waitcnt lgkmcnt(2)\n"
"v_mfma_f32_16x16x32_bf16 v[36:39], a[120:123], v[8:11], v[36:39]\n"
"v_exp_f32 v46, v46\n"
"s_waitcnt lgkmcnt(1)\n"
"v_mfma_f32_16x16x32_bf16 v[36:39], a[124:127], v[12:15], v[36:39]\n"
"v_exp_f32 v47, v47\n"
"v_mfma_f32_16x16x32_bf16 v[88:91], v[60:63], v[48:51], v[88:91]\n"
"v_cvt_pk_bf16_f32 v52, v40, v41\n"
"v_cvt_pk_bf16_f32 v53, v42, v43\n"
"v_cvt_pk_bf16_f32 v54, v44, v45\n"
"v_cvt_pk_bf16_f32 v55, v46, v47\n"
"ds_read_b128 v[0:3], v96 offset:8192\n"
"ds_read_b128 v[4:7], v97 offset:8192\n"
"ds_read_b128 v[8:11], v98 offset:8192\n"
"ds_read_b128 v[12:15], v99 offset:8192\n"
"ds_write_b128 v108, v[52:55] offset:20480\n"
"v_mfma_f32_16x16x32_bf16 v[40:43], a[96:99], v[16:19], -4.0\n"
"v_exp_f32 v32, v32\n"
"v_mfma_f32_16x16x32_bf16 v[40:43], a[100:103], v[20:23], v[40:43]\n"
"v_exp_f32 v33, v33\n"
"v_mfma_f32_16x16x32_bf16 v[40:43], a[104:107], v[24:27], v[40:43]\n"
"v_exp_f32 v34, v34\n"
"v_mfma_f32_16x16x32_bf16 v[40:43], a[108:111], v[28:31], v[40:43]\n"
"v_exp_f32 v35, v35\n"
"v_mfma_f32_16x16x32_bf16 v[44:47], a[112:115], v[16:19], -4.0\n"
"v_exp_f32 v36, v36\n"
"v_mfma_f32_16x16x32_bf16 v[44:47], a[116:119], v[20:23], v[44:47]\n"
"v_exp_f32 v37, v37\n"
"v_mfma_f32_16x16x32_bf16 v[44:47], a[120:123], v[24:27], v[44:47]\n"
"v_exp_f32 v38, v38\n"
"v_mfma_f32_16x16x32_bf16 v[44:47], a[124:127], v[28:31], v[44:47]\n"
"v_exp_f32 v39, v39\n"
"v_mfma_f32_16x16x32_bf16 v[92:95], v[60:63], v[52:55], v[92:95]\n"
"v_cvt_pk_bf16_f32 v48, v32, v33\n"
"v_cvt_pk_bf16_f32 v49, v34, v35\n"
"v_cvt_pk_bf16_f32 v50, v36, v37\n"
"v_cvt_pk_bf16_f32 v51, v38, v39\n"
"ds_read_b128 v[16:19], v96 offset:12288\n"
"ds_read_b128 v[20:23], v97 offset:12288\n"
"ds_read_b128 v[24:27], v98 offset:12288\n"
"ds_read_b128 v[28:31], v99 offset:12288\n"
"ds_write_b128 v108, v[48:51] offset:24576\n"
"s_waitcnt lgkmcnt(5)\n"
"s_barrier\n"
"v_mfma_f32_16x16x32_bf16 v[32:35], a[96:99], v[0:3], -4.0\n"
"v_exp_f32 v40, v40\n"
"v_mfma_f32_16x16x32_bf16 v[32:35], a[100:103], v[4:7], v[32:35]\n"
"v_exp_f32 v41, v41\n"
"v_mfma_f32_16x16x32_bf16 v[32:35], a[104:107], v[8:11], v[32:35]\n"
"v_exp_f32 v42, v42\n"
"v_mfma_f32_16x16x32_bf16 v[32:35], a[108:111], v[12:15], v[32:35]\n"
"v_exp_f32 v43, v43\n"
"v_mfma_f32_16x16x32_bf16 v[36:39], a[112:115], v[0:3], -4.0\n"
"v_exp_f32 v44, v44\n"
"v_mfma_f32_16x16x32_bf16 v[36:39], a[116:119], v[4:7], v[36:39]\n"
"v_exp_f32 v45, v45\n"
"v_mfma_f32_16x16x32_bf16 v[36:39], a[120:123], v[8:11], v[36:39]\n"
"v_exp_f32 v46, v46\n"
"v_mfma_f32_16x16x32_bf16 v[36:39], a[124:127], v[12:15], v[36:39]\n"
"v_exp_f32 v47, v47\n"
"v_mfma_f32_16x16x32_bf16 v[64:67], v[60:63], v[48:51], v[64:67]\n"
"v_cvt_pk_bf16_f32 v52, v40, v41\n"
"v_cvt_pk_bf16_f32 v53, v42, v43\n"
"v_cvt_pk_bf16_f32 v54, v44, v45\n"
"v_cvt_pk_bf16_f32 v55, v46, v47\n"
"ds_read_b128 v[0:3], v96 offset:16384\n"
"ds_read_b128 v[4:7], v97 offset:16384\n"
"ds_read_b128 v[8:11], v98 offset:16384\n"
"ds_read_b128 v[12:15], v99 offset:16384\n"
"ds_write_b128 v108, v[52:55] offset:28672\n"
"s_waitcnt lgkmcnt(6)\n"
"v_mfma_f32_16x16x32_bf16 v[40:43], a[96:99], v[16:19], -4.0\n"
"v_exp_f32 v32, v32\n"
"v_mfma_f32_16x16x32_bf16 v[40:43], a[100:103], v[20:23], v[40:43]\n"
"v_exp_f32 v33, v33\n"
"v_mfma_f32_16x16x32_bf16 v[40:43], a[104:107], v[24:27], v[40:43]\n"
"v_exp_f32 v34, v34\n"
"v_mfma_f32_16x16x32_bf16 v[40:43], a[108:111], v[28:31], v[40:43]\n"
"v_exp_f32 v35, v35\n"
"v_mfma_f32_16x16x32_bf16 v[44:47], a[112:115], v[16:19], -4.0\n"
"v_exp_f32 v36, v36\n"
"v_mfma_f32_16x16x32_bf16 v[44:47], a[116:119], v[20:23], v[44:47]\n"
"v_exp_f32 v37, v37\n"
"v_mfma_f32_16x16x32_bf16 v[44:47], a[120:123], v[24:27], v[44:47]\n"
"v_exp_f32 v38, v38\n"
"v_mfma_f32_16x16x32_bf16 v[44:47], a[124:127], v[28:31], v[44:47]\n"
"v_exp_f32 v39, v39\n"
"v_mfma_f32_16x16x32_bf16 v[68:71], v[60:63], v[52:55], v[68:71]\n"
"v_cvt_pk_bf16_f32 v48, v32, v33\n"
"v_cvt_pk_bf16_f32 v49, v34, v35\n"
"v_cvt_pk_bf16_f32 v50, v36, v37\n"
"v_cvt_pk_bf16_f32 v51, v38, v39\n"
"ds_read_b128 v[16:19], v96 offset:20480\n"
"ds_read_b128 v[20:23], v97 offset:20480\n"
"ds_read_b128 v[24:27], v98 offset:20480\n"
"ds_read_b128 v[28:31], v99 offset:20480\n"
"ds_write_b32 v109, v64 offset:0\n"
"ds_write_b128 v108, v[48:51] offset:32768\n"
"s_waitcnt lgkmcnt(7)\n"
"v_mfma_f32_16x16x32_bf16 v[32:35], a[96:99], v[0:3], -4.0\n"
"v_exp_f32 v40, v40\n"
"v_mfma_f32_16x16x32_bf16 v[32:35], a[100:103], v[4:7], v[32:35]\n"
"v_exp_f32 v41, v41\n"
"v_mfma_f32_16x16x32_bf16 v[32:35], a[104:107], v[8:11], v[32:35]\n"
"v_exp_f32 v42, v42\n"
"v_mfma_f32_16x16x32_bf16 v[32:35], a[108:111], v[12:15], v[32:35]\n"
"v_exp_f32 v43, v43\n"
"v_mfma_f32_16x16x32_bf16 v[36:39], a[112:115], v[0:3], -4.0\n"
"v_exp_f32 v44, v44\n"
"v_mfma_f32_16x16x32_bf16 v[36:39], a[116:119], v[4:7], v[36:39]\n"
"v_exp_f32 v45, v45\n"
"v_mfma_f32_16x16x32_bf16 v[36:39], a[120:123], v[8:11], v[36:39]\n"
"v_exp_f32 v46, v46\n"
"v_mfma_f32_16x16x32_bf16 v[36:39], a[124:127], v[12:15], v[36:39]\n"
"v_exp_f32 v47, v47\n"
"v_mfma_f32_16x16x32_bf16 v[72:75], v[60:63], v[48:51], v[72:75]\n"
"v_cvt_pk_bf16_f32 v52, v40, v41\n"
"v_cvt_pk_bf16_f32 v53, v42, v43\n"
"v_cvt_pk_bf16_f32 v54, v44, v45\n"
"v_cvt_pk_bf16_f32 v55, v46, v47\n"
"ds_read_b128 v[0:3], v96 offset:24576\n"
"ds_read_b128 v[4:7], v97 offset:24576\n"
"ds_read_b128 v[8:11], v98 offset:24576\n"
"ds_read_b128 v[12:15], v99 offset:24576\n"
"ds_write_b32 v109, v68 offset:1024\n"
"ds_write_b128 v108, v[52:55] offset:0\n"
"s_waitcnt lgkmcnt(8)\n"
"v_mfma_f32_16x16x32_bf16 v[40:43], a[96:99], v[16:19], -4.0\n"
"v_exp_f32 v32, v32\n"
"v_mfma_f32_16x16x32_bf16 v[40:43], a[100:103], v[20:23], v[40:43]\n"
"v_exp_f32 v33, v33\n"
"v_mfma_f32_16x16x32_bf16 v[40:43], a[104:107], v[24:27], v[40:43]\n"
"v_exp_f32 v34, v34\n"
"v_mfma_f32_16x16x32_bf16 v[40:43], a[108:111], v[28:31], v[40:43]\n"
"v_exp_f32 v35, v35\n"
"v_mfma_f32_16x16x32_bf16 v[44:47], a[112:115], v[16:19], -4.0\n"
"v_exp_f32 v36, v36\n"
"v_mfma_f32_16x16x32_bf16 v[44:47], a[116:119], v[20:23], v[44:47]\n"
"v_exp_f32 v37, v37\n"
"v_mfma_f32_16x16x32_bf16 v[44:47], a[120:123], v[24:27], v[44:47]\n"
"v_exp_f32 v38, v38\n"
"v_mfma_f32_16x16x32_bf16 v[44:47], a[124:127], v[28:31], v[44:47]\n"
"v_exp_f32 v39, v39\n"
"v_mfma_f32_16x16x32_bf16 v[76:79], v[60:63], v[52:55], v[76:79]\n"
"v_cvt_pk_bf16_f32 v48, v32, v33\n"
"v_cvt_pk_bf16_f32 v49, v34, v35\n"
"v_cvt_pk_bf16_f32 v50, v36, v37\n"
"v_cvt_pk_bf16_f32 v51, v38, v39\n"
"ds_read_b128 v[16:19], v96 offset:28672\n"
"ds_read_b128 v[20:23], v97 offset:28672\n"
"ds_read_b128 v[24:27], v98 offset:28672\n"
"ds_read_b128 v[28:31], v99 offset:28672\n"
"ds_write_b32 v109, v72 offset:2048\n"
"ds_write_b128 v108, v[48:51] offset:4096\n"
"s_waitcnt lgkmcnt(6)\n"
"s_barrier\n"
"v_mfma_f32_16x16x32_bf16 v[32:35], a[96:99], v[0:3], -4.0\n"
"v_exp_f32 v40, v40\n"
"v_mfma_f32_16x16x32_bf16 v[32:35], a[100:103], v[4:7], v[32:35]\n"
"v_exp_f32 v41, v41\n"
"v_mfma_f32_16x16x32_bf16 v[32:35], a[104:107], v[8:11], v[32:35]\n"
"v_exp_f32 v42, v42\n"
"v_mfma_f32_16x16x32_bf16 v[32:35], a[108:111], v[12:15], v[32:35]\n"
"v_exp_f32 v43, v43\n"
"v_mfma_f32_16x16x32_bf16 v[36:39], a[112:115], v[0:3], -4.0\n"
"v_exp_f32 v44, v44\n"
"v_mfma_f32_16x16x32_bf16 v[36:39], a[116:119], v[4:7], v[36:39]\n"
"v_exp_f32 v45, v45\n"
"v_mfma_f32_16x16x32_bf16 v[36:39], a[120:123], v[8:11], v[36:39]\n"
"v_exp_f32 v46, v46\n"
"v_mfma_f32_16x16x32_bf16 v[36:39], a[124:127], v[12:15], v[36:39]\n"
"v_exp_f32 v47, v47\n"
"v_mfma_f32_16x16x32_bf16 v[80:83], v[60:63], v[48:51], v[80:83]\n"
"v_cvt_pk_bf16_f32 v52, v40, v41\n"
"v_cvt_pk_bf16_f32 v53, v42, v43\n"
"v_cvt_pk_bf16_f32 v54, v44, v45\n"
"v_cvt_pk_bf16_f32 v55, v46, v47\n"
"ds_read_b128 v[0:3], v96 offset:32768\n"
"ds_read_b128 v[4:7], v97 offset:32768\n"
"ds_read_b128 v[8:11], v98 offset:32768\n"
"ds_read_b128 v[12:15], v99 offset:32768\n"
"s_xor_b32 s42, s62, 0\n"
"s_mov_b32 m0, s54\n"
"s_add_u32 s42, s42, s39\n"
"buffer_load_dwordx4 v104, s[24:27], s42 offen offset:0 nt lds\n"
"ds_write_b32 v109, v76 offset:3072\n"
"ds_write_b128 v108, v[52:55] offset:8192\n"
"s_waitcnt lgkmcnt(8)\n"
"v_mfma_f32_16x16x32_bf16 v[40:43], a[96:99], v[16:19], -4.0\n"
"v_exp_f32 v32, v32\n"
"v_mfma_f32_16x16x32_bf16 v[40:43], a[100:103], v[20:23], v[40:43]\n"
"v_exp_f32 v33, v33\n"
"v_mfma_f32_16x16x32_bf16 v[40:43], a[104:107], v[24:27], v[40:43]\n"
"v_exp_f32 v34, v34\n"
"v_mfma_f32_16x16x32_bf16 v[40:43], a[108:111], v[28:31], v[40:43]\n"
"v_exp_f32 v35, v35\n"
"v_mfma_f32_16x16x32_bf16 v[44:47], a[112:115], v[16:19], -4.0\n"
"v_exp_f32 v36, v36\n"
"v_mfma_f32_16x16x32_bf16 v[44:47], a[116:119], v[20:23], v[44:47]\n"
"v_exp_f32 v37, v37\n"
"v_mfma_f32_16x16x32_bf16 v[44:47], a[120:123], v[24:27], v[44:47]\n"
"v_exp_f32 v38, v38\n"
"v_mfma_f32_16x16x32_bf16 v[44:47], a[124:127], v[28:31], v[44:47]\n"
"v_exp_f32 v39, v39\n"
"v_mfma_f32_16x16x32_bf16 v[84:87], v[60:63], v[52:55], v[84:87]\n"
"v_cvt_pk_bf16_f32 v48, v32, v33\n"
"v_cvt_pk_bf16_f32 v49, v34, v35\n"
"v_cvt_pk_bf16_f32 v50, v36, v37\n"
"v_cvt_pk_bf16_f32 v51, v38, v39\n"
"ds_write_b32 v109, v80 offset:4096\n"
"ds_write_b128 v108, v[48:51] offset:12288\n"
"s_waitcnt lgkmcnt(4)\n"
"v_mfma_f32_16x16x32_bf16 v[32:35], a[0:3], v[0:3], -4.0\n"
"v_exp_f32 v40, v40\n"
"v_mfma_f32_16x16x32_bf16 v[32:35], a[4:7], v[4:7], v[32:35]\n"
"v_exp_f32 v41, v41\n"
"v_mfma_f32_16x16x32_bf16 v[32:35], a[8:11], v[8:11], v[32:35]\n"
"v_exp_f32 v42, v42\n"
"v_mfma_f32_16x16x32_bf16 v[32:35], a[12:15], v[12:15], v[32:35]\n"
"v_exp_f32 v43, v43\n"
"v_mfma_f32_16x16x32_bf16 v[36:39], a[16:19], v[0:3], -4.0\n"
"v_exp_f32 v44, v44\n"
"v_mfma_f32_16x16x32_bf16 v[36:39], a[20:23], v[4:7], v[36:39]\n"
"v_exp_f32 v45, v45\n"
"v_mfma_f32_16x16x32_bf16 v[36:39], a[24:27], v[8:11], v[36:39]\n"
"v_exp_f32 v46, v46\n"
"v_mfma_f32_16x16x32_bf16 v[36:39], a[28:31], v[12:15], v[36:39]\n"
"v_exp_f32 v47, v47\n"
"v_mfma_f32_16x16x32_bf16 v[88:91], v[60:63], v[48:51], v[88:91]\n"
"v_cvt_pk_bf16_f32 v52, v40, v41\n"
"v_cvt_pk_bf16_f32 v53, v42, v43\n"
"v_cvt_pk_bf16_f32 v54, v44, v45\n"
"v_cvt_pk_bf16_f32 v55, v46, v47\n"
"buffer_load_dwordx4 v105, s[24:27], s42 offen offset:1024 nt lds\n"
"ds_write_b32 v109, v84 offset:5120\n"
"ds_write_b128 v108, v[52:55] offset:16384\n"
"v_mfma_f32_16x16x32_bf16 v[40:43], a[32:35], v[0:3], -4.0\n"
"v_exp_f32 v32, v32\n"
"v_mfma_f32_16x16x32_bf16 v[40:43], a[36:39], v[4:7], v[40:43]\n"
"v_exp_f32 v33, v33\n"
"v_mfma_f32_16x16x32_bf16 v[40:43], a[40:43], v[8:11], v[40:43]\n"
"v_exp_f32 v34, v34\n"
"v_mfma_f32_16x16x32_bf16 v[40:43], a[44:47], v[12:15], v[40:43]\n"
"v_exp_f32 v35, v35\n"
"v_mfma_f32_16x16x32_bf16 v[44:47], a[48:51], v[0:3], -4.0\n"
"v_exp_f32 v36, v36\n"
"v_mfma_f32_16x16x32_bf16 v[44:47], a[52:55], v[4:7], v[44:47]\n"
"v_exp_f32 v37, v37\n"
"v_mfma_f32_16x16x32_bf16 v[44:47], a[56:59], v[8:11], v[44:47]\n"
"v_exp_f32 v38, v38\n"
"v_mfma_f32_16x16x32_bf16 v[44:47], a[60:63], v[12:15], v[44:47]\n"
"v_exp_f32 v39, v39\n"
"v_mfma_f32_16x16x32_bf16 v[92:95], v[60:63], v[52:55], v[92:95]\n"
"v_cvt_pk_bf16_f32 v48, v32, v33\n"
"v_cvt_pk_bf16_f32 v49, v34, v35\n"
"v_cvt_pk_bf16_f32 v50, v36, v37\n"
"v_cvt_pk_bf16_f32 v51, v38, v39\n"
"ds_write_b32 v109, v88 offset:6144\n"
"ds_write_b128 v108, v[48:51] offset:20480\n"
"s_waitcnt lgkmcnt(2)\n"
"s_barrier\n"
"v_mfma_f32_16x16x32_bf16 v[32:35], a[64:67], v[0:3], -4.0\n"
"v_exp_f32 v40, v40\n"
"v_mfma_f32_16x16x32_bf16 v[32:35], a[68:71], v[4:7], v[32:35]\n"
"v_exp_f32 v41, v41\n"
"v_mfma_f32_16x16x32_bf16 v[32:35], a[72:75], v[8:11], v[32:35]\n"
"v_exp_f32 v42, v42\n"
"v_mfma_f32_16x16x32_bf16 v[32:35], a[76:79], v[12:15], v[32:35]\n"
"v_exp_f32 v43, v43\n"
"v_mfma_f32_16x16x32_bf16 v[36:39], a[80:83], v[0:3], -4.0\n"
"v_exp_f32 v44, v44\n"
"v_mfma_f32_16x16x32_bf16 v[36:39], a[84:87], v[4:7], v[36:39]\n"
"v_exp_f32 v45, v45\n"
"v_mfma_f32_16x16x32_bf16 v[36:39], a[88:91], v[8:11], v[36:39]\n"
"v_exp_f32 v46, v46\n"
"v_mfma_f32_16x16x32_bf16 v[36:39], a[92:95], v[12:15], v[36:39]\n"
"v_exp_f32 v47, v47\n"
"v_mfma_f32_16x16x32_bf16 v[56:59], v[60:63], v[48:51], 0\n"
"v_cvt_pk_bf16_f32 v52, v40, v41\n"
"v_cvt_pk_bf16_f32 v53, v42, v43\n"
"v_cvt_pk_bf16_f32 v54, v44, v45\n"
"v_cvt_pk_bf16_f32 v55, v46, v47\n"
"ds_read_b128 v[16:19], v96 offset:36864\n"
"ds_read_b128 v[20:23], v97 offset:36864\n"
"buffer_load_dwordx4 v106, s[24:27], s42 offen offset:2048 nt lds\n"
"ds_write_b128 v108, v[52:55] offset:24576\n"
"v_mfma_f32_16x16x32_bf16 v[40:43], a[96:99], v[0:3], -4.0\n"
"v_exp_f32 v32, v32\n"
"v_mfma_f32_16x16x32_bf16 v[40:43], a[100:103], v[4:7], v[40:43]\n"
"v_exp_f32 v33, v33\n"
"v_mfma_f32_16x16x32_bf16 v[40:43], a[104:107], v[8:11], v[40:43]\n"
"v_exp_f32 v34, v34\n"
"v_mfma_f32_16x16x32_bf16 v[40:43], a[108:111], v[12:15], v[40:43]\n"
"v_exp_f32 v35, v35\n"
"v_mfma_f32_16x16x32_bf16 v[44:47], a[112:115], v[0:3], -4.0\n"
"v_exp_f32 v36, v36\n"
"v_mfma_f32_16x16x32_bf16 v[44:47], a[116:119], v[4:7], v[44:47]\n"
"v_exp_f32 v37, v37\n"
"v_mfma_f32_16x16x32_bf16 v[44:47], a[120:123], v[8:11], v[44:47]\n"
"v_exp_f32 v38, v38\n"
"v_mfma_f32_16x16x32_bf16 v[44:47], a[124:127], v[12:15], v[44:47]\n"
"v_exp_f32 v39, v39\n"
"v_mfma_f32_16x16x32_bf16 v[56:59], v[60:63], v[52:55], v[56:59]\n"
"v_cvt_pk_bf16_f32 v48, v32, v33\n"
"v_cvt_pk_bf16_f32 v49, v34, v35\n"
"v_cvt_pk_bf16_f32 v50, v36, v37\n"
"v_cvt_pk_bf16_f32 v51, v38, v39\n"
"ds_read_b128 v[24:27], v98 offset:36864\n"
"ds_read_b128 v[28:31], v99 offset:36864\n"
"ds_write_b128 v108, v[48:51] offset:28672\n"
"s_waitcnt lgkmcnt(1)\n"
"v_mfma_f32_16x16x32_bf16 v[32:35], a[0:3], v[16:19], -4.0\n"
"v_exp_f32 v40, v40\n"
"v_mfma_f32_16x16x32_bf16 v[32:35], a[4:7], v[20:23], v[32:35]\n"
"v_exp_f32 v41, v41\n"
"v_mfma_f32_16x16x32_bf16 v[32:35], a[8:11], v[24:27], v[32:35]\n"
"v_exp_f32 v42, v42\n"
"v_mfma_f32_16x16x32_bf16 v[32:35], a[12:15], v[28:31], v[32:35]\n"
"v_exp_f32 v43, v43\n"
"v_mfma_f32_16x16x32_bf16 v[36:39], a[16:19], v[16:19], -4.0\n"
"v_exp_f32 v44, v44\n"
"v_mfma_f32_16x16x32_bf16 v[36:39], a[20:23], v[20:23], v[36:39]\n"
"v_exp_f32 v45, v45\n"
"v_mfma_f32_16x16x32_bf16 v[36:39], a[24:27], v[24:27], v[36:39]\n"
"v_exp_f32 v46, v46\n"
"v_mfma_f32_16x16x32_bf16 v[36:39], a[28:31], v[28:31], v[36:39]\n"
"v_exp_f32 v47, v47\n"
"v_mfma_f32_16x16x32_bf16 v[56:59], v[60:63], v[48:51], v[56:59]\n"
"v_cvt_pk_bf16_f32 v52, v40, v41\n"
"v_cvt_pk_bf16_f32 v53, v42, v43\n"
"v_cvt_pk_bf16_f32 v54, v44, v45\n"
"v_cvt_pk_bf16_f32 v55, v46, v47\n"
"buffer_load_dwordx4 v107, s[24:27], s42 offen offset:3072 nt lds\n"
"ds_write_b32 v109, v92 offset:7168\n"
"ds_write_b128 v108, v[52:55] offset:32768\n"
"v_mfma_f32_16x16x32_bf16 v[40:43], a[32:35], v[16:19], -4.0\n"
"v_exp_f32 v32, v32\n"
"v_mfma_f32_16x16x32_bf16 v[40:43], a[36:39], v[20:23], v[40:43]\n"
"v_exp_f32 v33, v33\n"
"v_mfma_f32_16x16x32_bf16 v[40:43], a[40:43], v[24:27], v[40:43]\n"
"v_exp_f32 v34, v34\n"
"v_mfma_f32_16x16x32_bf16 v[40:43], a[44:47], v[28:31], v[40:43]\n"
"v_exp_f32 v35, v35\n"
"v_mfma_f32_16x16x32_bf16 v[44:47], a[48:51], v[16:19], -4.0\n"
"v_exp_f32 v36, v36\n"
"v_mfma_f32_16x16x32_bf16 v[44:47], a[52:55], v[20:23], v[44:47]\n"
"v_exp_f32 v37, v37\n"
"v_mfma_f32_16x16x32_bf16 v[44:47], a[56:59], v[24:27], v[44:47]\n"
"v_exp_f32 v38, v38\n"
"v_mfma_f32_16x16x32_bf16 v[44:47], a[60:63], v[28:31], v[44:47]\n"
"v_exp_f32 v39, v39\n"
"v_mfma_f32_16x16x32_bf16 v[56:59], v[60:63], v[52:55], v[56:59]\n"
"v_cvt_pk_bf16_f32 v48, v32, v33\n"
"v_cvt_pk_bf16_f32 v49, v34, v35\n"
"v_cvt_pk_bf16_f32 v50, v36, v37\n"
"v_cvt_pk_bf16_f32 v51, v38, v39\n"
"ds_write_b128 v108, v[48:51] offset:0\n"
"s_waitcnt lgkmcnt(1)\n"
"s_barrier\n"
"v_mfma_f32_16x16x32_bf16 v[32:35], a[64:67], v[16:19], -4.0\n"
"v_exp_f32 v40, v40\n"
"v_mfma_f32_16x16x32_bf16 v[32:35], a[68:71], v[20:23], v[32:35]\n"
"v_exp_f32 v41, v41\n"
"v_mfma_f32_16x16x32_bf16 v[32:35], a[72:75], v[24:27], v[32:35]\n"
"v_exp_f32 v42, v42\n"
"v_mfma_f32_16x16x32_bf16 v[32:35], a[76:79], v[28:31], v[32:35]\n"
"v_exp_f32 v43, v43\n"
"v_mfma_f32_16x16x32_bf16 v[36:39], a[80:83], v[16:19], -4.0\n"
"v_exp_f32 v44, v44\n"
"v_mfma_f32_16x16x32_bf16 v[36:39], a[84:87], v[20:23], v[36:39]\n"
"v_exp_f32 v45, v45\n"
"v_mfma_f32_16x16x32_bf16 v[36:39], a[88:91], v[24:27], v[36:39]\n"
"v_exp_f32 v46, v46\n"
"v_mfma_f32_16x16x32_bf16 v[36:39], a[92:95], v[28:31], v[36:39]\n"
"v_exp_f32 v47, v47\n"
"v_mfma_f32_16x16x32_bf16 v[116:119], v[60:63], v[48:51], 0\n"
"v_cvt_pk_bf16_f32 v52, v40, v41\n"
"v_cvt_pk_bf16_f32 v53, v42, v43\n"
"v_cvt_pk_bf16_f32 v54, v44, v45\n"
"v_cvt_pk_bf16_f32 v55, v46, v47\n"
"ds_read_b128 v[0:3], v96 offset:40960\n"
"ds_read_b128 v[4:7], v97 offset:40960\n"
"s_add_u32 s42, s42, s63\n"
"s_add_u32 m0, s54, 0x1000\n"
"s_nop 0\n"
"buffer_load_dwordx4 v104, s[24:27], s42 offen offset:0 nt lds\n"
"ds_write_b128 v108, v[52:55] offset:4096\n"
"v_mfma_f32_16x16x32_bf16 v[40:43], a[96:99], v[16:19], -4.0\n"
"v_exp_f32 v32, v32\n"
"v_mfma_f32_16x16x32_bf16 v[40:43], a[100:103], v[20:23], v[40:43]\n"
"v_exp_f32 v33, v33\n"
"v_mfma_f32_16x16x32_bf16 v[40:43], a[104:107], v[24:27], v[40:43]\n"
"v_exp_f32 v34, v34\n"
"v_mfma_f32_16x16x32_bf16 v[40:43], a[108:111], v[28:31], v[40:43]\n"
"v_exp_f32 v35, v35\n"
"v_mfma_f32_16x16x32_bf16 v[44:47], a[112:115], v[16:19], -4.0\n"
"v_exp_f32 v36, v36\n"
"v_mfma_f32_16x16x32_bf16 v[44:47], a[116:119], v[20:23], v[44:47]\n"
"v_exp_f32 v37, v37\n"
"v_mfma_f32_16x16x32_bf16 v[44:47], a[120:123], v[24:27], v[44:47]\n"
"v_exp_f32 v38, v38\n"
"v_mfma_f32_16x16x32_bf16 v[44:47], a[124:127], v[28:31], v[44:47]\n"
"v_exp_f32 v39, v39\n"
"v_mfma_f32_16x16x32_bf16 v[116:119], v[60:63], v[52:55], v[116:119]\n"
"v_cvt_pk_bf16_f32 v48, v32, v33\n"
"v_cvt_pk_bf16_f32 v49, v34, v35\n"
"v_cvt_pk_bf16_f32 v50, v36, v37\n"
"v_cvt_pk_bf16_f32 v51, v38, v39\n"
"ds_read_b128 v[8:11], v98 offset:40960\n"
"ds_read_b128 v[12:15], v99 offset:40960\n"
"ds_write_b128 v108, v[48:51] offset:8192\n"
"s_waitcnt lgkmcnt(1)\n"
"v_mfma_f32_16x16x32_bf16 v[32:35], a[0:3], v[0:3], -4.0\n"
"v_exp_f32 v40, v40\n"
"v_mfma_f32_16x16x32_bf16 v[32:35], a[4:7], v[4:7], v[32:35]\n"
"v_exp_f32 v41, v41\n"
"v_mfma_f32_16x16x32_bf16 v[32:35], a[8:11], v[8:11], v[32:35]\n"
"v_exp_f32 v42, v42\n"
"v_mfma_f32_16x16x32_bf16 v[32:35], a[12:15], v[12:15], v[32:35]\n"
"v_exp_f32 v43, v43\n"
"v_mfma_f32_16x16x32_bf16 v[36:39], a[16:19], v[0:3], -4.0\n"
"v_exp_f32 v44, v44\n"
"v_mfma_f32_16x16x32_bf16 v[36:39], a[20:23], v[4:7], v[36:39]\n"
"v_exp_f32 v45, v45\n"
"v_mfma_f32_16x16x32_bf16 v[36:39], a[24:27], v[8:11], v[36:39]\n"
"v_exp_f32 v46, v46\n"
"v_mfma_f32_16x16x32_bf16 v[36:39], a[28:31], v[12:15], v[36:39]\n"
"v_exp_f32 v47, v47\n"
"v_mfma_f32_16x16x32_bf16 v[116:119], v[60:63], v[48:51], v[116:119]\n"
"v_cvt_pk_bf16_f32 v52, v40, v41\n"
"v_cvt_pk_bf16_f32 v53, v42, v43\n"
"v_cvt_pk_bf16_f32 v54, v44, v45\n"
"v_cvt_pk_bf16_f32 v55, v46, v47\n"
"buffer_load_dwordx4 v105, s[24:27], s42 offen offset:1024 nt lds\n"
"ds_write_b32 v109, v56 offset:10240\n"
"ds_write_b128 v108, v[52:55] offset:12288\n"
"v_mfma_f32_16x16x32_bf16 v[40:43], a[32:35], v[0:3], -4.0\n"
"v_exp_f32 v32, v32\n"
"v_mfma_f32_16x16x32_bf16 v[40:43], a[36:39], v[4:7], v[40:43]\n"
"v_exp_f32 v33, v33\n"
"v_mfma_f32_16x16x32_bf16 v[40:43], a[40:43], v[8:11], v[40:43]\n"
"v_exp_f32 v34, v34\n"
"v_mfma_f32_16x16x32_bf16 v[40:43], a[44:47], v[12:15], v[40:43]\n"
"v_exp_f32 v35, v35\n"
"v_mfma_f32_16x16x32_bf16 v[44:47], a[48:51], v[0:3], -4.0\n"
"v_exp_f32 v36, v36\n"
"v_mfma_f32_16x16x32_bf16 v[44:47], a[52:55], v[4:7], v[44:47]\n"
"v_exp_f32 v37, v37\n"
"v_mfma_f32_16x16x32_bf16 v[44:47], a[56:59], v[8:11], v[44:47]\n"
"v_exp_f32 v38, v38\n"
"v_mfma_f32_16x16x32_bf16 v[44:47], a[60:63], v[12:15], v[44:47]\n"
"v_exp_f32 v39, v39\n"
"v_mfma_f32_16x16x32_bf16 v[116:119], v[60:63], v[52:55], v[116:119]\n"
"v_cvt_pk_bf16_f32 v48, v32, v33\n"
"v_cvt_pk_bf16_f32 v49, v34, v35\n"
"v_cvt_pk_bf16_f32 v50, v36, v37\n"
"v_cvt_pk_bf16_f32 v51, v38, v39\n"
"ds_write_b128 v108, v[48:51] offset:16384\n"
"s_waitcnt lgkmcnt(1)\n"
"s_barrier\n"
"v_mfma_f32_16x16x32_bf16 v[32:35], a[64:67], v[0:3], -4.0\n"
"v_exp_f32 v40, v40\n"
"v_mfma_f32_16x16x32_bf16 v[32:35], a[68:71], v[4:7], v[32:35]\n"
"v_exp_f32 v41, v41\n"
"v_mfma_f32_16x16x32_bf16 v[32:35], a[72:75], v[8:11], v[32:35]\n"
"v_exp_f32 v42, v42\n"
"v_mfma_f32_16x16x32_bf16 v[32:35], a[76:79], v[12:15], v[32:35]\n"
"v_exp_f32 v43, v43\n"
"v_mfma_f32_16x16x32_bf16 v[36:39], a[80:83], v[0:3], -4.0\n"
"v_exp_f32 v44, v44\n"
"v_mfma_f32_16x16x32_bf16 v[36:39], a[84:87], v[4:7], v[36:39]\n"
"v_exp_f32 v45, v45\n"
"v_mfma_f32_16x16x32_bf16 v[36:39], a[88:91], v[8:11], v[36:39]\n"
"v_exp_f32 v46, v46\n"
"v_mfma_f32_16x16x32_bf16 v[36:39], a[92:95], v[12:15], v[36:39]\n"
"v_exp_f32 v47, v47\n"
"v_mfma_f32_16x16x32_bf16 v[56:59], v[60:63], v[48:51], 0\n"
"v_cvt_pk_bf16_f32 v52, v40, v41\n"
"v_cvt_pk_bf16_f32 v53, v42, v43\n"
"v_cvt_pk_bf16_f32 v54, v44, v45\n"
"v_cvt_pk_bf16_f32 v55, v46, v47\n"
"ds_read_b128 v[16:19], v96 offset:45056\n"
"ds_read_b128 v[20:23], v97 offset:45056\n"
"buffer_load_dwordx4 v106, s[24:27], s42 offen offset:2048 nt lds\n"
"ds_write_b128 v108, v[52:55] offset:20480\n"
"v_mfma_f32_16x16x32_bf16 v[40:43], a[96:99], v[0:3], -4.0\n"
"v_exp_f32 v32, v32\n"
"v_mfma_f32_16x16x32_bf16 v[40:43], a[100:103], v[4:7], v[40:43]\n"
"v_exp_f32 v33, v33\n"
"v_mfma_f32_16x16x32_bf16 v[40:43], a[104:107], v[8:11], v[40:43]\n"
"v_exp_f32 v34, v34\n"
"v_mfma_f32_16x16x32_bf16 v[40:43], a[108:111], v[12:15], v[40:43]\n"
"v_exp_f32 v35, v35\n"
"v_mfma_f32_16x16x32_bf16 v[44:47], a[112:115], v[0:3], -4.0\n"
"v_exp_f32 v36, v36\n"
"v_mfma_f32_16x16x32_bf16 v[44:47], a[116:119], v[4:7], v[44:47]\n"
"v_exp_f32 v37, v37\n"
"v_mfma_f32_16x16x32_bf16 v[44:47], a[120:123], v[8:11], v[44:47]\n"
"v_exp_f32 v38, v38\n"
"v_mfma_f32_16x16x32_bf16 v[44:47], a[124:127], v[12:15], v[44:47]\n"
"v_exp_f32 v39, v39\n"
"v_mfma_f32_16x16x32_bf16 v[56:59], v[60:63], v[52:55], v[56:59]\n"
"v_cvt_pk_bf16_f32 v48, v32, v33\n"
"v_cvt_pk_bf16_f32 v49, v34, v35\n"
"v_cvt_pk_bf16_f32 v50, v36, v37\n"
"v_cvt_pk_bf16_f32 v51, v38, v39\n"
"ds_read_b128 v[24:27], v98 offset:45056\n"
"ds_read_b128 v[28:31], v99 offset:45056\n"
"ds_write_b128 v108, v[48:51] offset:24576\n"
"s_waitcnt lgkmcnt(1)\n"
"v_mfma_f32_16x16x32_bf16 v[32:35], a[0:3], v[16:19], -4.0\n"
"v_exp_f32 v40, v40\n"
"v_mfma_f32_16x16x32_bf16 v[32:35], a[4:7], v[20:23], v[32:35]\n"
"v_exp_f32 v41, v41\n"
"v_mfma_f32_16x16x32_bf16 v[32:35], a[8:11], v[24:27], v[32:35]\n"
"v_exp_f32 v42, v42\n"
"v_mfma_f32_16x16x32_bf16 v[32:35], a[12:15], v[28:31], v[32:35]\n"
"v_exp_f32 v43, v43\n"
"v_mfma_f32_16x16x32_bf16 v[36:39], a[16:19], v[16:19], -4.0\n"
"v_exp_f32 v44, v44\n"
"v_mfma_f32_16x16x32_bf16 v[36:39], a[20:23], v[20:23], v[36:39]\n"
"v_exp_f32 v45, v45\n"
"v_mfma_f32_16x16x32_bf16 v[36:39], a[24:27], v[24:27], v[36:39]\n"
"v_exp_f32 v46, v46\n"
"v_mfma_f32_16x16x32_bf16 v[36:39], a[28:31], v[28:31], v[36:39]\n"
"v_exp_f32 v47, v47\n"
"v_mfma_f32_16x16x32_bf16 v[56:59], v[60:63], v[48:51], v[56:59]\n"
"v_cvt_pk_bf16_f32 v52, v40, v41\n"
"v_cvt_pk_bf16_f32 v53, v42, v43\n"
"v_cvt_pk_bf16_f32 v54, v44, v45\n"
"v_cvt_pk_bf16_f32 v55, v46, v47\n"
"buffer_load_dwordx4 v107, s[24:27], s42 offen offset:3072 nt lds\n"
"ds_write_b32 v109, v116 offset:8192\n"
"ds_write_b128 v108, v[52:55] offset:28672\n"
"v_mfma_f32_16x16x32_bf16 v[40:43], a[32:35], v[16:19], -4.0\n"
"v_exp_f32 v32, v32\n"
"v_mfma_f32_16x16x32_bf16 v[40:43], a[36:39], v[20:23], v[40:43]\n"
"v_exp_f32 v33, v33\n"
"v_mfma_f32_16x16x32_bf16 v[40:43], a[40:43], v[24:27], v[40:43]\n"
"v_exp_f32 v34, v34\n"
"v_mfma_f32_16x16x32_bf16 v[40:43], a[44:47], v[28:31], v[40:43]\n"
"v_exp_f32 v35, v35\n"
"v_mfma_f32_16x16x32_bf16 v[44:47], a[48:51], v[16:19], -4.0\n"
"v_exp_f32 v36, v36\n"
"v_mfma_f32_16x16x32_bf16 v[44:47], a[52:55], v[20:23], v[44:47]\n"
"v_exp_f32 v37, v37\n"
"v_mfma_f32_16x16x32_bf16 v[44:47], a[56:59], v[24:27], v[44:47]\n"
"v_exp_f32 v38, v38\n"
"v_mfma_f32_16x16x32_bf16 v[44:47], a[60:63], v[28:31], v[44:47]\n"
"v_exp_f32 v39, v39\n"
"v_mfma_f32_16x16x32_bf16 v[56:59], v[60:63], v[52:55], v[56:59]\n"
"v_cvt_pk_bf16_f32 v48, v32, v33\n"
"v_cvt_pk_bf16_f32 v49, v34, v35\n"
"v_cvt_pk_bf16_f32 v50, v36, v37\n"
"v_cvt_pk_bf16_f32 v51, v38, v39\n"
"ds_write_b128 v108, v[48:51] offset:32768\n"
"s_waitcnt lgkmcnt(1)\n"
"s_barrier\n"
"v_mfma_f32_16x16x32_bf16 v[32:35], a[64:67], v[16:19], -4.0\n"
"v_exp_f32 v40, v40\n"
"v_mfma_f32_16x16x32_bf16 v[32:35], a[68:71], v[20:23], v[32:35]\n"
"v_exp_f32 v41, v41\n"
"v_mfma_f32_16x16x32_bf16 v[32:35], a[72:75], v[24:27], v[32:35]\n"
"v_exp_f32 v42, v42\n"
"v_mfma_f32_16x16x32_bf16 v[32:35], a[76:79], v[28:31], v[32:35]\n"
"v_exp_f32 v43, v43\n"
"v_mfma_f32_16x16x32_bf16 v[36:39], a[80:83], v[16:19], -4.0\n"
"v_exp_f32 v44, v44\n"
"v_mfma_f32_16x16x32_bf16 v[36:39], a[84:87], v[20:23], v[36:39]\n"
"v_exp_f32 v45, v45\n"
"v_mfma_f32_16x16x32_bf16 v[36:39], a[88:91], v[24:27], v[36:39]\n"
"v_exp_f32 v46, v46\n"
"v_mfma_f32_16x16x32_bf16 v[36:39], a[92:95], v[28:31], v[36:39]\n"
"v_exp_f32 v47, v47\n"
"v_mfma_f32_16x16x32_bf16 v[116:119], v[60:63], v[48:51], 0\n"
"v_cvt_pk_bf16_f32 v52, v40, v41\n"
"v_cvt_pk_bf16_f32 v53, v42, v43\n"
"v_cvt_pk_bf16_f32 v54, v44, v45\n"
"v_cvt_pk_bf16_f32 v55, v46, v47\n"
"ds_read_b128 v[0:3], v96 offset:32768\n"
"ds_read_b128 v[4:7], v97 offset:32768\n"
"ds_write_b128 v108, v[52:55] offset:0\n"
"v_mfma_f32_16x16x32_bf16 v[40:43], a[96:99], v[16:19], -4.0\n"
"v_exp_f32 v32, v32\n"
"v_mfma_f32_16x16x32_bf16 v[40:43], a[100:103], v[20:23], v[40:43]\n"
"v_exp_f32 v33, v33\n"
"v_mfma_f32_16x16x32_bf16 v[40:43], a[104:107], v[24:27], v[40:43]\n"
"v_exp_f32 v34, v34\n"
"v_mfma_f32_16x16x32_bf16 v[40:43], a[108:111], v[28:31], v[40:43]\n"
"v_exp_f32 v35, v35\n"
"v_mfma_f32_16x16x32_bf16 v[44:47], a[112:115], v[16:19], -4.0\n"
"v_exp_f32 v36, v36\n"
"v_mfma_f32_16x16x32_bf16 v[44:47], a[116:119], v[20:23], v[44:47]\n"
"v_exp_f32 v37, v37\n"
"v_mfma_f32_16x16x32_bf16 v[44:47], a[120:123], v[24:27], v[44:47]\n"
"v_exp_f32 v38, v38\n"
"v_mfma_f32_16x16x32_bf16 v[44:47], a[124:127], v[28:31], v[44:47]\n"
"v_exp_f32 v39, v39\n"
"v_mfma_f32_16x16x32_bf16 v[116:119], v[60:63], v[52:55], v[116:119]\n"
"v_cvt_pk_bf16_f32 v48, v32, v33\n"
"v_cvt_pk_bf16_f32 v49, v34, v35\n"
"v_cvt_pk_bf16_f32 v50, v36, v37\n"
"v_cvt_pk_bf16_f32 v51, v38, v39\n"
"ds_read_b128 v[8:11], v98 offset:32768\n"
"ds_read_b128 v[12:15], v99 offset:32768\n"
"ds_write_b128 v108, v[48:51] offset:4096\n"
"s_waitcnt lgkmcnt(1)\n"
"v_mfma_f32_16x16x32_bf16 v[32:35], a[0:3], v[0:3], -4.0\n"
"v_exp_f32 v40, v40\n"
"v_mfma_f32_16x16x32_bf16 v[32:35], a[4:7], v[4:7], v[32:35]\n"
"v_exp_f32 v41, v41\n"
"v_mfma_f32_16x16x32_bf16 v[32:35], a[8:11], v[8:11], v[32:35]\n"
"v_exp_f32 v42, v42\n"
"v_mfma_f32_16x16x32_bf16 v[32:35], a[12:15], v[12:15], v[32:35]\n"
"v_exp_f32 v43, v43\n"
"v_mfma_f32_16x16x32_bf16 v[36:39], a[16:19], v[0:3], -4.0\n"
"v_exp_f32 v44, v44\n"
"v_mfma_f32_16x16x32_bf16 v[36:39], a[20:23], v[4:7], v[36:39]\n"
"v_exp_f32 v45, v45\n"
"v_mfma_f32_16x16x32_bf16 v[36:39], a[24:27], v[8:11], v[36:39]\n"
"v_exp_f32 v46, v46\n"
"v_mfma_f32_16x16x32_bf16 v[36:39], a[28:31], v[12:15], v[36:39]\n"
"v_exp_f32 v47, v47\n"
"v_mfma_f32_16x16x32_bf16 v[116:119], v[60:63], v[48:51], v[116:119]\n"
"v_cvt_pk_bf16_f32 v52, v40, v41\n"
"v_cvt_pk_bf16_f32 v53, v42, v43\n"
"v_cvt_pk_bf16_f32 v54, v44, v45\n"
"v_cvt_pk_bf16_f32 v55, v46, v47\n"
"ds_write_b32 v109, v56 offset:9216\n"
"ds_write_b128 v108, v[52:55] offset:8192\n"
"v_mfma_f32_16x16x32_bf16 v[40:43], a[32:35], v[0:3], -4.0\n"
"v_exp_f32 v32, v32\n"
"v_mfma_f32_16x16x32_bf16 v[40:43], a[36:39], v[4:7], v[40:43]\n"
"v_exp_f32 v33, v33\n"
"v_mfma_f32_16x16x32_bf16 v[40:43], a[40:43], v[8:11], v[40:43]\n"
"v_exp_f32 v34, v34\n"
"v_mfma_f32_16x16x32_bf16 v[40:43], a[44:47], v[12:15], v[40:43]\n"
"v_exp_f32 v35, v35\n"
"v_mfma_f32_16x16x32_bf16 v[44:47], a[48:51], v[0:3], -4.0\n"
"v_exp_f32 v36, v36\n"
"v_mfma_f32_16x16x32_bf16 v[44:47], a[52:55], v[4:7], v[44:47]\n"
"v_exp_f32 v37, v37\n"
"v_mfma_f32_16x16x32_bf16 v[44:47], a[56:59], v[8:11], v[44:47]\n"
"v_exp_f32 v38, v38\n"
"v_mfma_f32_16x16x32_bf16 v[44:47], a[60:63], v[12:15], v[44:47]\n"
"v_exp_f32 v39, v39\n"
"v_mfma_f32_16x16x32_bf16 v[116:119], v[60:63], v[52:55], v[116:119]\n"
"v_cvt_pk_bf16_f32 v48, v32, v33\n"
"v_cvt_pk_bf16_f32 v49, v34, v35\n"
"v_cvt_pk_bf16_f32 v50, v36, v37\n"
"v_cvt_pk_bf16_f32 v51, v38, v39\n"
"ds_write_b128 v108, v[48:51] offset:12288\n"
"s_waitcnt lgkmcnt(1)\n"
"s_barrier\n"
"v_mfma_f32_16x16x32_bf16 v[32:35], a[64:67], v[0:3], -4.0\n"
"v_exp_f32 v40, v40\n"
"v_mfma_f32_16x16x32_bf16 v[32:35], a[68:71], v[4:7], v[32:35]\n"
"v_exp_f32 v41, v41\n"
"v_mfma_f32_16x16x32_bf16 v[32:35], a[72:75], v[8:11], v[32:35]\n"
"v_exp_f32 v42, v42\n"
"v_mfma_f32_16x16x32_bf16 v[32:35], a[76:79], v[12:15], v[32:35]\n"
"v_exp_f32 v43, v43\n"
"v_mfma_f32_16x16x32_bf16 v[36:39], a[80:83], v[0:3], -4.0\n"
"v_exp_f32 v44, v44\n"
"v_mfma_f32_16x16x32_bf16 v[36:39], a[84:87], v[4:7], v[36:39]\n"
"v_exp_f32 v45, v45\n"
"v_mfma_f32_16x16x32_bf16 v[36:39], a[88:91], v[8:11], v[36:39]\n"
"v_exp_f32 v46, v46\n"
"v_mfma_f32_16x16x32_bf16 v[36:39], a[92:95], v[12:15], v[36:39]\n"
"v_exp_f32 v47, v47\n"
"v_mfma_f32_16x16x32_bf16 v[56:59], v[60:63], v[48:51], 0\n"
"v_cvt_pk_bf16_f32 v52, v40, v41\n"
"v_cvt_pk_bf16_f32 v53, v42, v43\n"
"v_cvt_pk_bf16_f32 v54, v44, v45\n"
"v_cvt_pk_bf16_f32 v55, v46, v47\n"
"ds_read_b128 v[16:19], v96 offset:36864\n"
"ds_read_b128 v[20:23], v97 offset:36864\n"
"ds_write_b128 v108, v[52:55] offset:16384\n"
"v_mfma_f32_16x16x32_bf16 v[40:43], a[96:99], v[0:3], -4.0\n"
"v_exp_f32 v32, v32\n"
"v_mfma_f32_16x16x32_bf16 v[40:43], a[100:103], v[4:7], v[40:43]\n"
"v_exp_f32 v33, v33\n"
"v_mfma_f32_16x16x32_bf16 v[40:43], a[104:107], v[8:11], v[40:43]\n"
"v_exp_f32 v34, v34\n"
"v_mfma_f32_16x16x32_bf16 v[40:43], a[108:111], v[12:15], v[40:43]\n"
"v_exp_f32 v35, v35\n"
"v_mfma_f32_16x16x32_bf16 v[44:47], a[112:115], v[0:3], -4.0\n"
"v_exp_f32 v36, v36\n"
"v_mfma_f32_16x16x32_bf16 v[44:47], a[116:119], v[4:7], v[44:47]\n"
"v_exp_f32 v37, v37\n"
"v_mfma_f32_16x16x32_bf16 v[44:47], a[120:123], v[8:11], v[44:47]\n"
"v_exp_f32 v38, v38\n"
"v_mfma_f32_16x16x32_bf16 v[44:47], a[124:127], v[12:15], v[44:47]\n"
"v_exp_f32 v39, v39\n"
"v_mfma_f32_16x16x32_bf16 v[56:59], v[60:63], v[52:55], v[56:59]\n"
"v_cvt_pk_bf16_f32 v48, v32, v33\n"
"v_cvt_pk_bf16_f32 v49, v34, v35\n"
"v_cvt_pk_bf16_f32 v50, v36, v37\n"
"v_cvt_pk_bf16_f32 v51, v38, v39\n"
"ds_read_b128 v[24:27], v98 offset:36864\n"
"ds_read_b128 v[28:31], v99 offset:36864\n"
"ds_write_b128 v108, v[48:51] offset:20480\n"
"s_waitcnt lgkmcnt(1)\n"
"v_mfma_f32_16x16x32_bf16 v[32:35], a[0:3], v[16:19], -4.0\n"
"v_exp_f32 v40, v40\n"
"v_mfma_f32_16x16x32_bf16 v[32:35], a[4:7], v[20:23], v[32:35]\n"
"v_exp_f32 v41, v41\n"
"v_mfma_f32_16x16x32_bf16 v[32:35], a[8:11], v[24:27], v[32:35]\n"
"v_exp_f32 v42, v42\n"
"v_mfma_f32_16x16x32_bf16 v[32:35], a[12:15], v[28:31], v[32:35]\n"
"v_exp_f32 v43, v43\n"
"v_mfma_f32_16x16x32_bf16 v[36:39], a[16:19], v[16:19], -4.0\n"
"v_exp_f32 v44, v44\n"
"v_mfma_f32_16x16x32_bf16 v[36:39], a[20:23], v[20:23], v[36:39]\n"
"v_exp_f32 v45, v45\n"
"v_mfma_f32_16x16x32_bf16 v[36:39], a[24:27], v[24:27], v[36:39]\n"
"v_exp_f32 v46, v46\n"
"v_mfma_f32_16x16x32_bf16 v[36:39], a[28:31], v[28:31], v[36:39]\n"
"v_exp_f32 v47, v47\n"
"v_mfma_f32_16x16x32_bf16 v[56:59], v[60:63], v[48:51], v[56:59]\n"
"v_cvt_pk_bf16_f32 v52, v40, v41\n"
"v_cvt_pk_bf16_f32 v53, v42, v43\n"
"v_cvt_pk_bf16_f32 v54, v44, v45\n"
"v_cvt_pk_bf16_f32 v55, v46, v47\n"
"s_waitcnt vmcnt(4)\n"
"ds_read_b128 v[64:67], v100 offset:0\n"
"ds_read_b128 v[68:71], v101 offset:0\n"
"ds_read_b128 v[72:75], v102 offset:0\n"
"ds_read_b128 v[76:79], v103 offset:0\n"
"ds_write_b32 v109, v116 offset:10240\n"
"ds_write_b128 v108, v[52:55] offset:24576\n"
"v_mfma_f32_16x16x32_bf16 v[40:43], a[32:35], v[16:19], -4.0\n"
"v_exp_f32 v32, v32\n"
"v_mfma_f32_16x16x32_bf16 v[40:43], a[36:39], v[20:23], v[40:43]\n"
"v_exp_f32 v33, v33\n"
"v_mfma_f32_16x16x32_bf16 v[40:43], a[40:43], v[24:27], v[40:43]\n"
"v_exp_f32 v34, v34\n"
"v_mfma_f32_16x16x32_bf16 v[40:43], a[44:47], v[28:31], v[40:43]\n"
"v_exp_f32 v35, v35\n"
"v_mfma_f32_16x16x32_bf16 v[44:47], a[48:51], v[16:19], -4.0\n"
"v_exp_f32 v36, v36\n"
"v_mfma_f32_16x16x32_bf16 v[44:47], a[52:55], v[20:23], v[44:47]\n"
"v_exp_f32 v37, v37\n"
"v_mfma_f32_16x16x32_bf16 v[44:47], a[56:59], v[24:27], v[44:47]\n"
"v_exp_f32 v38, v38\n"
"v_mfma_f32_16x16x32_bf16 v[44:47], a[60:63], v[28:31], v[44:47]\n"
"v_exp_f32 v39, v39\n"
"v_mfma_f32_16x16x32_bf16 v[56:59], v[60:63], v[52:55], v[56:59]\n"
"v_cvt_pk_bf16_f32 v48, v32, v33\n"
"v_cvt_pk_bf16_f32 v49, v34, v35\n"
"v_cvt_pk_bf16_f32 v50, v36, v37\n"
"v_cvt_pk_bf16_f32 v51, v38, v39\n"
"ds_write_b128 v108, v[48:51] offset:28672\n"
"s_waitcnt lgkmcnt(1)\n"
"s_barrier\n"
"v_mfma_f32_16x16x32_bf16 v[32:35], a[64:67], v[16:19], -4.0\n"
"v_exp_f32 v40, v40\n"
"v_mfma_f32_16x16x32_bf16 v[32:35], a[68:71], v[20:23], v[32:35]\n"
"v_exp_f32 v41, v41\n"
"v_mfma_f32_16x16x32_bf16 v[32:35], a[72:75], v[24:27], v[32:35]\n"
"v_exp_f32 v42, v42\n"
"v_mfma_f32_16x16x32_bf16 v[32:35], a[76:79], v[28:31], v[32:35]\n"
"v_exp_f32 v43, v43\n"
"v_mfma_f32_16x16x32_bf16 v[36:39], a[80:83], v[16:19], -4.0\n"
"v_exp_f32 v44, v44\n"
"v_mfma_f32_16x16x32_bf16 v[36:39], a[84:87], v[20:23], v[36:39]\n"
"v_exp_f32 v45, v45\n"
"v_mfma_f32_16x16x32_bf16 v[36:39], a[88:91], v[24:27], v[36:39]\n"
"v_exp_f32 v46, v46\n"
"v_mfma_f32_16x16x32_bf16 v[36:39], a[92:95], v[28:31], v[36:39]\n"
"v_exp_f32 v47, v47\n"
"v_mfma_f32_16x16x32_bf16 v[116:119], v[60:63], v[48:51], 0\n"
"v_cvt_pk_bf16_f32 v52, v40, v41\n"
"v_cvt_pk_bf16_f32 v53, v42, v43\n"
"v_cvt_pk_bf16_f32 v54, v44, v45\n"
"v_cvt_pk_bf16_f32 v55, v46, v47\n"
"ds_read_b128 v[0:3], v96 offset:40960\n"
"ds_read_b128 v[4:7], v97 offset:40960\n"
"s_waitcnt vmcnt(0)\n"
"ds_read_b128 v[80:83], v100 offset:4096\n"
"ds_read_b128 v[84:87], v101 offset:4096\n"
"ds_read_b128 v[88:91], v102 offset:4096\n"
"ds_read_b128 v[92:95], v103 offset:4096\n"
"ds_write_b128 v108, v[52:55] offset:32768\n"
"v_mfma_f32_16x16x32_bf16 v[40:43], a[96:99], v[16:19], -4.0\n"
"v_exp_f32 v32, v32\n"
"v_mfma_f32_16x16x32_bf16 v[40:43], a[100:103], v[20:23], v[40:43]\n"
"v_exp_f32 v33, v33\n"
"v_mfma_f32_16x16x32_bf16 v[40:43], a[104:107], v[24:27], v[40:43]\n"
"v_exp_f32 v34, v34\n"
"v_mfma_f32_16x16x32_bf16 v[40:43], a[108:111], v[28:31], v[40:43]\n"
"v_exp_f32 v35, v35\n"
"v_mfma_f32_16x16x32_bf16 v[44:47], a[112:115], v[16:19], -4.0\n"
"v_exp_f32 v36, v36\n"
"v_mfma_f32_16x16x32_bf16 v[44:47], a[116:119], v[20:23], v[44:47]\n"
"v_exp_f32 v37, v37\n"
"v_mfma_f32_16x16x32_bf16 v[44:47], a[120:123], v[24:27], v[44:47]\n"
"v_exp_f32 v38, v38\n"
"v_mfma_f32_16x16x32_bf16 v[44:47], a[124:127], v[28:31], v[44:47]\n"
"v_exp_f32 v39, v39\n"
"v_mfma_f32_16x16x32_bf16 v[116:119], v[60:63], v[52:55], v[116:119]\n"
"v_cvt_pk_bf16_f32 v48, v32, v33\n"
"v_cvt_pk_bf16_f32 v49, v34, v35\n"
"v_cvt_pk_bf16_f32 v50, v36, v37\n"
"v_cvt_pk_bf16_f32 v51, v38, v39\n"
"ds_read_b128 v[8:11], v98 offset:40960\n"
"ds_read_b128 v[12:15], v99 offset:40960\n"
"ds_write_b128 v108, v[48:51] offset:0\n"
"s_waitcnt lgkmcnt(1)\n"
"v_mfma_f32_16x16x32_bf16 v[32:35], a[0:3], v[0:3], -4.0\n"
"v_exp_f32 v40, v40\n"
"v_mfma_f32_16x16x32_bf16 v[32:35], a[4:7], v[4:7], v[32:35]\n"
"v_exp_f32 v41, v41\n"
"v_mfma_f32_16x16x32_bf16 v[32:35], a[8:11], v[8:11], v[32:35]\n"
"v_exp_f32 v42, v42\n"
"v_mfma_f32_16x16x32_bf16 v[32:35], a[12:15], v[12:15], v[32:35]\n"
"v_exp_f32 v43, v43\n"
"v_mfma_f32_16x16x32_bf16 v[36:39], a[16:19], v[0:3], -4.0\n"
"v_exp_f32 v44, v44\n"
"v_mfma_f32_16x16x32_bf16 v[36:39], a[20:23], v[4:7], v[36:39]\n"
"v_exp_f32 v45, v45\n"
"v_mfma_f32_16x16x32_bf16 v[36:39], a[24:27], v[8:11], v[36:39]\n"
"v_exp_f32 v46, v46\n"
"v_mfma_f32_16x16x32_bf16 v[36:39], a[28:31], v[12:15], v[36:39]\n"
"v_exp_f32 v47, v47\n"
"v_mfma_f32_16x16x32_bf16 v[116:119], v[60:63], v[48:51], v[116:119]\n"
"v_cvt_pk_bf16_f32 v52, v40, v41\n"
"v_cvt_pk_bf16_f32 v53, v42, v43\n"
"v_cvt_pk_bf16_f32 v54, v44, v45\n"
"v_cvt_pk_bf16_f32 v55, v46, v47\n"
"s_xor_b32 s42, s62, 0x8000\n"
"s_mov_b32 m0, s54\n"
"s_add_u32 s42, s42, s39\n"
"buffer_load_dwordx4 v104, s[24:27], s42 offen offset:0 nt lds\n"
"ds_write_b32 v109, v56 offset:8192\n"
"ds_write_b128 v108, v[52:55] offset:4096\n"
"v_mfma_f32_16x16x32_bf16 v[40:43], a[32:35], v[0:3], -4.0\n"
"v_exp_f32 v32, v32\n"
"v_mfma_f32_16x16x32_bf16 v[40:43], a[36:39], v[4:7], v[40:43]\n"
"v_exp_f32 v33, v33\n"
"v_mfma_f32_16x16x32_bf16 v[40:43], a[40:43], v[8:11], v[40:43]\n"
"v_exp_f32 v34, v34\n"
"v_mfma_f32_16x16x32_bf16 v[40:43], a[44:47], v[12:15], v[40:43]\n"
"v_exp_f32 v35, v35\n"
"v_mfma_f32_16x16x32_bf16 v[44:47], a[48:51], v[0:3], -4.0\n"
"v_exp_f32 v36, v36\n"
"v_mfma_f32_16x16x32_bf16 v[44:47], a[52:55], v[4:7], v[44:47]\n"
"v_exp_f32 v37, v37\n"
"v_mfma_f32_16x16x32_bf16 v[44:47], a[56:59], v[8:11], v[44:47]\n"
"v_exp_f32 v38, v38\n"
"v_mfma_f32_16x16x32_bf16 v[44:47], a[60:63], v[12:15], v[44:47]\n"
"v_exp_f32 v39, v39\n"
"v_mfma_f32_16x16x32_bf16 v[116:119], v[60:63], v[52:55], v[116:119]\n"
"v_cvt_pk_bf16_f32 v48, v32, v33\n"
"v_cvt_pk_bf16_f32 v49, v34, v35\n"
"v_cvt_pk_bf16_f32 v50, v36, v37\n"
"v_cvt_pk_bf16_f32 v51, v38, v39\n"
"ds_write_b128 v108, v[48:51] offset:8192\n"
"s_waitcnt lgkmcnt(1)\n"
"s_barrier\n"
"v_mfma_f32_16x16x32_bf16 v[32:35], a[64:67], v[0:3], -4.0\n"
"v_exp_f32 v40, v40\n"
"v_mfma_f32_16x16x32_bf16 v[32:35], a[68:71], v[4:7], v[32:35]\n"
"v_exp_f32 v41, v41\n"
"v_mfma_f32_16x16x32_bf16 v[32:35], a[72:75], v[8:11], v[32:35]\n"
"v_exp_f32 v42, v42\n"
"v_mfma_f32_16x16x32_bf16 v[32:35], a[76:79], v[12:15], v[32:35]\n"
"v_exp_f32 v43, v43\n"
"v_mfma_f32_16x16x32_bf16 v[36:39], a[80:83], v[0:3], -4.0\n"
"v_exp_f32 v44, v44\n"
"v_mfma_f32_16x16x32_bf16 v[36:39], a[84:87], v[4:7], v[36:39]\n"
"v_exp_f32 v45, v45\n"
"v_mfma_f32_16x16x32_bf16 v[36:39], a[88:91], v[8:11], v[36:39]\n"
"v_exp_f32 v46, v46\n"
"v_mfma_f32_16x16x32_bf16 v[36:39], a[92:95], v[12:15], v[36:39]\n"
"v_exp_f32 v47, v47\n"
"v_mfma_f32_16x16x32_bf16 v[56:59], v[60:63], v[48:51], 0\n"
"v_cvt_pk_bf16_f32 v52, v40, v41\n"
"v_cvt_pk_bf16_f32 v53, v42, v43\n"
"v_cvt_pk_bf16_f32 v54, v44, v45\n"
"v_cvt_pk_bf16_f32 v55, v46, v47\n"
"ds_read_b128 v[16:19], v96 offset:45056\n"
"ds_read_b128 v[20:23], v97 offset:45056\n"
"ds_write_b128 v108, v[52:55] offset:12288\n"
"v_mfma_f32_16x16x32_bf16 v[40:43], a[96:99], v[0:3], -4.0\n"
"v_exp_f32 v32, v32\n"
"v_mfma_f32_16x16x32_bf16 v[40:43], a[100:103], v[4:7], v[40:43]\n"
"v_exp_f32 v33, v33\n"
"v_mfma_f32_16x16x32_bf16 v[40:43], a[104:107], v[8:11], v[40:43]\n"
"v_exp_f32 v34, v34\n"
"v_mfma_f32_16x16x32_bf16 v[40:43], a[108:111], v[12:15], v[40:43]\n"
"v_exp_f32 v35, v35\n"
"v_mfma_f32_16x16x32_bf16 v[44:47], a[112:115], v[0:3], -4.0\n"
"v_exp_f32 v36, v36\n"
"v_mfma_f32_16x16x32_bf16 v[44:47], a[116:119], v[4:7], v[44:47]\n"
"v_exp_f32 v37, v37\n"
"v_mfma_f32_16x16x32_bf16 v[44:47], a[120:123], v[8:11], v[44:47]\n"
"v_exp_f32 v38, v38\n"
"v_mfma_f32_16x16x32_bf16 v[44:47], a[124:127], v[12:15], v[44:47]\n"
"v_exp_f32 v39, v39\n"
"v_mfma_f32_16x16x32_bf16 v[56:59], v[60:63], v[52:55], v[56:59]\n"
"v_cvt_pk_bf16_f32 v48, v32, v33\n"
"v_cvt_pk_bf16_f32 v49, v34, v35\n"
"v_cvt_pk_bf16_f32 v50, v36, v37\n"
"v_cvt_pk_bf16_f32 v51, v38, v39\n"
"ds_read_b128 v[24:27], v98 offset:45056\n"
"ds_read_b128 v[28:31], v99 offset:45056\n"
"buffer_load_dwordx4 v105, s[24:27], s42 offen offset:1024 nt lds\n"
"ds_write_b128 v108, v[48:51] offset:16384\n"
"s_waitcnt lgkmcnt(1)\n"
"v_mfma_f32_16x16x32_bf16 v[32:35], a[0:3], v[16:19], -4.0\n"
"v_exp_f32 v40, v40\n"
"v_mfma_f32_16x16x32_bf16 v[32:35], a[4:7], v[20:23], v[32:35]\n"
"v_exp_f32 v41, v41\n"
"v_mfma_f32_16x16x32_bf16 v[32:35], a[8:11], v[24:27], v[32:35]\n"
"v_exp_f32 v42, v42\n"
"v_mfma_f32_16x16x32_bf16 v[32:35], a[12:15], v[28:31], v[32:35]\n"
"v_exp_f32 v43, v43\n"
"v_mfma_f32_16x16x32_bf16 v[36:39], a[16:19], v[16:19], -4.0\n"
"v_exp_f32 v44, v44\n"
"v_mfma_f32_16x16x32_bf16 v[36:39], a[20:23], v[20:23], v[36:39]\n"
"v_exp_f32 v45, v45\n"
"v_mfma_f32_16x16x32_bf16 v[36:39], a[24:27], v[24:27], v[36:39]\n"
"v_exp_f32 v46, v46\n"
"v_mfma_f32_16x16x32_bf16 v[36:39], a[28:31], v[28:31], v[36:39]\n"
"v_exp_f32 v47, v47\n"
"v_mfma_f32_16x16x32_bf16 v[56:59], v[60:63], v[48:51], v[56:59]\n"
"v_cvt_pk_bf16_f32 v52, v40, v41\n"
"v_cvt_pk_bf16_f32 v53, v42, v43\n"
"v_cvt_pk_bf16_f32 v54, v44, v45\n"
"v_cvt_pk_bf16_f32 v55, v46, v47\n"
"ds_write_b32 v109, v116 offset:9216\n"
"ds_write_b128 v108, v[52:55] offset:20480\n"
"v_mfma_f32_16x16x32_bf16 v[40:43], a[32:35], v[16:19], -4.0\n"
"v_exp_f32 v32, v32\n"
"v_mfma_f32_16x16x32_bf16 v[40:43], a[36:39], v[20:23], v[40:43]\n"
"v_exp_f32 v33, v33\n"
"v_mfma_f32_16x16x32_bf16 v[40:43], a[40:43], v[24:27], v[40:43]\n"
"v_exp_f32 v34, v34\n"
"v_mfma_f32_16x16x32_bf16 v[40:43], a[44:47], v[28:31], v[40:43]\n"
"v_exp_f32 v35, v35\n"
"v_mfma_f32_16x16x32_bf16 v[44:47], a[48:51], v[16:19], -4.0\n"
"v_exp_f32 v36, v36\n"
"v_mfma_f32_16x16x32_bf16 v[44:47], a[52:55], v[20:23], v[44:47]\n"
"v_exp_f32 v37, v37\n"
"v_mfma_f32_16x16x32_bf16 v[44:47], a[56:59], v[24:27], v[44:47]\n"
"v_exp_f32 v38, v38\n"
"v_mfma_f32_16x16x32_bf16 v[44:47], a[60:63], v[28:31], v[44:47]\n"
"v_exp_f32 v39, v39\n"
"v_mfma_f32_16x16x32_bf16 v[56:59], v[60:63], v[52:55], v[56:59]\n"
"v_cvt_pk_bf16_f32 v48, v32, v33\n"
"v_cvt_pk_bf16_f32 v49, v34, v35\n"
"v_cvt_pk_bf16_f32 v50, v36, v37\n"
"v_cvt_pk_bf16_f32 v51, v38, v39\n"
"ds_write_b128 v108, v[48:51] offset:24576\n"
"s_waitcnt lgkmcnt(1)\n"
"s_barrier\n"
"v_mfma_f32_16x16x32_bf16 v[32:35], a[64:67], v[16:19], -4.0\n"
"v_exp_f32 v40, v40\n"
"v_mfma_f32_16x16x32_bf16 v[32:35], a[68:71], v[20:23], v[32:35]\n"
"v_exp_f32 v41, v41\n"
"v_mfma_f32_16x16x32_bf16 v[32:35], a[72:75], v[24:27], v[32:35]\n"
"v_exp_f32 v42, v42\n"
"v_mfma_f32_16x16x32_bf16 v[32:35], a[76:79], v[28:31], v[32:35]\n"
"v_exp_f32 v43, v43\n"
"v_mfma_f32_16x16x32_bf16 v[36:39], a[80:83], v[16:19], -4.0\n"
"v_exp_f32 v44, v44\n"
"v_mfma_f32_16x16x32_bf16 v[36:39], a[84:87], v[20:23], v[36:39]\n"
"v_exp_f32 v45, v45\n"
"v_mfma_f32_16x16x32_bf16 v[36:39], a[88:91], v[24:27], v[36:39]\n"
"v_exp_f32 v46, v46\n"
"v_mfma_f32_16x16x32_bf16 v[36:39], a[92:95], v[28:31], v[36:39]\n"
"v_exp_f32 v47, v47\n"
"v_mfma_f32_16x16x32_bf16 v[116:119], v[60:63], v[48:51], 0\n"
"v_cvt_pk_bf16_f32 v52, v40, v41\n"
"v_cvt_pk_bf16_f32 v53, v42, v43\n"
"v_cvt_pk_bf16_f32 v54, v44, v45\n"
"v_cvt_pk_bf16_f32 v55, v46, v47\n"
"ds_read_b128 v[0:3], v96 offset:32768\n"
"ds_read_b128 v[4:7], v97 offset:32768\n"
"buffer_load_dwordx4 v106, s[24:27], s42 offen offset:2048 nt lds\n"
"ds_write_b128 v108, v[52:55] offset:28672\n"
"v_mfma_f32_16x16x32_bf16 v[40:43], a[96:99], v[16:19], -4.0\n"
"v_exp_f32 v32, v32\n"
"v_mfma_f32_16x16x32_bf16 v[40:43], a[100:103], v[20:23], v[40:43]\n"
"v_exp_f32 v33, v33\n"
"v_mfma_f32_16x16x32_bf16 v[40:43], a[104:107], v[24:27], v[40:43]\n"
"v_exp_f32 v34, v34\n"
"v_mfma_f32_16x16x32_bf16 v[40:43], a[108:111], v[28:31], v[40:43]\n"
"v_exp_f32 v35, v35\n"
"v_mfma_f32_16x16x32_bf16 v[44:47], a[112:115], v[16:19], -4.0\n"
"v_exp_f32 v36, v36\n"
"v_mfma_f32_16x16x32_bf16 v[44:47], a[116:119], v[20:23], v[44:47]\n"
"v_exp_f32 v37, v37\n"
"v_mfma_f32_16x16x32_bf16 v[44:47], a[120:123], v[24:27], v[44:47]\n"
"v_exp_f32 v38, v38\n"
"v_mfma_f32_16x16x32_bf16 v[44:47], a[124:127], v[28:31], v[44:47]\n"
"v_exp_f32 v39, v39\n"
"v_mfma_f32_16x16x32_bf16 v[116:119], v[60:63], v[52:55], v[116:119]\n"
"v_cvt_pk_bf16_f32 v48, v32, v33\n"
"v_cvt_pk_bf16_f32 v49, v34, v35\n"
"v_cvt_pk_bf16_f32 v50, v36, v37\n"
"v_cvt_pk_bf16_f32 v51, v38, v39\n"
"ds_read_b128 v[8:11], v98 offset:32768\n"
"ds_read_b128 v[12:15], v99 offset:32768\n"
"ds_write_b128 v108, v[48:51] offset:32768\n"
"s_waitcnt lgkmcnt(1)\n"
"v_mfma_f32_16x16x32_bf16 v[32:35], a[0:3], v[0:3], -4.0\n"
"v_exp_f32 v40, v40\n"
"v_mfma_f32_16x16x32_bf16 v[32:35], a[4:7], v[4:7], v[32:35]\n"
"v_exp_f32 v41, v41\n"
"v_mfma_f32_16x16x32_bf16 v[32:35], a[8:11], v[8:11], v[32:35]\n"
"v_exp_f32 v42, v42\n"
"v_mfma_f32_16x16x32_bf16 v[32:35], a[12:15], v[12:15], v[32:35]\n"
"v_exp_f32 v43, v43\n"
"v_mfma_f32_16x16x32_bf16 v[36:39], a[16:19], v[0:3], -4.0\n"
"v_exp_f32 v44, v44\n"
"v_mfma_f32_16x16x32_bf16 v[36:39], a[20:23], v[4:7], v[36:39]\n"
"v_exp_f32 v45, v45\n"
"v_mfma_f32_16x16x32_bf16 v[36:39], a[24:27], v[8:11], v[36:39]\n"
"v_exp_f32 v46, v46\n"
"v_mfma_f32_16x16x32_bf16 v[36:39], a[28:31], v[12:15], v[36:39]\n"
"v_exp_f32 v47, v47\n"
"v_mfma_f32_16x16x32_bf16 v[116:119], v[60:63], v[48:51], v[116:119]\n"
"v_cvt_pk_bf16_f32 v52, v40, v41\n"
"v_cvt_pk_bf16_f32 v53, v42, v43\n"
"v_cvt_pk_bf16_f32 v54, v44, v45\n"
"v_cvt_pk_bf16_f32 v55, v46, v47\n"
"buffer_load_dwordx4 v107, s[24:27], s42 offen offset:3072 nt lds\n"
"ds_write_b32 v109, v56 offset:10240\n"
"ds_write_b128 v108, v[52:55] offset:0\n"
"v_mfma_f32_16x16x32_bf16 v[40:43], a[32:35], v[0:3], -4.0\n"
"v_exp_f32 v32, v32\n"
"v_mfma_f32_16x16x32_bf16 v[40:43], a[36:39], v[4:7], v[40:43]\n"
"v_exp_f32 v33, v33\n"
"v_mfma_f32_16x16x32_bf16 v[40:43], a[40:43], v[8:11], v[40:43]\n"
"v_exp_f32 v34, v34\n"
"v_mfma_f32_16x16x32_bf16 v[40:43], a[44:47], v[12:15], v[40:43]\n"
"v_exp_f32 v35, v35\n"
"v_mfma_f32_16x16x32_bf16 v[44:47], a[48:51], v[0:3], -4.0\n"
"v_exp_f32 v36, v36\n"
"v_mfma_f32_16x16x32_bf16 v[44:47], a[52:55], v[4:7], v[44:47]\n"
"v_exp_f32 v37, v37\n"
"v_mfma_f32_16x16x32_bf16 v[44:47], a[56:59], v[8:11], v[44:47]\n"
"v_exp_f32 v38, v38\n"
"v_mfma_f32_16x16x32_bf16 v[44:47], a[60:63], v[12:15], v[44:47]\n"
"v_exp_f32 v39, v39\n"
"v_mfma_f32_16x16x32_bf16 v[116:119], v[60:63], v[52:55], v[116:119]\n"
"v_cvt_pk_bf16_f32 v48, v32, v33\n"
"v_cvt_pk_bf16_f32 v49, v34, v35\n"
"v_cvt_pk_bf16_f32 v50, v36, v37\n"
"v_cvt_pk_bf16_f32 v51, v38, v39\n"
"ds_write_b128 v108, v[48:51] offset:4096\n"
"s_waitcnt lgkmcnt(1)\n"
"s_barrier\n"
"v_mfma_f32_16x16x32_bf16 v[32:35], a[64:67], v[0:3], -4.0\n"
"v_exp_f32 v40, v40\n"
"v_mfma_f32_16x16x32_bf16 v[32:35], a[68:71], v[4:7], v[32:35]\n"
"v_exp_f32 v41, v41\n"
"v_mfma_f32_16x16x32_bf16 v[32:35], a[72:75], v[8:11], v[32:35]\n"
"v_exp_f32 v42, v42\n"
"v_mfma_f32_16x16x32_bf16 v[32:35], a[76:79], v[12:15], v[32:35]\n"
"v_exp_f32 v43, v43\n"
"v_mfma_f32_16x16x32_bf16 v[36:39], a[80:83], v[0:3], -4.0\n"
"v_exp_f32 v44, v44\n"
"v_mfma_f32_16x16x32_bf16 v[36:39], a[84:87], v[4:7], v[36:39]\n"
"v_exp_f32 v45, v45\n"
"v_mfma_f32_16x16x32_bf16 v[36:39], a[88:91], v[8:11], v[36:39]\n"
"v_exp_f32 v46, v46\n"
"v_mfma_f32_16x16x32_bf16 v[36:39], a[92:95], v[12:15], v[36:39]\n"
"v_exp_f32 v47, v47\n"
"v_mfma_f32_16x16x32_bf16 v[56:59], v[60:63], v[48:51], 0\n"
"v_cvt_pk_bf16_f32 v52, v40, v41\n"
"v_cvt_pk_bf16_f32 v53, v42, v43\n"
"v_cvt_pk_bf16_f32 v54, v44, v45\n"
"v_cvt_pk_bf16_f32 v55, v46, v47\n"
"ds_read_b128 v[16:19], v96 offset:36864\n"
"ds_read_b128 v[20:23], v97 offset:36864\n"
"ds_write_b128 v108, v[52:55] offset:8192\n"
"v_mfma_f32_16x16x32_bf16 v[40:43], a[96:99], v[0:3], -4.0\n"
"v_exp_f32 v32, v32\n"
"v_mfma_f32_16x16x32_bf16 v[40:43], a[100:103], v[4:7], v[40:43]\n"
"v_exp_f32 v33, v33\n"
"v_mfma_f32_16x16x32_bf16 v[40:43], a[104:107], v[8:11], v[40:43]\n"
"v_exp_f32 v34, v34\n"
"v_mfma_f32_16x16x32_bf16 v[40:43], a[108:111], v[12:15], v[40:43]\n"
"v_exp_f32 v35, v35\n"
"v_mfma_f32_16x16x32_bf16 v[44:47], a[112:115], v[0:3], -4.0\n"
"v_exp_f32 v36, v36\n"
"v_mfma_f32_16x16x32_bf16 v[44:47], a[116:119], v[4:7], v[44:47]\n"
"v_exp_f32 v37, v37\n"
"v_mfma_f32_16x16x32_bf16 v[44:47], a[120:123], v[8:11], v[44:47]\n"
"v_exp_f32 v38, v38\n"
"v_mfma_f32_16x16x32_bf16 v[44:47], a[124:127], v[12:15], v[44:47]\n"
"v_exp_f32 v39, v39\n"
"v_mfma_f32_16x16x32_bf16 v[56:59], v[60:63], v[52:55], v[56:59]\n"
"v_cvt_pk_bf16_f32 v48, v32, v33\n"
"v_cvt_pk_bf16_f32 v49, v34, v35\n"
"v_cvt_pk_bf16_f32 v50, v36, v37\n"
"v_cvt_pk_bf16_f32 v51, v38, v39\n"
"ds_read_b128 v[24:27], v98 offset:36864\n"
"ds_read_b128 v[28:31], v99 offset:36864\n"
"ds_write_b128 v108, v[48:51] offset:12288\n"
"s_waitcnt lgkmcnt(1)\n"
"v_mfma_f32_16x16x32_bf16 v[32:35], a[0:3], v[16:19], -4.0\n"
"v_exp_f32 v40, v40\n"
"v_mfma_f32_16x16x32_bf16 v[32:35], a[4:7], v[20:23], v[32:35]\n"
"v_exp_f32 v41, v41\n"
"v_mfma_f32_16x16x32_bf16 v[32:35], a[8:11], v[24:27], v[32:35]\n"
"v_exp_f32 v42, v42\n"
"v_mfma_f32_16x16x32_bf16 v[32:35], a[12:15], v[28:31], v[32:35]\n"
"v_exp_f32 v43, v43\n"
"v_mfma_f32_16x16x32_bf16 v[36:39], a[16:19], v[16:19], -4.0\n"
"v_exp_f32 v44, v44\n"
"v_mfma_f32_16x16x32_bf16 v[36:39], a[20:23], v[20:23], v[36:39]\n"
"v_exp_f32 v45, v45\n"
"v_mfma_f32_16x16x32_bf16 v[36:39], a[24:27], v[24:27], v[36:39]\n"
"v_exp_f32 v46, v46\n"
"v_mfma_f32_16x16x32_bf16 v[36:39], a[28:31], v[28:31], v[36:39]\n"
"v_exp_f32 v47, v47\n"
"v_mfma_f32_16x16x32_bf16 v[56:59], v[60:63], v[48:51], v[56:59]\n"
"v_cvt_pk_bf16_f32 v52, v40, v41\n"
"v_cvt_pk_bf16_f32 v53, v42, v43\n"
"v_cvt_pk_bf16_f32 v54, v44, v45\n"
"v_cvt_pk_bf16_f32 v55, v46, v47\n"
"s_add_u32 s42, s42, s63\n"
"s_add_u32 m0, s54, 0x1000\n"
"s_nop 0\n"
"buffer_load_dwordx4 v104, s[24:27], s42 offen offset:0 nt lds\n"
"ds_write_b32 v109, v116 offset:8192\n"
"ds_write_b128 v108, v[52:55] offset:16384\n"
"v_mfma_f32_16x16x32_bf16 v[40:43], a[32:35], v[16:19], -4.0\n"
"v_exp_f32 v32, v32\n"
"v_mfma_f32_16x16x32_bf16 v[40:43], a[36:39], v[20:23], v[40:43]\n"
"v_exp_f32 v33, v33\n"
"v_mfma_f32_16x16x32_bf16 v[40:43], a[40:43], v[24:27], v[40:43]\n"
"v_exp_f32 v34, v34\n"
"v_mfma_f32_16x16x32_bf16 v[40:43], a[44:47], v[28:31], v[40:43]\n"
"v_exp_f32 v35, v35\n"
"v_mfma_f32_16x16x32_bf16 v[44:47], a[48:51], v[16:19], -4.0\n"
"v_exp_f32 v36, v36\n"
"v_mfma_f32_16x16x32_bf16 v[44:47], a[52:55], v[20:23], v[44:47]\n"
"v_exp_f32 v37, v37\n"
"v_mfma_f32_16x16x32_bf16 v[44:47], a[56:59], v[24:27], v[44:47]\n"
"v_exp_f32 v38, v38\n"
"v_mfma_f32_16x16x32_bf16 v[44:47], a[60:63], v[28:31], v[44:47]\n"
"v_exp_f32 v39, v39\n"
"v_mfma_f32_16x16x32_bf16 v[56:59], v[60:63], v[52:55], v[56:59]\n"
"v_cvt_pk_bf16_f32 v48, v32, v33\n"
"v_cvt_pk_bf16_f32 v49, v34, v35\n"
"v_cvt_pk_bf16_f32 v50, v36, v37\n"
"v_cvt_pk_bf16_f32 v51, v38, v39\n"
"ds_write_b128 v108, v[48:51] offset:20480\n"
"s_waitcnt lgkmcnt(1)\n"
"s_barrier\n"
"v_mfma_f32_16x16x32_bf16 v[32:35], a[64:67], v[16:19], -4.0\n"
"v_exp_f32 v40, v40\n"
"v_mfma_f32_16x16x32_bf16 v[32:35], a[68:71], v[20:23], v[32:35]\n"
"v_exp_f32 v41, v41\n"
"v_mfma_f32_16x16x32_bf16 v[32:35], a[72:75], v[24:27], v[32:35]\n"
"v_exp_f32 v42, v42\n"
"v_mfma_f32_16x16x32_bf16 v[32:35], a[76:79], v[28:31], v[32:35]\n"
"v_exp_f32 v43, v43\n"
"v_mfma_f32_16x16x32_bf16 v[36:39], a[80:83], v[16:19], -4.0\n"
"v_exp_f32 v44, v44\n"
"v_mfma_f32_16x16x32_bf16 v[36:39], a[84:87], v[20:23], v[36:39]\n"
"v_exp_f32 v45, v45\n"
"v_mfma_f32_16x16x32_bf16 v[36:39], a[88:91], v[24:27], v[36:39]\n"
"v_exp_f32 v46, v46\n"
"v_mfma_f32_16x16x32_bf16 v[36:39], a[92:95], v[28:31], v[36:39]\n"
"v_exp_f32 v47, v47\n"
"v_mfma_f32_16x16x32_bf16 v[116:119], v[60:63], v[48:51], 0\n"
"v_cvt_pk_bf16_f32 v52, v40, v41\n"
"v_cvt_pk_bf16_f32 v53, v42, v43\n"
"v_cvt_pk_bf16_f32 v54, v44, v45\n"
"v_cvt_pk_bf16_f32 v55, v46, v47\n"
"ds_read_b128 v[0:3], v96 offset:40960\n"
"ds_read_b128 v[4:7], v97 offset:40960\n"
"ds_write_b128 v108, v[52:55] offset:24576\n"
"v_mfma_f32_16x16x32_bf16 v[40:43], a[96:99], v[16:19], -4.0\n"
"v_exp_f32 v32, v32\n"
"v_mfma_f32_16x16x32_bf16 v[40:43], a[100:103], v[20:23], v[40:43]\n"
"v_exp_f32 v33, v33\n"
"v_mfma_f32_16x16x32_bf16 v[40:43], a[104:107], v[24:27], v[40:43]\n"
"v_exp_f32 v34, v34\n"
"v_mfma_f32_16x16x32_bf16 v[40:43], a[108:111], v[28:31], v[40:43]\n"
"v_exp_f32 v35, v35\n"
"v_mfma_f32_16x16x32_bf16 v[44:47], a[112:115], v[16:19], -4.0\n"
"v_exp_f32 v36, v36\n"
"v_mfma_f32_16x16x32_bf16 v[44:47], a[116:119], v[20:23], v[44:47]\n"
"v_exp_f32 v37, v37\n"
"v_mfma_f32_16x16x32_bf16 v[44:47], a[120:123], v[24:27], v[44:47]\n"
"v_exp_f32 v38, v38\n"
"v_mfma_f32_16x16x32_bf16 v[44:47], a[124:127], v[28:31], v[44:47]\n"
"v_exp_f32 v39, v39\n"
"v_mfma_f32_16x16x32_bf16 v[116:119], v[60:63], v[52:55], v[116:119]\n"
"v_cvt_pk_bf16_f32 v48, v32, v33\n"
"v_cvt_pk_bf16_f32 v49, v34, v35\n"
"v_cvt_pk_bf16_f32 v50, v36, v37\n"
"v_cvt_pk_bf16_f32 v51, v38, v39\n"
"ds_read_b128 v[8:11], v98 offset:40960\n"
"ds_read_b128 v[12:15], v99 offset:40960\n"
"buffer_load_dwordx4 v105, s[24:27], s42 offen offset:1024 nt lds\n"
"ds_write_b128 v108, v[48:51] offset:28672\n"
"s_waitcnt lgkmcnt(1)\n"
"v_mfma_f32_16x16x32_bf16 v[32:35], a[0:3], v[0:3], -4.0\n"
"v_exp_f32 v40, v40\n"
"v_mfma_f32_16x16x32_bf16 v[32:35], a[4:7], v[4:7], v[32:35]\n"
"v_exp_f32 v41, v41\n"
"v_mfma_f32_16x16x32_bf16 v[32:35], a[8:11], v[8:11], v[32:35]\n"
"v_exp_f32 v42, v42\n"
"v_mfma_f32_16x16x32_bf16 v[32:35], a[12:15], v[12:15], v[32:35]\n"
"v_exp_f32 v43, v43\n"
"v_mfma_f32_16x16x32_bf16 v[36:39], a[16:19], v[0:3], -4.0\n"
"v_exp_f32 v44, v44\n"
"v_mfma_f32_16x16x32_bf16 v[36:39], a[20:23], v[4:7], v[36:39]\n"
"v_exp_f32 v45, v45\n"
"v_mfma_f32_16x16x32_bf16 v[36:39], a[24:27], v[8:11], v[36:39]\n"
"v_exp_f32 v46, v46\n"
"v_mfma_f32_16x16x32_bf16 v[36:39], a[28:31], v[12:15], v[36:39]\n"
"v_exp_f32 v47, v47\n"
"v_mfma_f32_16x16x32_bf16 v[116:119], v[60:63], v[48:51], v[116:119]\n"
"v_cvt_pk_bf16_f32 v52, v40, v41\n"
"v_cvt_pk_bf16_f32 v53, v42, v43\n"
"v_cvt_pk_bf16_f32 v54, v44, v45\n"
"v_cvt_pk_bf16_f32 v55, v46, v47\n"
"ds_write_b32 v109, v56 offset:9216\n"
"ds_write_b128 v108, v[52:55] offset:32768\n"
"v_mfma_f32_16x16x32_bf16 v[40:43], a[32:35], v[0:3], -4.0\n"
"v_exp_f32 v32, v32\n"
"v_mfma_f32_16x16x32_bf16 v[40:43], a[36:39], v[4:7], v[40:43]\n"
"v_exp_f32 v33, v33\n"
"v_mfma_f32_16x16x32_bf16 v[40:43], a[40:43], v[8:11], v[40:43]\n"
"v_exp_f32 v34, v34\n"
"v_mfma_f32_16x16x32_bf16 v[40:43], a[44:47], v[12:15], v[40:43]\n"
"v_exp_f32 v35, v35\n"
"v_mfma_f32_16x16x32_bf16 v[44:47], a[48:51], v[0:3], -4.0\n"
"v_exp_f32 v36, v36\n"
"v_mfma_f32_16x16x32_bf16 v[44:47], a[52:55], v[4:7], v[44:47]\n"
"v_exp_f32 v37, v37\n"
"v_mfma_f32_16x16x32_bf16 v[44:47], a[56:59], v[8:11], v[44:47]\n"
"v_exp_f32 v38, v38\n"
"v_mfma_f32_16x16x32_bf16 v[44:47], a[60:63], v[12:15], v[44:47]\n"
"v_exp_f32 v39, v39\n"
"v_mfma_f32_16x16x32_bf16 v[116:119], v[60:63], v[52:55], v[116:119]\n"
"v_cvt_pk_bf16_f32 v48, v32, v33\n"
"v_cvt_pk_bf16_f32 v49, v34, v35\n"
"v_cvt_pk_bf16_f32 v50, v36, v37\n"
"v_cvt_pk_bf16_f32 v51, v38, v39\n"
"ds_write_b128 v108, v[48:51] offset:0\n"
"s_waitcnt lgkmcnt(1)\n"
"s_barrier\n"
"v_mfma_f32_16x16x32_bf16 v[32:35], a[64:67], v[0:3], -4.0\n"
"v_exp_f32 v40, v40\n"
"v_mfma_f32_16x16x32_bf16 v[32:35], a[68:71], v[4:7], v[32:35]\n"
"v_exp_f32 v41, v41\n"
"v_mfma_f32_16x16x32_bf16 v[32:35], a[72:75], v[8:11], v[32:35]\n"
"v_exp_f32 v42, v42\n"
"v_mfma_f32_16x16x32_bf16 v[32:35], a[76:79], v[12:15], v[32:35]\n"
"v_exp_f32 v43, v43\n"
"v_mfma_f32_16x16x32_bf16 v[36:39], a[80:83], v[0:3], -4.0\n"
"v_exp_f32 v44, v44\n"
"v_mfma_f32_16x16x32_bf16 v[36:39], a[84:87], v[4:7], v[36:39]\n"
"v_exp_f32 v45, v45\n"
"v_mfma_f32_16x16x32_bf16 v[36:39], a[88:91], v[8:11], v[36:39]\n"
"v_exp_f32 v46, v46\n"
"v_mfma_f32_16x16x32_bf16 v[36:39], a[92:95], v[12:15], v[36:39]\n"
"v_exp_f32 v47, v47\n"
"v_mfma_f32_16x16x32_bf16 v[56:59], v[60:63], v[48:51], 0\n"
"v_cvt_pk_bf16_f32 v52, v40, v41\n"
"v_cvt_pk_bf16_f32 v53, v42, v43\n"
"v_cvt_pk_bf16_f32 v54, v44, v45\n"
"v_cvt_pk_bf16_f32 v55, v46, v47\n"
"ds_read_b128 v[16:19], v96 offset:45056\n"
"ds_read_b128 v[20:23], v97 offset:45056\n"
"buffer_load_dwordx4 v106, s[24:27], s42 offen offset:2048 nt lds\n"
"ds_write_b128 v108, v[52:55] offset:4096\n"
"v_mfma_f32_16x16x32_bf16 v[40:43], a[96:99], v[0:3], -4.0\n"
"v_exp_f32 v32, v32\n"
"v_mfma_f32_16x16x32_bf16 v[40:43], a[100:103], v[4:7], v[40:43]\n"
"v_exp_f32 v33, v33\n"
"v_mfma_f32_16x16x32_bf16 v[40:43], a[104:107], v[8:11], v[40:43]\n"
"v_exp_f32 v34, v34\n"
"v_mfma_f32_16x16x32_bf16 v[40:43], a[108:111], v[12:15], v[40:43]\n"
"v_exp_f32 v35, v35\n"
"v_mfma_f32_16x16x32_bf16 v[44:47], a[112:115], v[0:3], -4.0\n"
"v_exp_f32 v36, v36\n"
"v_mfma_f32_16x16x32_bf16 v[44:47], a[116:119], v[4:7], v[44:47]\n"
"v_exp_f32 v37, v37\n"
"v_mfma_f32_16x16x32_bf16 v[44:47], a[120:123], v[8:11], v[44:47]\n"
"v_exp_f32 v38, v38\n"
"v_mfma_f32_16x16x32_bf16 v[44:47], a[124:127], v[12:15], v[44:47]\n"
"v_exp_f32 v39, v39\n"
"v_mfma_f32_16x16x32_bf16 v[56:59], v[60:63], v[52:55], v[56:59]\n"
"v_cvt_pk_bf16_f32 v48, v32, v33\n"
"v_cvt_pk_bf16_f32 v49, v34, v35\n"
"v_cvt_pk_bf16_f32 v50, v36, v37\n"
"v_cvt_pk_bf16_f32 v51, v38, v39\n"
"ds_read_b128 v[24:27], v98 offset:45056\n"
"ds_read_b128 v[28:31], v99 offset:45056\n"
"ds_write_b128 v108, v[48:51] offset:8192\n"
"s_waitcnt lgkmcnt(1)\n"
"v_mfma_f32_16x16x32_bf16 v[32:35], a[0:3], v[16:19], -4.0\n"
"v_exp_f32 v40, v40\n"
"v_mfma_f32_16x16x32_bf16 v[32:35], a[4:7], v[20:23], v[32:35]\n"
"v_exp_f32 v41, v41\n"
"v_mfma_f32_16x16x32_bf16 v[32:35], a[8:11], v[24:27], v[32:35]\n"
"v_exp_f32 v42, v42\n"
"v_mfma_f32_16x16x32_bf16 v[32:35], a[12:15], v[28:31], v[32:35]\n"
"v_exp_f32 v43, v43\n"
"v_mfma_f32_16x16x32_bf16 v[36:39], a[16:19], v[16:19], -4.0\n"
"v_exp_f32 v44, v44\n"
"v_mfma_f32_16x16x32_bf16 v[36:39], a[20:23], v[20:23], v[36:39]\n"
"v_exp_f32 v45, v45\n"
"v_mfma_f32_16x16x32_bf16 v[36:39], a[24:27], v[24:27], v[36:39]\n"
"v_exp_f32 v46, v46\n"
"v_mfma_f32_16x16x32_bf16 v[36:39], a[28:31], v[28:31], v[36:39]\n"
"v_exp_f32 v47, v47\n"
"v_mfma_f32_16x16x32_bf16 v[56:59], v[60:63], v[48:51], v[56:59]\n"
"v_cvt_pk_bf16_f32 v52, v40, v41\n"
"v_cvt_pk_bf16_f32 v53, v42, v43\n"
"v_cvt_pk_bf16_f32 v54, v44, v45\n"
"v_cvt_pk_bf16_f32 v55, v46, v47\n"
"buffer_load_dwordx4 v107, s[24:27], s42 offen offset:3072 nt lds\n"
"ds_write_b32 v109, v116 offset:10240\n"
"ds_write_b128 v108, v[52:55] offset:12288\n"
"v_mfma_f32_16x16x32_bf16 v[40:43], a[32:35], v[16:19], -4.0\n"
"v_exp_f32 v32, v32\n"
"v_mfma_f32_16x16x32_bf16 v[40:43], a[36:39], v[20:23], v[40:43]\n"
"v_exp_f32 v33, v33\n"
"v_mfma_f32_16x16x32_bf16 v[40:43], a[40:43], v[24:27], v[40:43]\n"
"v_exp_f32 v34, v34\n"
"v_mfma_f32_16x16x32_bf16 v[40:43], a[44:47], v[28:31], v[40:43]\n"
"v_exp_f32 v35, v35\n"
"v_mfma_f32_16x16x32_bf16 v[44:47], a[48:51], v[16:19], -4.0\n"
"v_exp_f32 v36, v36\n"
"v_mfma_f32_16x16x32_bf16 v[44:47], a[52:55], v[20:23], v[44:47]\n"
"v_exp_f32 v37, v37\n"
"v_mfma_f32_16x16x32_bf16 v[44:47], a[56:59], v[24:27], v[44:47]\n"
"v_exp_f32 v38, v38\n"
"v_mfma_f32_16x16x32_bf16 v[44:47], a[60:63], v[28:31], v[44:47]\n"
"v_exp_f32 v39, v39\n"
"v_mfma_f32_16x16x32_bf16 v[56:59], v[60:63], v[52:55], v[56:59]\n"
"v_cvt_pk_bf16_f32 v48, v32, v33\n"
"v_cvt_pk_bf16_f32 v49, v34, v35\n"
"v_cvt_pk_bf16_f32 v50, v36, v37\n"
"v_cvt_pk_bf16_f32 v51, v38, v39\n"
"ds_write_b128 v108, v[48:51] offset:16384\n"
"s_waitcnt lgkmcnt(1)\n"
"s_barrier\n"
"v_mfma_f32_16x16x32_bf16 v[32:35], a[64:67], v[16:19], -4.0\n"
"v_exp_f32 v40, v40\n"
"v_mfma_f32_16x16x32_bf16 v[32:35], a[68:71], v[20:23], v[32:35]\n"
"v_exp_f32 v41, v41\n"
"v_mfma_f32_16x16x32_bf16 v[32:35], a[72:75], v[24:27], v[32:35]\n"
"v_exp_f32 v42, v42\n"
"v_mfma_f32_16x16x32_bf16 v[32:35], a[76:79], v[28:31], v[32:35]\n"
"v_exp_f32 v43, v43\n"
"v_mfma_f32_16x16x32_bf16 v[36:39], a[80:83], v[16:19], -4.0\n"
"v_exp_f32 v44, v44\n"
"v_mfma_f32_16x16x32_bf16 v[36:39], a[84:87], v[20:23], v[36:39]\n"
"v_exp_f32 v45, v45\n"
"v_mfma_f32_16x16x32_bf16 v[36:39], a[88:91], v[24:27], v[36:39]\n"
"v_exp_f32 v46, v46\n"
"v_mfma_f32_16x16x32_bf16 v[36:39], a[92:95], v[28:31], v[36:39]\n"
"v_exp_f32 v47, v47\n"
"v_mfma_f32_16x16x32_bf16 v[116:119], v[60:63], v[48:51], 0\n"
"v_cvt_pk_bf16_f32 v52, v40, v41\n"
"v_cvt_pk_bf16_f32 v53, v42, v43\n"
"v_cvt_pk_bf16_f32 v54, v44, v45\n"
"v_cvt_pk_bf16_f32 v55, v46, v47\n"
"ds_read_b128 v[0:3], v96 offset:32768\n"
"ds_read_b128 v[4:7], v97 offset:32768\n"
"ds_write_b128 v108, v[52:55] offset:20480\n"
"v_mfma_f32_16x16x32_bf16 v[40:43], a[96:99], v[16:19], -4.0\n"
"v_exp_f32 v32, v32\n"
"v_mfma_f32_16x16x32_bf16 v[40:43], a[100:103], v[20:23], v[40:43]\n"
"v_exp_f32 v33, v33\n"
"v_mfma_f32_16x16x32_bf16 v[40:43], a[104:107], v[24:27], v[40:43]\n"
"v_exp_f32 v34, v34\n"
"v_mfma_f32_16x16x32_bf16 v[40:43], a[108:111], v[28:31], v[40:43]\n"
"v_exp_f32 v35, v35\n"
"v_mfma_f32_16x16x32_bf16 v[44:47], a[112:115], v[16:19], -4.0\n"
"v_exp_f32 v36, v36\n"
"v_mfma_f32_16x16x32_bf16 v[44:47], a[116:119], v[20:23], v[44:47]\n"
"v_exp_f32 v37, v37\n"
"v_mfma_f32_16x16x32_bf16 v[44:47], a[120:123], v[24:27], v[44:47]\n"
"v_exp_f32 v38, v38\n"
"v_mfma_f32_16x16x32_bf16 v[44:47], a[124:127], v[28:31], v[44:47]\n"
"v_exp_f32 v39, v39\n"
"v_mfma_f32_16x16x32_bf16 v[116:119], v[60:63], v[52:55], v[116:119]\n"
"v_cvt_pk_bf16_f32 v48, v32, v33\n"
"v_cvt_pk_bf16_f32 v49, v34, v35\n"
"v_cvt_pk_bf16_f32 v50, v36, v37\n"
"v_cvt_pk_bf16_f32 v51, v38, v39\n"
"ds_read_b128 v[8:11], v98 offset:32768\n"
"ds_read_b128 v[12:15], v99 offset:32768\n"
"ds_write_b128 v108, v[48:51] offset:24576\n"
"s_waitcnt lgkmcnt(1)\n"
"v_mfma_f32_16x16x32_bf16 v[32:35], a[0:3], v[0:3], -4.0\n"
"v_exp_f32 v40, v40\n"
"v_mfma_f32_16x16x32_bf16 v[32:35], a[4:7], v[4:7], v[32:35]\n"
"v_exp_f32 v41, v41\n"
"v_mfma_f32_16x16x32_bf16 v[32:35], a[8:11], v[8:11], v[32:35]\n"
"v_exp_f32 v42, v42\n"
"v_mfma_f32_16x16x32_bf16 v[32:35], a[12:15], v[12:15], v[32:35]\n"
"v_exp_f32 v43, v43\n"
"v_mfma_f32_16x16x32_bf16 v[36:39], a[16:19], v[0:3], -4.0\n"
"v_exp_f32 v44, v44\n"
"v_mfma_f32_16x16x32_bf16 v[36:39], a[20:23], v[4:7], v[36:39]\n"
"v_exp_f32 v45, v45\n"
"v_mfma_f32_16x16x32_bf16 v[36:39], a[24:27], v[8:11], v[36:39]\n"
"v_exp_f32 v46, v46\n"
"v_mfma_f32_16x16x32_bf16 v[36:39], a[28:31], v[12:15], v[36:39]\n"
"v_exp_f32 v47, v47\n"
"v_mfma_f32_16x16x32_bf16 v[116:119], v[60:63], v[48:51], v[116:119]\n"
"v_cvt_pk_bf16_f32 v52, v40, v41\n"
"v_cvt_pk_bf16_f32 v53, v42, v43\n"
"v_cvt_pk_bf16_f32 v54, v44, v45\n"
"v_cvt_pk_bf16_f32 v55, v46, v47\n"
"ds_write_b32 v109, v56 offset:8192\n"
"ds_write_b128 v108, v[52:55] offset:28672\n"
"v_mfma_f32_16x16x32_bf16 v[40:43], a[32:35], v[0:3], -4.0\n"
"v_exp_f32 v32, v32\n"
"v_mfma_f32_16x16x32_bf16 v[40:43], a[36:39], v[4:7], v[40:43]\n"
"v_exp_f32 v33, v33\n"
"v_mfma_f32_16x16x32_bf16 v[40:43], a[40:43], v[8:11], v[40:43]\n"
"v_exp_f32 v34, v34\n"
"v_mfma_f32_16x16x32_bf16 v[40:43], a[44:47], v[12:15], v[40:43]\n"
"v_exp_f32 v35, v35\n"
"v_mfma_f32_16x16x32_bf16 v[44:47], a[48:51], v[0:3], -4.0\n"
"v_exp_f32 v36, v36\n"
"v_mfma_f32_16x16x32_bf16 v[44:47], a[52:55], v[4:7], v[44:47]\n"
"v_exp_f32 v37, v37\n"
"v_mfma_f32_16x16x32_bf16 v[44:47], a[56:59], v[8:11], v[44:47]\n"
"v_exp_f32 v38, v38\n"
"v_mfma_f32_16x16x32_bf16 v[44:47], a[60:63], v[12:15], v[44:47]\n"
"v_exp_f32 v39, v39\n"
"v_mfma_f32_16x16x32_bf16 v[116:119], v[60:63], v[52:55], v[116:119]\n"
"v_cvt_pk_bf16_f32 v48, v32, v33\n"
"v_cvt_pk_bf16_f32 v49, v34, v35\n"
"v_cvt_pk_bf16_f32 v50, v36, v37\n"
"v_cvt_pk_bf16_f32 v51, v38, v39\n"
"ds_write_b128 v108, v[48:51] offset:32768\n"
"s_waitcnt lgkmcnt(1)\n"
"s_barrier\n"
"v_mfma_f32_16x16x32_bf16 v[32:35], a[64:67], v[0:3], -4.0\n"
"v_exp_f32 v40, v40\n"
"v_mfma_f32_16x16x32_bf16 v[32:35], a[68:71], v[4:7], v[32:35]\n"
"v_exp_f32 v41, v41\n"
"v_mfma_f32_16x16x32_bf16 v[32:35], a[72:75], v[8:11], v[32:35]\n"
"v_exp_f32 v42, v42\n"
"v_mfma_f32_16x16x32_bf16 v[32:35], a[76:79], v[12:15], v[32:35]\n"
"v_exp_f32 v43, v43\n"
"v_mfma_f32_16x16x32_bf16 v[36:39], a[80:83], v[0:3], -4.0\n"
"v_exp_f32 v44, v44\n"
"v_mfma_f32_16x16x32_bf16 v[36:39], a[84:87], v[4:7], v[36:39]\n"
"v_exp_f32 v45, v45\n"
"v_mfma_f32_16x16x32_bf16 v[36:39], a[88:91], v[8:11], v[36:39]\n"
"v_exp_f32 v46, v46\n"
"v_mfma_f32_16x16x32_bf16 v[36:39], a[92:95], v[12:15], v[36:39]\n"
"v_exp_f32 v47, v47\n"
"v_mfma_f32_16x16x32_bf16 v[56:59], v[60:63], v[48:51], 0\n"
"v_cvt_pk_bf16_f32 v52, v40, v41\n"
"v_cvt_pk_bf16_f32 v53, v42, v43\n"
"v_cvt_pk_bf16_f32 v54, v44, v45\n"
"v_cvt_pk_bf16_f32 v55, v46, v47\n"
"ds_read_b128 v[16:19], v96 offset:36864\n"
"ds_read_b128 v[20:23], v97 offset:36864\n"
"ds_write_b128 v108, v[52:55] offset:0\n"
"v_mfma_f32_16x16x32_bf16 v[40:43], a[96:99], v[0:3], -4.0\n"
"v_exp_f32 v32, v32\n"
"v_mfma_f32_16x16x32_bf16 v[40:43], a[100:103], v[4:7], v[40:43]\n"
"v_exp_f32 v33, v33\n"
"v_mfma_f32_16x16x32_bf16 v[40:43], a[104:107], v[8:11], v[40:43]\n"
"v_exp_f32 v34, v34\n"
"v_mfma_f32_16x16x32_bf16 v[40:43], a[108:111], v[12:15], v[40:43]\n"
"v_exp_f32 v35, v35\n"
"v_mfma_f32_16x16x32_bf16 v[44:47], a[112:115], v[0:3], -4.0\n"
"v_exp_f32 v36, v36\n"
"v_mfma_f32_16x16x32_bf16 v[44:47], a[116:119], v[4:7], v[44:47]\n"
"v_exp_f32 v37, v37\n"
"v_mfma_f32_16x16x32_bf16 v[44:47], a[120:123], v[8:11], v[44:47]\n"
"v_exp_f32 v38, v38\n"
"v_mfma_f32_16x16x32_bf16 v[44:47], a[124:127], v[12:15], v[44:47]\n"
"v_exp_f32 v39, v39\n"
"v_mfma_f32_16x16x32_bf16 v[56:59], v[60:63], v[52:55], v[56:59]\n"
"v_cvt_pk_bf16_f32 v48, v32, v33\n"
"v_cvt_pk_bf16_f32 v49, v34, v35\n"
"v_cvt_pk_bf16_f32 v50, v36, v37\n"
"v_cvt_pk_bf16_f32 v51, v38, v39\n"
"ds_read_b128 v[24:27], v98 offset:36864\n"
"ds_read_b128 v[28:31], v99 offset:36864\n"
"ds_write_b128 v108, v[48:51] offset:4096\n"
"s_waitcnt lgkmcnt(1)\n"
"v_mfma_f32_16x16x32_bf16 v[32:35], a[0:3], v[16:19], -4.0\n"
"v_exp_f32 v40, v40\n"
"v_mfma_f32_16x16x32_bf16 v[32:35], a[4:7], v[20:23], v[32:35]\n"
"v_exp_f32 v41, v41\n"
"v_mfma_f32_16x16x32_bf16 v[32:35], a[8:11], v[24:27], v[32:35]\n"
"v_exp_f32 v42, v42\n"
"v_mfma_f32_16x16x32_bf16 v[32:35], a[12:15], v[28:31], v[32:35]\n"
"v_exp_f32 v43, v43\n"
"v_mfma_f32_16x16x32_bf16 v[36:39], a[16:19], v[16:19], -4.0\n"
"v_exp_f32 v44, v44\n"
"v_mfma_f32_16x16x32_bf16 v[36:39], a[20:23], v[20:23], v[36:39]\n"
"v_exp_f32 v45, v45\n"
"v_mfma_f32_16x16x32_bf16 v[36:39], a[24:27], v[24:27], v[36:39]\n"
"v_exp_f32 v46, v46\n"
"v_mfma_f32_16x16x32_bf16 v[36:39], a[28:31], v[28:31], v[36:39]\n"
"v_exp_f32 v47, v47\n"
"v_mfma_f32_16x16x32_bf16 v[56:59], v[60:63], v[48:51], v[56:59]\n"
"v_cvt_pk_bf16_f32 v52, v40, v41\n"
"v_cvt_pk_bf16_f32 v53, v42, v43\n"
"v_cvt_pk_bf16_f32 v54, v44, v45\n"
"v_cvt_pk_bf16_f32 v55, v46, v47\n"
"ds_write_b32 v109, v116 offset:9216\n"
"ds_write_b128 v108, v[52:55] offset:8192\n"
"v_mfma_f32_16x16x32_bf16 v[40:43], a[32:35], v[16:19], -4.0\n"
"v_exp_f32 v32, v32\n"
"v_mfma_f32_16x16x32_bf16 v[40:43], a[36:39], v[20:23], v[40:43]\n"
"v_exp_f32 v33, v33\n"
"v_mfma_f32_16x16x32_bf16 v[40:43], a[40:43], v[24:27], v[40:43]\n"
"v_exp_f32 v34, v34\n"
"v_mfma_f32_16x16x32_bf16 v[40:43], a[44:47], v[28:31], v[40:43]\n"
"v_exp_f32 v35, v35\n"
"v_mfma_f32_16x16x32_bf16 v[44:47], a[48:51], v[16:19], -4.0\n"
"v_exp_f32 v36, v36\n"
"v_mfma_f32_16x16x32_bf16 v[44:47], a[52:55], v[20:23], v[44:47]\n"
"v_exp_f32 v37, v37\n"
"v_mfma_f32_16x16x32_bf16 v[44:47], a[56:59], v[24:27], v[44:47]\n"
"v_exp_f32 v38, v38\n"
"v_mfma_f32_16x16x32_bf16 v[44:47], a[60:63], v[28:31], v[44:47]\n"
"v_exp_f32 v39, v39\n"
"v_mfma_f32_16x16x32_bf16 v[56:59], v[60:63], v[52:55], v[56:59]\n"
"v_cvt_pk_bf16_f32 v48, v32, v33\n"
"v_cvt_pk_bf16_f32 v49, v34, v35\n"
"v_cvt_pk_bf16_f32 v50, v36, v37\n"
"v_cvt_pk_bf16_f32 v51, v38, v39\n"
"ds_write_b128 v108, v[48:51] offset:12288\n"
"s_waitcnt lgkmcnt(1)\n"
"s_barrier\n"
"v_mfma_f32_16x16x32_bf16 v[32:35], a[64:67], v[16:19], -4.0\n"
"v_exp_f32 v40, v40\n"
"v_mfma_f32_16x16x32_bf16 v[32:35], a[68:71], v[20:23], v[32:35]\n"
"v_exp_f32 v41, v41\n"
"v_mfma_f32_16x16x32_bf16 v[32:35], a[72:75], v[24:27], v[32:35]\n"
"v_exp_f32 v42, v42\n"
"v_mfma_f32_16x16x32_bf16 v[32:35], a[76:79], v[28:31], v[32:35]\n"
"v_exp_f32 v43, v43\n"
"v_mfma_f32_16x16x32_bf16 v[36:39], a[80:83], v[16:19], -4.0\n"
"v_exp_f32 v44, v44\n"
"v_mfma_f32_16x16x32_bf16 v[36:39], a[84:87], v[20:23], v[36:39]\n"
"v_exp_f32 v45, v45\n"
"v_mfma_f32_16x16x32_bf16 v[36:39], a[88:91], v[24:27], v[36:39]\n"
"v_exp_f32 v46, v46\n"
"v_mfma_f32_16x16x32_bf16 v[36:39], a[92:95], v[28:31], v[36:39]\n"
"v_exp_f32 v47, v47\n"
"v_mfma_f32_16x16x32_bf16 v[116:119], v[60:63], v[48:51], 0\n"
"v_cvt_pk_bf16_f32 v52, v40, v41\n"
"v_cvt_pk_bf16_f32 v53, v42, v43\n"
"v_cvt_pk_bf16_f32 v54, v44, v45\n"
"v_cvt_pk_bf16_f32 v55, v46, v47\n"
"ds_read_b128 v[0:3], v96 offset:40960\n"
"ds_read_b128 v[4:7], v97 offset:40960\n"
"ds_write_b128 v108, v[52:55] offset:16384\n"
"v_mfma_f32_16x16x32_bf16 v[40:43], a[96:99], v[16:19], -4.0\n"
"v_exp_f32 v32, v32\n"
"v_mfma_f32_16x16x32_bf16 v[40:43], a[100:103], v[20:23], v[40:43]\n"
"v_exp_f32 v33, v33\n"
"v_mfma_f32_16x16x32_bf16 v[40:43], a[104:107], v[24:27], v[40:43]\n"
"v_exp_f32 v34, v34\n"
"v_mfma_f32_16x16x32_bf16 v[40:43], a[108:111], v[28:31], v[40:43]\n"
"v_exp_f32 v35, v35\n"
"v_mfma_f32_16x16x32_bf16 v[44:47], a[112:115], v[16:19], -4.0\n"
"v_exp_f32 v36, v36\n"
"v_mfma_f32_16x16x32_bf16 v[44:47], a[116:119], v[20:23], v[44:47]\n"
"v_exp_f32 v37, v37\n"
"v_mfma_f32_16x16x32_bf16 v[44:47], a[120:123], v[24:27], v[44:47]\n"
"v_exp_f32 v38, v38\n"
"v_mfma_f32_16x16x32_bf16 v[44:47], a[124:127], v[28:31], v[44:47]\n"
"v_exp_f32 v39, v39\n"
"v_mfma_f32_16x16x32_bf16 v[116:119], v[60:63], v[52:55], v[116:119]\n"
"v_cvt_pk_bf16_f32 v48, v32, v33\n"
"v_cvt_pk_bf16_f32 v49, v34, v35\n"
"v_cvt_pk_bf16_f32 v50, v36, v37\n"
"v_cvt_pk_bf16_f32 v51, v38, v39\n"
"ds_read_b128 v[8:11], v98 offset:40960\n"
"ds_read_b128 v[12:15], v99 offset:40960\n"
"ds_write_b128 v108, v[48:51] offset:20480\n"
"s_waitcnt lgkmcnt(1)\n"
"v_mfma_f32_16x16x32_bf16 v[32:35], a[0:3], v[0:3], -4.0\n"
"v_exp_f32 v40, v40\n"
"v_mfma_f32_16x16x32_bf16 v[32:35], a[4:7], v[4:7], v[32:35]\n"
"v_exp_f32 v41, v41\n"
"v_mfma_f32_16x16x32_bf16 v[32:35], a[8:11], v[8:11], v[32:35]\n"
"v_exp_f32 v42, v42\n"
"v_mfma_f32_16x16x32_bf16 v[32:35], a[12:15], v[12:15], v[32:35]\n"
"v_exp_f32 v43, v43\n"
"v_mfma_f32_16x16x32_bf16 v[36:39], a[16:19], v[0:3], -4.0\n"
"v_exp_f32 v44, v44\n"
"v_mfma_f32_16x16x32_bf16 v[36:39], a[20:23], v[4:7], v[36:39]\n"
"v_exp_f32 v45, v45\n"
"v_mfma_f32_16x16x32_bf16 v[36:39], a[24:27], v[8:11], v[36:39]\n"
"v_exp_f32 v46, v46\n"
"v_mfma_f32_16x16x32_bf16 v[36:39], a[28:31], v[12:15], v[36:39]\n"
"v_exp_f32 v47, v47\n"
"v_mfma_f32_16x16x32_bf16 v[116:119], v[60:63], v[48:51], v[116:119]\n"
"v_cvt_pk_bf16_f32 v52, v40, v41\n"
"v_cvt_pk_bf16_f32 v53, v42, v43\n"
"v_cvt_pk_bf16_f32 v54, v44, v45\n"
"v_cvt_pk_bf16_f32 v55, v46, v47\n"
"ds_write_b32 v109, v56 offset:10240\n"
"ds_write_b128 v108, v[52:55] offset:24576\n"
"v_mfma_f32_16x16x32_bf16 v[40:43], a[32:35], v[0:3], -4.0\n"
"v_exp_f32 v32, v32\n"
"v_mfma_f32_16x16x32_bf16 v[40:43], a[36:39], v[4:7], v[40:43]\n"
"v_exp_f32 v33, v33\n"
"v_mfma_f32_16x16x32_bf16 v[40:43], a[40:43], v[8:11], v[40:43]\n"
"v_exp_f32 v34, v34\n"
"v_mfma_f32_16x16x32_bf16 v[40:43], a[44:47], v[12:15], v[40:43]\n"
"v_exp_f32 v35, v35\n"
"v_mfma_f32_16x16x32_bf16 v[44:47], a[48:51], v[0:3], -4.0\n"
"v_exp_f32 v36, v36\n"
"v_mfma_f32_16x16x32_bf16 v[44:47], a[52:55], v[4:7], v[44:47]\n"
"v_exp_f32 v37, v37\n"
"v_mfma_f32_16x16x32_bf16 v[44:47], a[56:59], v[8:11], v[44:47]\n"
"v_exp_f32 v38, v38\n"
"v_mfma_f32_16x16x32_bf16 v[44:47], a[60:63], v[12:15], v[44:47]\n"
"v_exp_f32 v39, v39\n"
"v_mfma_f32_16x16x32_bf16 v[116:119], v[60:63], v[52:55], v[116:119]\n"
"v_cvt_pk_bf16_f32 v48, v32, v33\n"
"v_cvt_pk_bf16_f32 v49, v34, v35\n"
"v_cvt_pk_bf16_f32 v50, v36, v37\n"
"v_cvt_pk_bf16_f32 v51, v38, v39\n"
"ds_write_b128 v108, v[48:51] offset:28672\n"
"s_waitcnt lgkmcnt(1)\n"
"s_barrier\n"
"v_mfma_f32_16x16x32_bf16 v[32:35], a[64:67], v[0:3], -4.0\n"
"v_exp_f32 v40, v40\n"
"v_mfma_f32_16x16x32_bf16 v[32:35], a[68:71], v[4:7], v[32:35]\n"
"v_exp_f32 v41, v41\n"
"v_mfma_f32_16x16x32_bf16 v[32:35], a[72:75], v[8:11], v[32:35]\n"
"v_exp_f32 v42, v42\n"
"v_mfma_f32_16x16x32_bf16 v[32:35], a[76:79], v[12:15], v[32:35]\n"
"v_exp_f32 v43, v43\n"
"v_mfma_f32_16x16x32_bf16 v[36:39], a[80:83], v[0:3], -4.0\n"
"v_exp_f32 v44, v44\n"
"v_mfma_f32_16x16x32_bf16 v[36:39], a[84:87], v[4:7], v[36:39]\n"
"v_exp_f32 v45, v45\n"
"v_mfma_f32_16x16x32_bf16 v[36:39], a[88:91], v[8:11], v[36:39]\n"
"v_exp_f32 v46, v46\n"
"v_mfma_f32_16x16x32_bf16 v[36:39], a[92:95], v[12:15], v[36:39]\n"
"v_exp_f32 v47, v47\n"
"v_mfma_f32_16x16x32_bf16 v[56:59], v[60:63], v[48:51], 0\n"
"v_cvt_pk_bf16_f32 v52, v40, v41\n"
"v_cvt_pk_bf16_f32 v53, v42, v43\n"
"v_cvt_pk_bf16_f32 v54, v44, v45\n"
"v_cvt_pk_bf16_f32 v55, v46, v47\n"
"ds_read_b128 v[16:19], v96 offset:0\n"
"ds_read_b128 v[20:23], v97 offset:0\n"
"s_waitcnt vmcnt(4)\n"
"ds_read_b128 a[32:35], v100 offset:0\n"
"ds_read_b128 a[36:39], v101 offset:0\n"
"ds_read_b128 a[40:43], v102 offset:0\n"
"ds_read_b128 a[44:47], v103 offset:0\n"
"ds_write_b128 v108, v[52:55] offset:32768\n"
"v_mfma_f32_16x16x32_bf16 v[40:43], a[96:99], v[0:3], -4.0\n"
"v_exp_f32 v32, v32\n"
"v_mfma_f32_16x16x32_bf16 v[40:43], a[100:103], v[4:7], v[40:43]\n"
"v_exp_f32 v33, v33\n"
"v_mfma_f32_16x16x32_bf16 v[40:43], a[104:107], v[8:11], v[40:43]\n"
"v_exp_f32 v34, v34\n"
"v_mfma_f32_16x16x32_bf16 v[40:43], a[108:111], v[12:15], v[40:43]\n"
"v_exp_f32 v35, v35\n"
"v_mfma_f32_16x16x32_bf16 v[44:47], a[112:115], v[0:3], -4.0\n"
"v_exp_f32 v36, v36\n"
"v_mfma_f32_16x16x32_bf16 v[44:47], a[116:119], v[4:7], v[44:47]\n"
"v_exp_f32 v37, v37\n"
"v_mfma_f32_16x16x32_bf16 v[44:47], a[120:123], v[8:11], v[44:47]\n"
"v_exp_f32 v38, v38\n"
"v_mfma_f32_16x16x32_bf16 v[44:47], a[124:127], v[12:15], v[44:47]\n"
"v_exp_f32 v39, v39\n"
"v_mfma_f32_16x16x32_bf16 v[56:59], v[60:63], v[52:55], v[56:59]\n"
"v_cvt_pk_bf16_f32 v48, v32, v33\n"
"v_cvt_pk_bf16_f32 v49, v34, v35\n"
"v_cvt_pk_bf16_f32 v50, v36, v37\n"
"v_cvt_pk_bf16_f32 v51, v38, v39\n"
"ds_read_b128 v[24:27], v98 offset:0\n"
"ds_read_b128 v[28:31], v99 offset:0\n"
"ds_read_b128 v[0:3], v96 offset:4096\n"
"ds_read_b128 v[4:7], v97 offset:4096\n"
"s_waitcnt vmcnt(0)\n"
"ds_read_b128 a[48:51], v100 offset:4096\n"
"ds_read_b128 a[52:55], v101 offset:4096\n"
"ds_read_b128 a[56:59], v102 offset:4096\n"
"ds_read_b128 a[60:63], v103 offset:4096\n"
"s_waitcnt lgkmcnt(9)\n"
"s_xor_b32 s42, s62, 0x10000\n"
"s_mov_b32 m0, s54\n"
"s_add_u32 s42, s42, s39\n"
"buffer_load_dwordx4 v104, s[24:27], s42 offen offset:0 nt lds\n"
"buffer_load_dwordx4 v105, s[24:27], s42 offen offset:1024 nt lds\n"
"buffer_load_dwordx4 v106, s[24:27], s42 offen offset:2048 nt lds\n"
"buffer_load_dwordx4 v107, s[24:27], s42 offen offset:3072 nt lds\n"
"ds_write_b128 v108, v[48:51] offset:0\n"
"s_waitcnt lgkmcnt(7)\n"
"v_mfma_f32_16x16x32_bf16 v[32:35], v[64:67], v[16:19], -4.0\n"
"v_exp_f32 v40, v40\n"
"v_mfma_f32_16x16x32_bf16 v[32:35], v[68:71], v[20:23], v[32:35]\n"
"v_exp_f32 v41, v41\n"
"v_mfma_f32_16x16x32_bf16 v[32:35], v[72:75], v[24:27], v[32:35]\n"
"v_exp_f32 v42, v42\n"
"v_mfma_f32_16x16x32_bf16 v[32:35], v[76:79], v[28:31], v[32:35]\n"
"v_exp_f32 v43, v43\n"
"v_mfma_f32_16x16x32_bf16 v[36:39], v[80:83], v[16:19], -4.0\n"
"v_exp_f32 v44, v44\n"
"v_mfma_f32_16x16x32_bf16 v[36:39], v[84:87], v[20:23], v[36:39]\n"
"v_exp_f32 v45, v45\n"
"v_mfma_f32_16x16x32_bf16 v[36:39], v[88:91], v[24:27], v[36:39]\n"
"v_exp_f32 v46, v46\n"
"v_mfma_f32_16x16x32_bf16 v[36:39], v[92:95], v[28:31], v[36:39]\n"
"v_exp_f32 v47, v47\n"
"v_mfma_f32_16x16x32_bf16 v[56:59], v[60:63], v[48:51], v[56:59]\n"
"v_cvt_pk_bf16_f32 v52, v40, v41\n"
"v_cvt_pk_bf16_f32 v53, v42, v43\n"
"v_cvt_pk_bf16_f32 v54, v44, v45\n"
"v_cvt_pk_bf16_f32 v55, v46, v47\n"
"ds_read_b128 v[8:11], v98 offset:4096\n"
"ds_read_b128 v[12:15], v99 offset:4096\n"
"s_waitcnt lgkmcnt(3)\n"
"s_add_u32 s42, s42, s63\n"
"s_add_u32 m0, s54, 0x1000\n"
"s_nop 0\n"
"buffer_load_dwordx4 v104, s[24:27], s42 offen offset:0 nt lds\n"
"buffer_load_dwordx4 v105, s[24:27], s42 offen offset:1024 nt lds\n"
"buffer_load_dwordx4 v106, s[24:27], s42 offen offset:2048 nt lds\n"
"buffer_load_dwordx4 v107, s[24:27], s42 offen offset:3072 nt lds\n"
"ds_write_b32 v109, v116 offset:8192\n"
"ds_write_b128 v108, v[52:55] offset:4096\n"
"v_mfma_f32_16x16x32_bf16 v[40:43], a[32:35], v[16:19], -4.0\n"
"v_exp_f32 v32, v32\n"
"v_mfma_f32_16x16x32_bf16 v[40:43], a[36:39], v[20:23], v[40:43]\n"
"v_exp_f32 v33, v33\n"
"v_mfma_f32_16x16x32_bf16 v[40:43], a[40:43], v[24:27], v[40:43]\n"
"v_exp_f32 v34, v34\n"
"v_mfma_f32_16x16x32_bf16 v[40:43], a[44:47], v[28:31], v[40:43]\n"
"v_exp_f32 v35, v35\n"
"v_mfma_f32_16x16x32_bf16 v[44:47], a[48:51], v[16:19], -4.0\n"
"v_exp_f32 v36, v36\n"
"v_mfma_f32_16x16x32_bf16 v[44:47], a[52:55], v[20:23], v[44:47]\n"
"v_exp_f32 v37, v37\n"
"v_mfma_f32_16x16x32_bf16 v[44:47], a[56:59], v[24:27], v[44:47]\n"
"v_exp_f32 v38, v38\n"
"v_mfma_f32_16x16x32_bf16 v[44:47], a[60:63], v[28:31], v[44:47]\n"
"v_exp_f32 v39, v39\n"
"v_mfma_f32_16x16x32_bf16 v[56:59], v[60:63], v[52:55], v[56:59]\n"
"v_cvt_pk_bf16_f32 v48, v32, v33\n"
"v_cvt_pk_bf16_f32 v49, v34, v35\n"
"v_cvt_pk_bf16_f32 v50, v36, v37\n"
"v_cvt_pk_bf16_f32 v51, v38, v39\n"
"ds_write_b128 v108, v[48:51] offset:8192\n"
"s_waitcnt lgkmcnt(1)\n"
"s_barrier\n"
"v_mfma_f32_16x16x32_bf16 v[32:35], v[64:67], v[0:3], -4.0\n"
"v_exp_f32 v40, v40\n"
"v_mfma_f32_16x16x32_bf16 v[32:35], v[68:71], v[4:7], v[32:35]\n"
"v_exp_f32 v41, v41\n"
"v_mfma_f32_16x16x32_bf16 v[32:35], v[72:75], v[8:11], v[32:35]\n"
"v_exp_f32 v42, v42\n"
"v_mfma_f32_16x16x32_bf16 v[32:35], v[76:79], v[12:15], v[32:35]\n"
"v_exp_f32 v43, v43\n"
"v_mfma_f32_16x16x32_bf16 v[36:39], v[80:83], v[0:3], -4.0\n"
"v_exp_f32 v44, v44\n"
"v_mfma_f32_16x16x32_bf16 v[36:39], v[84:87], v[4:7], v[36:39]\n"
"v_exp_f32 v45, v45\n"
"v_mfma_f32_16x16x32_bf16 v[36:39], v[88:91], v[8:11], v[36:39]\n"
"v_exp_f32 v46, v46\n"
"v_mfma_f32_16x16x32_bf16 v[36:39], v[92:95], v[12:15], v[36:39]\n"
"v_exp_f32 v47, v47\n"
"v_mfma_f32_16x16x32_bf16 a[0:3], v[60:63], v[48:51], 0\n"
"v_cvt_pk_bf16_f32 v52, v40, v41\n"
"v_cvt_pk_bf16_f32 v53, v42, v43\n"
"v_cvt_pk_bf16_f32 v54, v44, v45\n"
"v_cvt_pk_bf16_f32 v55, v46, v47\n"
"ds_read_b128 v[16:19], v96 offset:8192\n"
"ds_read_b128 v[20:23], v97 offset:8192\n"
"ds_write_b32 v109, v56 offset:9216\n"
"ds_write_b128 v108, v[52:55] offset:12288\n"
"v_mfma_f32_16x16x32_bf16 v[40:43], a[32:35], v[0:3], -4.0\n"
"v_exp_f32 v32, v32\n"
"v_mfma_f32_16x16x32_bf16 v[40:43], a[36:39], v[4:7], v[40:43]\n"
"v_exp_f32 v33, v33\n"
"v_mfma_f32_16x16x32_bf16 v[40:43], a[40:43], v[8:11], v[40:43]\n"
"v_exp_f32 v34, v34\n"
"v_mfma_f32_16x16x32_bf16 v[40:43], a[44:47], v[12:15], v[40:43]\n"
"v_exp_f32 v35, v35\n"
"v_mfma_f32_16x16x32_bf16 v[44:47], a[48:51], v[0:3], -4.0\n"
"v_exp_f32 v36, v36\n"
"v_mfma_f32_16x16x32_bf16 v[44:47], a[52:55], v[4:7], v[44:47]\n"
"v_exp_f32 v37, v37\n"
"v_mfma_f32_16x16x32_bf16 v[44:47], a[56:59], v[8:11], v[44:47]\n"
"v_exp_f32 v38, v38\n"
"v_mfma_f32_16x16x32_bf16 v[44:47], a[60:63], v[12:15], v[44:47]\n"
"v_exp_f32 v39, v39\n"
"v_mfma_f32_16x16x32_bf16 a[0:3], v[60:63], v[52:55], a[0:3]\n"
"v_cvt_pk_bf16_f32 v48, v32, v33\n"
"v_cvt_pk_bf16_f32 v49, v34, v35\n"
"v_cvt_pk_bf16_f32 v50, v36, v37\n"
"v_cvt_pk_bf16_f32 v51, v38, v39\n"
"ds_read_b128 v[24:27], v98 offset:8192\n"
"ds_read_b128 v[28:31], v99 offset:8192\n"
"ds_read_b128 v[0:3], v96 offset:12288\n"
"ds_read_b128 v[4:7], v97 offset:12288\n"
"ds_write_b128 v108, v[48:51] offset:16384\n"
"s_waitcnt lgkmcnt(3)\n"
"v_mfma_f32_16x16x32_bf16 v[32:35], v[64:67], v[16:19], -4.0\n"
"v_exp_f32 v40, v40\n"
"v_mfma_f32_16x16x32_bf16 v[32:35], v[68:71], v[20:23], v[32:35]\n"
"v_exp_f32 v41, v41\n"
"v_mfma_f32_16x16x32_bf16 v[32:35], v[72:75], v[24:27], v[32:35]\n"
"v_exp_f32 v42, v42\n"
"v_mfma_f32_16x16x32_bf16 v[32:35], v[76:79], v[28:31], v[32:35]\n"
"v_exp_f32 v43, v43\n"
"v_mfma_f32_16x16x32_bf16 v[36:39], v[80:83], v[16:19], -4.0\n"
"v_exp_f32 v44, v44\n"
"v_mfma_f32_16x16x32_bf16 v[36:39], v[84:87], v[20:23], v[36:39]\n"
"v_exp_f32 v45, v45\n"
"v_mfma_f32_16x16x32_bf16 v[36:39], v[88:91], v[24:27], v[36:39]\n"
"v_exp_f32 v46, v46\n"
"v_mfma_f32_16x16x32_bf16 v[36:39], v[92:95], v[28:31], v[36:39]\n"
"v_exp_f32 v47, v47\n"
"v_mfma_f32_16x16x32_bf16 a[4:7], v[60:63], v[48:51], 0\n"
"v_cvt_pk_bf16_f32 v52, v40, v41\n"
"v_cvt_pk_bf16_f32 v53, v42, v43\n"
"v_cvt_pk_bf16_f32 v54, v44, v45\n"
"v_cvt_pk_bf16_f32 v55, v46, v47\n"
"ds_read_b128 v[8:11], v98 offset:12288\n"
"ds_read_b128 v[12:15], v99 offset:12288\n"
"ds_write_b128 v108, v[52:55] offset:20480\n"
"v_mfma_f32_16x16x32_bf16 v[40:43], a[32:35], v[16:19], -4.0\n"
"v_exp_f32 v32, v32\n"
"v_mfma_f32_16x16x32_bf16 v[40:43], a[36:39], v[20:23], v[40:43]\n"
"v_exp_f32 v33, v33\n"
"v_mfma_f32_16x16x32_bf16 v[40:43], a[40:43], v[24:27], v[40:43]\n"
"v_exp_f32 v34, v34\n"
"v_mfma_f32_16x16x32_bf16 v[40:43], a[44:47], v[28:31], v[40:43]\n"
"v_exp_f32 v35, v35\n"
"v_mfma_f32_16x16x32_bf16 v[44:47], a[48:51], v[16:19], -4.0\n"
"v_exp_f32 v36, v36\n"
"v_mfma_f32_16x16x32_bf16 v[44:47], a[52:55], v[20:23], v[44:47]\n"
"v_exp_f32 v37, v37\n"
"v_mfma_f32_16x16x32_bf16 v[44:47], a[56:59], v[24:27], v[44:47]\n"
"v_exp_f32 v38, v38\n"
"v_mfma_f32_16x16x32_bf16 v[44:47], a[60:63], v[28:31], v[44:47]\n"
"v_exp_f32 v39, v39\n"
"v_mfma_f32_16x16x32_bf16 a[4:7], v[60:63], v[52:55], a[4:7]\n"
"v_cvt_pk_bf16_f32 v48, v32, v33\n"
"v_cvt_pk_bf16_f32 v49, v34, v35\n"
"v_cvt_pk_bf16_f32 v50, v36, v37\n"
"v_cvt_pk_bf16_f32 v51, v38, v39\n"
"ds_write_b128 v108, v[48:51] offset:24576\n"
"s_waitcnt lgkmcnt(1)\n"
"s_barrier\n"
"v_mfma_f32_16x16x32_bf16 v[32:35], v[64:67], v[0:3], -4.0\n"
"v_exp_f32 v40, v40\n"
"v_mfma_f32_16x16x32_bf16 v[32:35], v[68:71], v[4:7], v[32:35]\n"
"v_exp_f32 v41, v41\n"
"v_mfma_f32_16x16x32_bf16 v[32:35], v[72:75], v[8:11], v[32:35]\n"
"v_exp_f32 v42, v42\n"
"v_mfma_f32_16x16x32_bf16 v[32:35], v[76:79], v[12:15], v[32:35]\n"
"v_exp_f32 v43, v43\n"
"v_mfma_f32_16x16x32_bf16 v[36:39], v[80:83], v[0:3], -4.0\n"
"v_exp_f32 v44, v44\n"
"v_mfma_f32_16x16x32_bf16 v[36:39], v[84:87], v[4:7], v[36:39]\n"
"v_exp_f32 v45, v45\n"
"v_mfma_f32_16x16x32_bf16 v[36:39], v[88:91], v[8:11], v[36:39]\n"
"v_exp_f32 v46, v46\n"
"v_mfma_f32_16x16x32_bf16 v[36:39], v[92:95], v[12:15], v[36:39]\n"
"v_exp_f32 v47, v47\n"
"v_mfma_f32_16x16x32_bf16 a[8:11], v[60:63], v[48:51], 0\n"
"v_cvt_pk_bf16_f32 v52, v40, v41\n"
"v_cvt_pk_bf16_f32 v53, v42, v43\n"
"v_cvt_pk_bf16_f32 v54, v44, v45\n"
"v_cvt_pk_bf16_f32 v55, v46, v47\n"
"ds_read_b128 v[16:19], v96 offset:16384\n"
"ds_read_b128 v[20:23], v97 offset:16384\n"
"ds_write_b128 v108, v[52:55] offset:28672\n"
"v_mfma_f32_16x16x32_bf16 v[40:43], a[32:35], v[0:3], -4.0\n"
"v_exp_f32 v32, v32\n"
"v_mfma_f32_16x16x32_bf16 v[40:43], a[36:39], v[4:7], v[40:43]\n"
"v_exp_f32 v33, v33\n"
"v_mfma_f32_16x16x32_bf16 v[40:43], a[40:43], v[8:11], v[40:43]\n"
"v_exp_f32 v34, v34\n"
"v_mfma_f32_16x16x32_bf16 v[40:43], a[44:47], v[12:15], v[40:43]\n"
"v_exp_f32 v35, v35\n"
"v_mfma_f32_16x16x32_bf16 v[44:47], a[48:51], v[0:3], -4.0\n"
"v_exp_f32 v36, v36\n"
"v_mfma_f32_16x16x32_bf16 v[44:47], a[52:55], v[4:7], v[44:47]\n"
"v_exp_f32 v37, v37\n"
"v_mfma_f32_16x16x32_bf16 v[44:47], a[56:59], v[8:11], v[44:47]\n"
"v_exp_f32 v38, v38\n"
"v_mfma_f32_16x16x32_bf16 v[44:47], a[60:63], v[12:15], v[44:47]\n"
"v_exp_f32 v39, v39\n"
"v_mfma_f32_16x16x32_bf16 a[8:11], v[60:63], v[52:55], a[8:11]\n"
"v_cvt_pk_bf16_f32 v48, v32, v33\n"
"v_cvt_pk_bf16_f32 v49, v34, v35\n"
"v_cvt_pk_bf16_f32 v50, v36, v37\n"
"v_cvt_pk_bf16_f32 v51, v38, v39\n"
"ds_read_b128 v[24:27], v98 offset:16384\n"
"ds_read_b128 v[28:31], v99 offset:16384\n"
"ds_read_b128 v[0:3], v96 offset:20480\n"
"ds_read_b128 v[4:7], v97 offset:20480\n"
"ds_write_b128 v108, v[48:51] offset:32768\n"
"s_waitcnt lgkmcnt(3)\n"
"v_mfma_f32_16x16x32_bf16 v[32:35], v[64:67], v[16:19], -4.0\n"
"v_exp_f32 v40, v40\n"
"v_mfma_f32_16x16x32_bf16 v[32:35], v[68:71], v[20:23], v[32:35]\n"
"v_exp_f32 v41, v41\n"
"v_mfma_f32_16x16x32_bf16 v[32:35], v[72:75], v[24:27], v[32:35]\n"
"v_exp_f32 v42, v42\n"
"v_mfma_f32_16x16x32_bf16 v[32:35], v[76:79], v[28:31], v[32:35]\n"
"v_exp_f32 v43, v43\n"
"v_mfma_f32_16x16x32_bf16 v[36:39], v[80:83], v[16:19], -4.0\n"
"v_exp_f32 v44, v44\n"
"v_mfma_f32_16x16x32_bf16 v[36:39], v[84:87], v[20:23], v[36:39]\n"
"v_exp_f32 v45, v45\n"
"v_mfma_f32_16x16x32_bf16 v[36:39], v[88:91], v[24:27], v[36:39]\n"
"v_exp_f32 v46, v46\n"
"v_mfma_f32_16x16x32_bf16 v[36:39], v[92:95], v[28:31], v[36:39]\n"
"v_exp_f32 v47, v47\n"
"v_mfma_f32_16x16x32_bf16 a[12:15], v[60:63], v[48:51], 0\n"
"v_cvt_pk_bf16_f32 v52, v40, v41\n"
"v_cvt_pk_bf16_f32 v53, v42, v43\n"
"v_cvt_pk_bf16_f32 v54, v44, v45\n"
"v_cvt_pk_bf16_f32 v55, v46, v47\n"
"ds_read_b128 v[8:11], v98 offset:20480\n"
"ds_read_b128 v[12:15], v99 offset:20480\n"
"ds_write_b128 v108, v[52:55] offset:0\n"
"v_mfma_f32_16x16x32_bf16 v[40:43], a[32:35], v[16:19], -4.0\n"
"v_exp_f32 v32, v32\n"
"v_mfma_f32_16x16x32_bf16 v[40:43], a[36:39], v[20:23], v[40:43]\n"
"v_exp_f32 v33, v33\n"
"v_mfma_f32_16x16x32_bf16 v[40:43], a[40:43], v[24:27], v[40:43]\n"
"v_exp_f32 v34, v34\n"
"v_mfma_f32_16x16x32_bf16 v[40:43], a[44:47], v[28:31], v[40:43]\n"
"v_exp_f32 v35, v35\n"
"v_mfma_f32_16x16x32_bf16 v[44:47], a[48:51], v[16:19], -4.0\n"
"v_exp_f32 v36, v36\n"
"v_mfma_f32_16x16x32_bf16 v[44:47], a[52:55], v[20:23], v[44:47]\n"
"v_exp_f32 v37, v37\n"
"v_mfma_f32_16x16x32_bf16 v[44:47], a[56:59], v[24:27], v[44:47]\n"
"v_exp_f32 v38, v38\n"
"v_mfma_f32_16x16x32_bf16 v[44:47], a[60:63], v[28:31], v[44:47]\n"
"v_exp_f32 v39, v39\n"
"v_mfma_f32_16x16x32_bf16 a[12:15], v[60:63], v[52:55], a[12:15]\n"
"v_cvt_pk_bf16_f32 v48, v32, v33\n"
"v_cvt_pk_bf16_f32 v49, v34, v35\n"
"v_cvt_pk_bf16_f32 v50, v36, v37\n"
"v_cvt_pk_bf16_f32 v51, v38, v39\n"
"ds_write_b128 v108, v[48:51] offset:4096\n"
"s_waitcnt lgkmcnt(1)\n"
"s_barrier\n"
"v_mfma_f32_16x16x32_bf16 v[32:35], v[64:67], v[0:3], -4.0\n"
"v_exp_f32 v40, v40\n"
"v_mfma_f32_16x16x32_bf16 v[32:35], v[68:71], v[4:7], v[32:35]\n"
"v_exp_f32 v41, v41\n"
"v_mfma_f32_16x16x32_bf16 v[32:35], v[72:75], v[8:11], v[32:35]\n"
"v_exp_f32 v42, v42\n"
"v_mfma_f32_16x16x32_bf16 v[32:35], v[76:79], v[12:15], v[32:35]\n"
"v_exp_f32 v43, v43\n"
"v_mfma_f32_16x16x32_bf16 v[36:39], v[80:83], v[0:3], -4.0\n"
"v_exp_f32 v44, v44\n"
"v_mfma_f32_16x16x32_bf16 v[36:39], v[84:87], v[4:7], v[36:39]\n"
"v_exp_f32 v45, v45\n"
"v_mfma_f32_16x16x32_bf16 v[36:39], v[88:91], v[8:11], v[36:39]\n"
"v_exp_f32 v46, v46\n"
"v_mfma_f32_16x16x32_bf16 v[36:39], v[92:95], v[12:15], v[36:39]\n"
"v_exp_f32 v47, v47\n"
"v_mfma_f32_16x16x32_bf16 a[16:19], v[60:63], v[48:51], 0\n"
"v_cvt_pk_bf16_f32 v52, v40, v41\n"
"v_cvt_pk_bf16_f32 v53, v42, v43\n"
"v_cvt_pk_bf16_f32 v54, v44, v45\n"
"v_cvt_pk_bf16_f32 v55, v46, v47\n"
"ds_read_b128 v[16:19], v96 offset:24576\n"
"ds_read_b128 v[20:23], v97 offset:24576\n"
"s_waitcnt vmcnt(4)\n"
"ds_read_b128 a[64:67], v100 offset:0\n"
"ds_read_b128 a[68:71], v101 offset:0\n"
"ds_read_b128 a[72:75], v102 offset:0\n"
"ds_read_b128 a[76:79], v103 offset:0\n"
"ds_write_b128 v108, v[52:55] offset:8192\n"
"v_mfma_f32_16x16x32_bf16 v[40:43], a[32:35], v[0:3], -4.0\n"
"v_exp_f32 v32, v32\n"
"v_mfma_f32_16x16x32_bf16 v[40:43], a[36:39], v[4:7], v[40:43]\n"
"v_exp_f32 v33, v33\n"
"v_mfma_f32_16x16x32_bf16 v[40:43], a[40:43], v[8:11], v[40:43]\n"
"v_exp_f32 v34, v34\n"
"v_mfma_f32_16x16x32_bf16 v[40:43], a[44:47], v[12:15], v[40:43]\n"
"v_exp_f32 v35, v35\n"
"v_mfma_f32_16x16x32_bf16 v[44:47], a[48:51], v[0:3], -4.0\n"
"v_exp_f32 v36, v36\n"
"v_mfma_f32_16x16x32_bf16 v[44:47], a[52:55], v[4:7], v[44:47]\n"
"v_exp_f32 v37, v37\n"
"v_mfma_f32_16x16x32_bf16 v[44:47], a[56:59], v[8:11], v[44:47]\n"
"v_exp_f32 v38, v38\n"
"v_mfma_f32_16x16x32_bf16 v[44:47], a[60:63], v[12:15], v[44:47]\n"
"v_exp_f32 v39, v39\n"
"v_mfma_f32_16x16x32_bf16 a[16:19], v[60:63], v[52:55], a[16:19]\n"
"v_cvt_pk_bf16_f32 v48, v32, v33\n"
"v_cvt_pk_bf16_f32 v49, v34, v35\n"
"v_cvt_pk_bf16_f32 v50, v36, v37\n"
"v_cvt_pk_bf16_f32 v51, v38, v39\n"
"ds_read_b128 v[24:27], v98 offset:24576\n"
"ds_read_b128 v[28:31], v99 offset:24576\n"
"ds_read_b128 v[0:3], v96 offset:28672\n"
"ds_read_b128 v[4:7], v97 offset:28672\n"
"s_waitcnt vmcnt(0)\n"
"ds_read_b128 a[80:83], v100 offset:4096\n"
"ds_read_b128 a[84:87], v101 offset:4096\n"
"ds_read_b128 a[88:91], v102 offset:4096\n"
"ds_read_b128 a[92:95], v103 offset:4096\n"
"s_waitcnt lgkmcnt(9)\n"
"s_xor_b32 s42, s62, 0x18000\n"
"s_mov_b32 m0, s54\n"
"s_add_u32 s42, s42, s39\n"
"buffer_load_dwordx4 v104, s[24:27], s42 offen offset:0 nt lds\n"
"buffer_load_dwordx4 v105, s[24:27], s42 offen offset:1024 nt lds\n"
"buffer_load_dwordx4 v106, s[24:27], s42 offen offset:2048 nt lds\n"
"buffer_load_dwordx4 v107, s[24:27], s42 offen offset:3072 nt lds\n"
"ds_write_b128 v108, v[48:51] offset:12288\n"
"s_waitcnt lgkmcnt(7)\n"
"v_mfma_f32_16x16x32_bf16 v[32:35], v[64:67], v[16:19], -4.0\n"
"v_exp_f32 v40, v40\n"
"v_mfma_f32_16x16x32_bf16 v[32:35], v[68:71], v[20:23], v[32:35]\n"
"v_exp_f32 v41, v41\n"
"v_mfma_f32_16x16x32_bf16 v[32:35], v[72:75], v[24:27], v[32:35]\n"
"v_exp_f32 v42, v42\n"
"v_mfma_f32_16x16x32_bf16 v[32:35], v[76:79], v[28:31], v[32:35]\n"
"v_exp_f32 v43, v43\n"
"v_mfma_f32_16x16x32_bf16 v[36:39], v[80:83], v[16:19], -4.0\n"
"v_exp_f32 v44, v44\n"
"v_mfma_f32_16x16x32_bf16 v[36:39], v[84:87], v[20:23], v[36:39]\n"
"v_exp_f32 v45, v45\n"
"v_mfma_f32_16x16x32_bf16 v[36:39], v[88:91], v[24:27], v[36:39]\n"
"v_exp_f32 v46, v46\n"
"v_mfma_f32_16x16x32_bf16 v[36:39], v[92:95], v[28:31], v[36:39]\n"
"v_exp_f32 v47, v47\n"
"v_mfma_f32_16x16x32_bf16 a[20:23], v[60:63], v[48:51], 0\n"
"v_cvt_pk_bf16_f32 v52, v40, v41\n"
"v_cvt_pk_bf16_f32 v53, v42, v43\n"
"v_cvt_pk_bf16_f32 v54, v44, v45\n"
"v_cvt_pk_bf16_f32 v55, v46, v47\n"
"ds_read_b128 v[8:11], v98 offset:28672\n"
"ds_read_b128 v[12:15], v99 offset:28672\n"
"s_waitcnt lgkmcnt(3)\n"
"s_add_u32 s42, s42, s63\n"
"s_add_u32 m0, s54, 0x1000\n"
"s_nop 0\n"
"buffer_load_dwordx4 v104, s[24:27], s42 offen offset:0 nt lds\n"
"buffer_load_dwordx4 v105, s[24:27], s42 offen offset:1024 nt lds\n"
"buffer_load_dwordx4 v106, s[24:27], s42 offen offset:2048 nt lds\n"
"buffer_load_dwordx4 v107, s[24:27], s42 offen offset:3072 nt lds\n"
"ds_write_b128 v108, v[52:55] offset:16384\n"
"v_mfma_f32_16x16x32_bf16 v[40:43], a[32:35], v[16:19], -4.0\n"
"v_exp_f32 v32, v32\n"
"v_mfma_f32_16x16x32_bf16 v[40:43], a[36:39], v[20:23], v[40:43]\n"
"v_exp_f32 v33, v33\n"
"v_mfma_f32_16x16x32_bf16 v[40:43], a[40:43], v[24:27], v[40:43]\n"
"v_exp_f32 v34, v34\n"
"v_mfma_f32_16x16x32_bf16 v[40:43], a[44:47], v[28:31], v[40:43]\n"
"v_exp_f32 v35, v35\n"
"v_mfma_f32_16x16x32_bf16 v[44:47], a[48:51], v[16:19], -4.0\n"
"v_exp_f32 v36, v36\n"
"v_mfma_f32_16x16x32_bf16 v[44:47], a[52:55], v[20:23], v[44:47]\n"
"v_exp_f32 v37, v37\n"
"v_mfma_f32_16x16x32_bf16 v[44:47], a[56:59], v[24:27], v[44:47]\n"
"v_exp_f32 v38, v38\n"
"v_mfma_f32_16x16x32_bf16 v[44:47], a[60:63], v[28:31], v[44:47]\n"
"v_exp_f32 v39, v39\n"
"v_mfma_f32_16x16x32_bf16 a[20:23], v[60:63], v[52:55], a[20:23]\n"
"v_cvt_pk_bf16_f32 v48, v32, v33\n"
"v_cvt_pk_bf16_f32 v49, v34, v35\n"
"v_cvt_pk_bf16_f32 v50, v36, v37\n"
"v_cvt_pk_bf16_f32 v51, v38, v39\n"
"ds_write_b128 v108, v[48:51] offset:20480\n"
"s_waitcnt lgkmcnt(1)\n"
"s_barrier\n"
"v_mfma_f32_16x16x32_bf16 v[32:35], v[64:67], v[0:3], -4.0\n"
"v_exp_f32 v40, v40\n"
"v_mfma_f32_16x16x32_bf16 v[32:35], v[68:71], v[4:7], v[32:35]\n"
"v_exp_f32 v41, v41\n"
"v_mfma_f32_16x16x32_bf16 v[32:35], v[72:75], v[8:11], v[32:35]\n"
"v_exp_f32 v42, v42\n"
"v_mfma_f32_16x16x32_bf16 v[32:35], v[76:79], v[12:15], v[32:35]\n"
"v_exp_f32 v43, v43\n"
"v_mfma_f32_16x16x32_bf16 v[36:39], v[80:83], v[0:3], -4.0\n"
"v_exp_f32 v44, v44\n"
"v_mfma_f32_16x16x32_bf16 v[36:39], v[84:87], v[4:7], v[36:39]\n"
"v_exp_f32 v45, v45\n"
"v_mfma_f32_16x16x32_bf16 v[36:39], v[88:91], v[8:11], v[36:39]\n"
"v_exp_f32 v46, v46\n"
"v_mfma_f32_16x16x32_bf16 v[36:39], v[92:95], v[12:15], v[36:39]\n"
"v_exp_f32 v47, v47\n"
"v_mfma_f32_16x16x32_bf16 a[24:27], v[60:63], v[48:51], 0\n"
"v_cvt_pk_bf16_f32 v52, v40, v41\n"
"v_cvt_pk_bf16_f32 v53, v42, v43\n"
"v_cvt_pk_bf16_f32 v54, v44, v45\n"
"v_cvt_pk_bf16_f32 v55, v46, v47\n"
"ds_read_b128 v[16:19], v96 offset:0\n"
"ds_read_b128 v[20:23], v97 offset:0\n"
"ds_write_b128 v108, v[52:55] offset:24576\n"
"v_mfma_f32_16x16x32_bf16 v[40:43], a[32:35], v[0:3], -4.0\n"
"v_exp_f32 v32, v32\n"
"v_mfma_f32_16x16x32_bf16 v[40:43], a[36:39], v[4:7], v[40:43]\n"
"v_exp_f32 v33, v33\n"
"v_mfma_f32_16x16x32_bf16 v[40:43], a[40:43], v[8:11], v[40:43]\n"
"v_exp_f32 v34, v34\n"
"v_mfma_f32_16x16x32_bf16 v[40:43], a[44:47], v[12:15], v[40:43]\n"
"v_exp_f32 v35, v35\n"
"v_mfma_f32_16x16x32_bf16 v[44:47], a[48:51], v[0:3], -4.0\n"
"v_exp_f32 v36, v36\n"
"v_mfma_f32_16x16x32_bf16 v[44:47], a[52:55], v[4:7], v[44:47]\n"
"v_exp_f32 v37, v37\n"
"v_mfma_f32_16x16x32_bf16 v[44:47], a[56:59], v[8:11], v[44:47]\n"
"v_exp_f32 v38, v38\n"
"v_mfma_f32_16x16x32_bf16 v[44:47], a[60:63], v[12:15], v[44:47]\n"
"v_exp_f32 v39, v39\n"
"v_mfma_f32_16x16x32_bf16 a[24:27], v[60:63], v[52:55], a[24:27]\n"
"v_cvt_pk_bf16_f32 v48, v32, v33\n"
"v_cvt_pk_bf16_f32 v49, v34, v35\n"
"v_cvt_pk_bf16_f32 v50, v36, v37\n"
"v_cvt_pk_bf16_f32 v51, v38, v39\n"
"ds_read_b128 v[24:27], v98 offset:0\n"
"ds_read_b128 v[28:31], v99 offset:0\n"
"ds_read_b128 v[0:3], v96 offset:4096\n"
"ds_read_b128 v[4:7], v97 offset:4096\n"
"ds_read_b128 v[8:11], v98 offset:4096\n"
"ds_read_b128 v[12:15], v99 offset:4096\n"
"ds_write_b128 v108, v[48:51] offset:28672\n"
"s_waitcnt lgkmcnt(5)\n"
"v_mfma_f32_16x16x32_bf16 v[32:35], a[64:67], v[16:19], -4.0\n"
"v_exp_f32 v40, v40\n"
"v_mfma_f32_16x16x32_bf16 v[32:35], a[68:71], v[20:23], v[32:35]\n"
"v_exp_f32 v41, v41\n"
"v_mfma_f32_16x16x32_bf16 v[32:35], a[72:75], v[24:27], v[32:35]\n"
"v_exp_f32 v42, v42\n"
"v_mfma_f32_16x16x32_bf16 v[32:35], a[76:79], v[28:31], v[32:35]\n"
"v_exp_f32 v43, v43\n"
"v_mfma_f32_16x16x32_bf16 v[36:39], a[80:83], v[16:19], -4.0\n"
"v_exp_f32 v44, v44\n"
"v_mfma_f32_16x16x32_bf16 v[36:39], a[84:87], v[20:23], v[36:39]\n"
"v_exp_f32 v45, v45\n"
"v_mfma_f32_16x16x32_bf16 v[36:39], a[88:91], v[24:27], v[36:39]\n"
"v_exp_f32 v46, v46\n"
"v_mfma_f32_16x16x32_bf16 v[36:39], a[92:95], v[28:31], v[36:39]\n"
"v_exp_f32 v47, v47\n"
"v_mfma_f32_16x16x32_bf16 a[28:31], v[60:63], v[48:51], 0\n"
"v_cvt_pk_bf16_f32 v52, v40, v41\n"
"v_cvt_pk_bf16_f32 v53, v42, v43\n"
"v_cvt_pk_bf16_f32 v54, v44, v45\n"
"v_cvt_pk_bf16_f32 v55, v46, v47\n"
"ds_read_b128 v[16:19], v96 offset:8192\n"
"ds_read_b128 v[20:23], v97 offset:8192\n"
"ds_read_b128 v[24:27], v98 offset:8192\n"
"ds_read_b128 v[28:31], v99 offset:8192\n"
"ds_write_b128 v108, v[52:55] offset:32768\n"
"s_waitcnt lgkmcnt(6)\n"
"v_mfma_f32_16x16x32_bf16 v[40:43], a[64:67], v[0:3], -4.0\n"
"v_exp_f32 v32, v32\n"
"v_mfma_f32_16x16x32_bf16 v[40:43], a[68:71], v[4:7], v[40:43]\n"
"v_exp_f32 v33, v33\n"
"v_mfma_f32_16x16x32_bf16 v[40:43], a[72:75], v[8:11], v[40:43]\n"
"v_exp_f32 v34, v34\n"
"v_mfma_f32_16x16x32_bf16 v[40:43], a[76:79], v[12:15], v[40:43]\n"
"v_exp_f32 v35, v35\n"
"v_mfma_f32_16x16x32_bf16 v[44:47], a[80:83], v[0:3], -4.0\n"
"v_exp_f32 v36, v36\n"
"v_mfma_f32_16x16x32_bf16 v[44:47], a[84:87], v[4:7], v[44:47]\n"
"v_exp_f32 v37, v37\n"
"v_mfma_f32_16x16x32_bf16 v[44:47], a[88:91], v[8:11], v[44:47]\n"
"v_exp_f32 v38, v38\n"
"v_mfma_f32_16x16x32_bf16 v[44:47], a[92:95], v[12:15], v[44:47]\n"
"v_exp_f32 v39, v39\n"
"v_mfma_f32_16x16x32_bf16 a[28:31], v[60:63], v[52:55], a[28:31]\n"
"v_cvt_pk_bf16_f32 v48, v32, v33\n"
"v_cvt_pk_bf16_f32 v49, v34, v35\n"
"v_cvt_pk_bf16_f32 v50, v36, v37\n"
"v_cvt_pk_bf16_f32 v51, v38, v39\n"
"ds_read_b128 v[0:3], v96 offset:12288\n"
"ds_read_b128 v[4:7], v97 offset:12288\n"
"ds_read_b128 v[8:11], v98 offset:12288\n"
"ds_read_b128 v[12:15], v99 offset:12288\n"
"ds_write_b128 v108, v[48:51] offset:0\n"
"s_waitcnt lgkmcnt(5)\n"
"s_barrier\n"
"v_mfma_f32_16x16x32_bf16 v[32:35], a[64:67], v[16:19], -4.0\n"
"v_exp_f32 v40, v40\n"
"v_mfma_f32_16x16x32_bf16 v[32:35], a[68:71], v[20:23], v[32:35]\n"
"v_exp_f32 v41, v41\n"
"v_mfma_f32_16x16x32_bf16 v[32:35], a[72:75], v[24:27], v[32:35]\n"
"v_exp_f32 v42, v42\n"
"v_mfma_f32_16x16x32_bf16 v[32:35], a[76:79], v[28:31], v[32:35]\n"
"v_exp_f32 v43, v43\n"
"v_mfma_f32_16x16x32_bf16 v[36:39], a[80:83], v[16:19], -4.0\n"
"v_exp_f32 v44, v44\n"
"v_mfma_f32_16x16x32_bf16 v[36:39], a[84:87], v[20:23], v[36:39]\n"
"v_exp_f32 v45, v45\n"
"v_mfma_f32_16x16x32_bf16 v[36:39], a[88:91], v[24:27], v[36:39]\n"
"v_exp_f32 v46, v46\n"
"v_mfma_f32_16x16x32_bf16 v[36:39], a[92:95], v[28:31], v[36:39]\n"
"v_exp_f32 v47, v47\n"
"v_mfma_f32_16x16x32_bf16 a[0:3], v[60:63], v[48:51], a[0:3]\n"
"v_cvt_pk_bf16_f32 v52, v40, v41\n"
"v_cvt_pk_bf16_f32 v53, v42, v43\n"
"v_cvt_pk_bf16_f32 v54, v44, v45\n"
"v_cvt_pk_bf16_f32 v55, v46, v47\n"
"ds_read_b128 v[16:19], v96 offset:16384\n"
"ds_read_b128 v[20:23], v97 offset:16384\n"
"ds_read_b128 v[24:27], v98 offset:16384\n"
"ds_read_b128 v[28:31], v99 offset:16384\n"
"ds_write_b128 v108, v[52:55] offset:4096\n"
"s_waitcnt lgkmcnt(6)\n"
"v_mfma_f32_16x16x32_bf16 v[40:43], a[64:67], v[0:3], -4.0\n"
"v_exp_f32 v32, v32\n"
"v_mfma_f32_16x16x32_bf16 v[40:43], a[68:71], v[4:7], v[40:43]\n"
"v_exp_f32 v33, v33\n"
"v_mfma_f32_16x16x32_bf16 v[40:43], a[72:75], v[8:11], v[40:43]\n"
"v_exp_f32 v34, v34\n"
"v_mfma_f32_16x16x32_bf16 v[40:43], a[76:79], v[12:15], v[40:43]\n"
"v_exp_f32 v35, v35\n"
"v_mfma_f32_16x16x32_bf16 v[44:47], a[80:83], v[0:3], -4.0\n"
"v_exp_f32 v36, v36\n"
"v_mfma_f32_16x16x32_bf16 v[44:47], a[84:87], v[4:7], v[44:47]\n"
"v_exp_f32 v37, v37\n"
"v_mfma_f32_16x16x32_bf16 v[44:47], a[88:91], v[8:11], v[44:47]\n"
"v_exp_f32 v38, v38\n"
"v_mfma_f32_16x16x32_bf16 v[44:47], a[92:95], v[12:15], v[44:47]\n"
"v_exp_f32 v39, v39\n"
"v_mfma_f32_16x16x32_bf16 a[4:7], v[60:63], v[52:55], a[4:7]\n"
"v_cvt_pk_bf16_f32 v48, v32, v33\n"
"v_cvt_pk_bf16_f32 v49, v34, v35\n"
"v_cvt_pk_bf16_f32 v50, v36, v37\n"
"v_cvt_pk_bf16_f32 v51, v38, v39\n"
"ds_read_b128 v[0:3], v96 offset:20480\n"
"ds_read_b128 v[4:7], v97 offset:20480\n"
"ds_read_b128 v[8:11], v98 offset:20480\n"
"ds_read_b128 v[12:15], v99 offset:20480\n"
"ds_write_b128 v108, v[48:51] offset:8192\n"
"s_waitcnt lgkmcnt(6)\n"
"v_mfma_f32_16x16x32_bf16 v[32:35], a[64:67], v[16:19], -4.0\n"
"v_exp_f32 v40, v40\n"
"v_mfma_f32_16x16x32_bf16 v[32:35], a[68:71], v[20:23], v[32:35]\n"
"v_exp_f32 v41, v41\n"
"v_mfma_f32_16x16x32_bf16 v[32:35], a[72:75], v[24:27], v[32:35]\n"
"v_exp_f32 v42, v42\n"
"v_mfma_f32_16x16x32_bf16 v[32:35], a[76:79], v[28:31], v[32:35]\n"
"v_exp_f32 v43, v43\n"
"v_mfma_f32_16x16x32_bf16 v[36:39], a[80:83], v[16:19], -4.0\n"
"v_exp_f32 v44, v44\n"
"v_mfma_f32_16x16x32_bf16 v[36:39], a[84:87], v[20:23], v[36:39]\n"
"v_exp_f32 v45, v45\n"
"v_mfma_f32_16x16x32_bf16 v[36:39], a[88:91], v[24:27], v[36:39]\n"
"v_exp_f32 v46, v46\n"
"v_mfma_f32_16x16x32_bf16 v[36:39], a[92:95], v[28:31], v[36:39]\n"
"v_exp_f32 v47, v47\n"
"v_mfma_f32_16x16x32_bf16 a[8:11], v[60:63], v[48:51], a[8:11]\n"
"v_cvt_pk_bf16_f32 v52, v40, v41\n"
"v_cvt_pk_bf16_f32 v53, v42, v43\n"
"v_cvt_pk_bf16_f32 v54, v44, v45\n"
"v_cvt_pk_bf16_f32 v55, v46, v47\n"
"ds_read_b128 v[16:19], v96 offset:24576\n"
"ds_read_b128 v[20:23], v97 offset:24576\n"
"ds_read_b128 v[24:27], v98 offset:24576\n"
"ds_read_b128 v[28:31], v99 offset:24576\n"
"ds_write_b128 v108, v[52:55] offset:12288\n"
"s_waitcnt lgkmcnt(6)\n"
"v_mfma_f32_16x16x32_bf16 v[40:43], a[64:67], v[0:3], -4.0\n"
"v_exp_f32 v32, v32\n"
"v_mfma_f32_16x16x32_bf16 v[40:43], a[68:71], v[4:7], v[40:43]\n"
"v_exp_f32 v33, v33\n"
"v_mfma_f32_16x16x32_bf16 v[40:43], a[72:75], v[8:11], v[40:43]\n"
"v_exp_f32 v34, v34\n"
"v_mfma_f32_16x16x32_bf16 v[40:43], a[76:79], v[12:15], v[40:43]\n"
"v_exp_f32 v35, v35\n"
"v_mfma_f32_16x16x32_bf16 v[44:47], a[80:83], v[0:3], -4.0\n"
"v_exp_f32 v36, v36\n"
"v_mfma_f32_16x16x32_bf16 v[44:47], a[84:87], v[4:7], v[44:47]\n"
"v_exp_f32 v37, v37\n"
"v_mfma_f32_16x16x32_bf16 v[44:47], a[88:91], v[8:11], v[44:47]\n"
"v_exp_f32 v38, v38\n"
"v_mfma_f32_16x16x32_bf16 v[44:47], a[92:95], v[12:15], v[44:47]\n"
"v_exp_f32 v39, v39\n"
"v_mfma_f32_16x16x32_bf16 a[12:15], v[60:63], v[52:55], a[12:15]\n"
"v_cvt_pk_bf16_f32 v48, v32, v33\n"
"v_cvt_pk_bf16_f32 v49, v34, v35\n"
"v_cvt_pk_bf16_f32 v50, v36, v37\n"
"v_cvt_pk_bf16_f32 v51, v38, v39\n"
"ds_read_b128 v[0:3], v96 offset:28672\n"
"ds_read_b128 v[4:7], v97 offset:28672\n"
"ds_read_b128 v[8:11], v98 offset:28672\n"
"ds_read_b128 v[12:15], v99 offset:28672\n"
"ds_write_b128 v108, v[48:51] offset:16384\n"
"s_waitcnt lgkmcnt(5)\n"
"s_barrier\n"
"v_mfma_f32_16x16x32_bf16 v[32:35], a[64:67], v[16:19], -4.0\n"
"v_exp_f32 v40, v40\n"
"v_mfma_f32_16x16x32_bf16 v[32:35], a[68:71], v[20:23], v[32:35]\n"
"v_exp_f32 v41, v41\n"
"v_mfma_f32_16x16x32_bf16 v[32:35], a[72:75], v[24:27], v[32:35]\n"
"v_exp_f32 v42, v42\n"
"v_mfma_f32_16x16x32_bf16 v[32:35], a[76:79], v[28:31], v[32:35]\n"
"v_exp_f32 v43, v43\n"
"v_mfma_f32_16x16x32_bf16 v[36:39], a[80:83], v[16:19], -4.0\n"
"v_exp_f32 v44, v44\n"
"v_mfma_f32_16x16x32_bf16 v[36:39], a[84:87], v[20:23], v[36:39]\n"
"v_exp_f32 v45, v45\n"
"v_mfma_f32_16x16x32_bf16 v[36:39], a[88:91], v[24:27], v[36:39]\n"
"v_exp_f32 v46, v46\n"
"v_mfma_f32_16x16x32_bf16 v[36:39], a[92:95], v[28:31], v[36:39]\n"
"v_exp_f32 v47, v47\n"
"v_mfma_f32_16x16x32_bf16 a[16:19], v[60:63], v[48:51], a[16:19]\n"
"v_cvt_pk_bf16_f32 v52, v40, v41\n"
"v_cvt_pk_bf16_f32 v53, v42, v43\n"
"v_cvt_pk_bf16_f32 v54, v44, v45\n"
"v_cvt_pk_bf16_f32 v55, v46, v47\n"
"ds_read_b128 v[16:19], v96 offset:0\n"
"ds_read_b128 v[20:23], v97 offset:0\n"
"ds_read_b128 v[24:27], v98 offset:0\n"
"ds_read_b128 v[28:31], v99 offset:0\n"
"s_waitcnt vmcnt(4)\n"
"ds_read_b128 a[96:99], v100 offset:0\n"
"ds_read_b128 a[100:103], v101 offset:0\n"
"ds_read_b128 a[104:107], v102 offset:0\n"
"ds_read_b128 a[108:111], v103 offset:0\n"
"ds_write_b128 v108, v[52:55] offset:20480\n"
"s_waitcnt lgkmcnt(10)\n"
"v_mfma_f32_16x16x32_bf16 v[40:43], a[64:67], v[0:3], -4.0\n"
"v_exp_f32 v32, v32\n"
"v_mfma_f32_16x16x32_bf16 v[40:43], a[68:71], v[4:7], v[40:43]\n"
"v_exp_f32 v33, v33\n"
"v_mfma_f32_16x16x32_bf16 v[40:43], a[72:75], v[8:11], v[40:43]\n"
"v_exp_f32 v34, v34\n"
"v_mfma_f32_16x16x32_bf16 v[40:43], a[76:79], v[12:15], v[40:43]\n"
"v_exp_f32 v35, v35\n"
"v_mfma_f32_16x16x32_bf16 v[44:47], a[80:83], v[0:3], -4.0\n"
"v_exp_f32 v36, v36\n"
"v_mfma_f32_16x16x32_bf16 v[44:47], a[84:87], v[4:7], v[44:47]\n"
"v_exp_f32 v37, v37\n"
"v_mfma_f32_16x16x32_bf16 v[44:47], a[88:91], v[8:11], v[44:47]\n"
"v_exp_f32 v38, v38\n"
"v_mfma_f32_16x16x32_bf16 v[44:47], a[92:95], v[12:15], v[44:47]\n"
"v_exp_f32 v39, v39\n"
"v_mfma_f32_16x16x32_bf16 a[20:23], v[60:63], v[52:55], a[20:23]\n"
"v_cvt_pk_bf16_f32 v48, v32, v33\n"
"v_cvt_pk_bf16_f32 v49, v34, v35\n"
"v_cvt_pk_bf16_f32 v50, v36, v37\n"
"v_cvt_pk_bf16_f32 v51, v38, v39\n"
"ds_read_b128 v[0:3], v96 offset:4096\n"
"ds_read_b128 v[4:7], v97 offset:4096\n"
"ds_read_b128 v[8:11], v98 offset:4096\n"
"ds_read_b128 v[12:15], v99 offset:4096\n"
"s_waitcnt vmcnt(0)\n"
"ds_read_b128 a[112:115], v100 offset:4096\n"
"ds_read_b128 a[116:119], v101 offset:4096\n"
"ds_read_b128 a[120:123], v102 offset:4096\n"
"ds_read_b128 a[124:127], v103 offset:4096\n"
"ds_write_b128 v108, v[48:51] offset:24576\n"
"s_waitcnt lgkmcnt(13)\n"
"v_mfma_f32_16x16x32_bf16 v[32:35], a[96:99], v[16:19], -4.0\n"
"v_exp_f32 v40, v40\n"
"s_waitcnt lgkmcnt(12)\n"
"v_mfma_f32_16x16x32_bf16 v[32:35], a[100:103], v[20:23], v[32:35]\n"
"v_exp_f32 v41, v41\n"
"s_waitcnt lgkmcnt(11)\n"
"v_mfma_f32_16x16x32_bf16 v[32:35], a[104:107], v[24:27], v[32:35]\n"
"v_exp_f32 v42, v42\n"
"s_waitcnt lgkmcnt(10)\n"
"v_mfma_f32_16x16x32_bf16 v[32:35], a[108:111], v[28:31], v[32:35]\n"
"v_exp_f32 v43, v43\n"
"s_waitcnt lgkmcnt(4)\n"
"v_mfma_f32_16x16x32_bf16 v[36:39], a[112:115], v[16:19], -4.0\n"
"v_exp_f32 v44, v44\n"
"s_waitcnt lgkmcnt(3)\n"
"v_mfma_f32_16x16x32_bf16 v[36:39], a[116:119], v[20:23], v[36:39]\n"
"v_exp_f32 v45, v45\n"
"s_waitcnt lgkmcnt(2)\n"
"v_mfma_f32_16x16x32_bf16 v[36:39], a[120:123], v[24:27], v[36:39]\n"
"v_exp_f32 v46, v46\n"
"s_waitcnt lgkmcnt(1)\n"
"v_mfma_f32_16x16x32_bf16 v[36:39], a[124:127], v[28:31], v[36:39]\n"
"v_exp_f32 v47, v47\n"
"v_mfma_f32_16x16x32_bf16 a[24:27], v[60:63], v[48:51], a[24:27]\n"
"v_cvt_pk_bf16_f32 v52, v40, v41\n"
"v_cvt_pk_bf16_f32 v53, v42, v43\n"
"v_cvt_pk_bf16_f32 v54, v44, v45\n"
"v_cvt_pk_bf16_f32 v55, v46, v47\n"
"ds_read_b128 v[16:19], v96 offset:8192\n"
"ds_read_b128 v[20:23], v97 offset:8192\n"
"ds_read_b128 v[24:27], v98 offset:8192\n"
"ds_read_b128 v[28:31], v99 offset:8192\n"
"ds_write_b128 v108, v[52:55] offset:28672\n"
"v_mfma_f32_16x16x32_bf16 v[40:43], a[96:99], v[0:3], -4.0\n"
"v_exp_f32 v32, v32\n"
"v_mfma_f32_16x16x32_bf16 v[40:43], a[100:103], v[4:7], v[40:43]\n"
"v_exp_f32 v33, v33\n"
"v_mfma_f32_16x16x32_bf16 v[40:43], a[104:107], v[8:11], v[40:43]\n"
"v_exp_f32 v34, v34\n"
"v_mfma_f32_16x16x32_bf16 v[40:43], a[108:111], v[12:15], v[40:43]\n"
"v_exp_f32 v35, v35\n"
"v_mfma_f32_16x16x32_bf16 v[44:47], a[112:115], v[0:3], -4.0\n"
"v_exp_f32 v36, v36\n"
"v_mfma_f32_16x16x32_bf16 v[44:47], a[116:119], v[4:7], v[44:47]\n"
"v_exp_f32 v37, v37\n"
"v_mfma_f32_16x16x32_bf16 v[44:47], a[120:123], v[8:11], v[44:47]\n"
"v_exp_f32 v38, v38\n"
"v_mfma_f32_16x16x32_bf16 v[44:47], a[124:127], v[12:15], v[44:47]\n"
"v_exp_f32 v39, v39\n"
"v_mfma_f32_16x16x32_bf16 a[28:31], v[60:63], v[52:55], a[28:31]\n"
"v_cvt_pk_bf16_f32 v48, v32, v33\n"
"v_cvt_pk_bf16_f32 v49, v34, v35\n"
"v_cvt_pk_bf16_f32 v50, v36, v37\n"
"v_cvt_pk_bf16_f32 v51, v38, v39\n"
"ds_read_b128 v[0:3], v96 offset:12288\n"
"ds_read_b128 v[4:7], v97 offset:12288\n"
"ds_read_b128 v[8:11], v98 offset:12288\n"
"ds_read_b128 v[12:15], v99 offset:12288\n"
"ds_write_b128 v108, v[48:51] offset:32768\n"
"s_waitcnt lgkmcnt(5)\n"
"s_barrier\n"
"v_mfma_f32_16x16x32_bf16 v[32:35], a[96:99], v[16:19], -4.0\n"
"v_exp_f32 v40, v40\n"
"v_mfma_f32_16x16x32_bf16 v[32:35], a[100:103], v[20:23], v[32:35]\n"
"v_exp_f32 v41, v41\n"
"v_mfma_f32_16x16x32_bf16 v[32:35], a[104:107], v[24:27], v[32:35]\n"
"v_exp_f32 v42, v42\n"
"v_mfma_f32_16x16x32_bf16 v[32:35], a[108:111], v[28:31], v[32:35]\n"
"v_exp_f32 v43, v43\n"
"v_mfma_f32_16x16x32_bf16 v[36:39], a[112:115], v[16:19], -4.0\n"
"v_exp_f32 v44, v44\n"
"v_mfma_f32_16x16x32_bf16 v[36:39], a[116:119], v[20:23], v[36:39]\n"
"v_exp_f32 v45, v45\n"
"v_mfma_f32_16x16x32_bf16 v[36:39], a[120:123], v[24:27], v[36:39]\n"
"v_exp_f32 v46, v46\n"
"v_mfma_f32_16x16x32_bf16 v[36:39], a[124:127], v[28:31], v[36:39]\n"
"v_exp_f32 v47, v47\n"
"v_mfma_f32_16x16x32_bf16 a[0:3], v[60:63], v[48:51], a[0:3]\n"
"v_cvt_pk_bf16_f32 v52, v40, v41\n"
"v_cvt_pk_bf16_f32 v53, v42, v43\n"
"v_cvt_pk_bf16_f32 v54, v44, v45\n"
"v_cvt_pk_bf16_f32 v55, v46, v47\n"
"ds_read_b128 v[16:19], v96 offset:16384\n"
"ds_read_b128 v[20:23], v97 offset:16384\n"
"ds_read_b128 v[24:27], v98 offset:16384\n"
"ds_read_b128 v[28:31], v99 offset:16384\n"
"ds_write_b128 v108, v[52:55] offset:0\n"
"s_waitcnt lgkmcnt(6)\n"
"v_mfma_f32_16x16x32_bf16 v[40:43], a[96:99], v[0:3], -4.0\n"
"v_exp_f32 v32, v32\n"
"v_mfma_f32_16x16x32_bf16 v[40:43], a[100:103], v[4:7], v[40:43]\n"
"v_exp_f32 v33, v33\n"
"v_mfma_f32_16x16x32_bf16 v[40:43], a[104:107], v[8:11], v[40:43]\n"
"v_exp_f32 v34, v34\n"
"v_mfma_f32_16x16x32_bf16 v[40:43], a[108:111], v[12:15], v[40:43]\n"
"v_exp_f32 v35, v35\n"
"v_mfma_f32_16x16x32_bf16 v[44:47], a[112:115], v[0:3], -4.0\n"
"v_exp_f32 v36, v36\n"
"v_mfma_f32_16x16x32_bf16 v[44:47], a[116:119], v[4:7], v[44:47]\n"
"v_exp_f32 v37, v37\n"
"v_mfma_f32_16x16x32_bf16 v[44:47], a[120:123], v[8:11], v[44:47]\n"
"v_exp_f32 v38, v38\n"
"v_mfma_f32_16x16x32_bf16 v[44:47], a[124:127], v[12:15], v[44:47]\n"
"v_exp_f32 v39, v39\n"
"v_mfma_f32_16x16x32_bf16 a[4:7], v[60:63], v[52:55], a[4:7]\n"
"v_cvt_pk_bf16_f32 v48, v32, v33\n"
"v_cvt_pk_bf16_f32 v49, v34, v35\n"
"v_cvt_pk_bf16_f32 v50, v36, v37\n"
"v_cvt_pk_bf16_f32 v51, v38, v39\n"
"ds_read_b128 v[0:3], v96 offset:20480\n"
"ds_read_b128 v[4:7], v97 offset:20480\n"
"ds_read_b128 v[8:11], v98 offset:20480\n"
"ds_read_b128 v[12:15], v99 offset:20480\n"
"ds_write_b32 v109, a0 offset:0\n"
"ds_write_b128 v108, v[48:51] offset:4096\n"
"s_waitcnt lgkmcnt(7)\n"
"v_mfma_f32_16x16x32_bf16 v[32:35], a[96:99], v[16:19], -4.0\n"
"v_exp_f32 v40, v40\n"
"v_mfma_f32_16x16x32_bf16 v[32:35], a[100:103], v[20:23], v[32:35]\n"
"v_exp_f32 v41, v41\n"
"v_mfma_f32_16x16x32_bf16 v[32:35], a[104:107], v[24:27], v[32:35]\n"
"v_exp_f32 v42, v42\n"
"v_mfma_f32_16x16x32_bf16 v[32:35], a[108:111], v[28:31], v[32:35]\n"
"v_exp_f32 v43, v43\n"
"v_mfma_f32_16x16x32_bf16 v[36:39], a[112:115], v[16:19], -4.0\n"
"v_exp_f32 v44, v44\n"
"v_mfma_f32_16x16x32_bf16 v[36:39], a[116:119], v[20:23], v[36:39]\n"
"v_exp_f32 v45, v45\n"
"v_mfma_f32_16x16x32_bf16 v[36:39], a[120:123], v[24:27], v[36:39]\n"
"v_exp_f32 v46, v46\n"
"v_mfma_f32_16x16x32_bf16 v[36:39], a[124:127], v[28:31], v[36:39]\n"
"v_exp_f32 v47, v47\n"
"v_mfma_f32_16x16x32_bf16 a[8:11], v[60:63], v[48:51], a[8:11]\n"
"v_cvt_pk_bf16_f32 v52, v40, v41\n"
"v_cvt_pk_bf16_f32 v53, v42, v43\n"
"v_cvt_pk_bf16_f32 v54, v44, v45\n"
"v_cvt_pk_bf16_f32 v55, v46, v47\n"
"ds_read_b128 v[16:19], v96 offset:24576\n"
"ds_read_b128 v[20:23], v97 offset:24576\n"
"ds_read_b128 v[24:27], v98 offset:24576\n"
"ds_read_b128 v[28:31], v99 offset:24576\n"
"ds_write_b32 v109, a4 offset:1024\n"
"ds_write_b128 v108, v[52:55] offset:8192\n"
"s_waitcnt lgkmcnt(8)\n"
"v_mfma_f32_16x16x32_bf16 v[40:43], a[96:99], v[0:3], -4.0\n"
"v_exp_f32 v32, v32\n"
"v_mfma_f32_16x16x32_bf16 v[40:43], a[100:103], v[4:7], v[40:43]\n"
"v_exp_f32 v33, v33\n"
"v_mfma_f32_16x16x32_bf16 v[40:43], a[104:107], v[8:11], v[40:43]\n"
"v_exp_f32 v34, v34\n"
"v_mfma_f32_16x16x32_bf16 v[40:43], a[108:111], v[12:15], v[40:43]\n"
"v_exp_f32 v35, v35\n"
"v_mfma_f32_16x16x32_bf16 v[44:47], a[112:115], v[0:3], -4.0\n"
"v_exp_f32 v36, v36\n"
"v_mfma_f32_16x16x32_bf16 v[44:47], a[116:119], v[4:7], v[44:47]\n"
"v_exp_f32 v37, v37\n"
"v_mfma_f32_16x16x32_bf16 v[44:47], a[120:123], v[8:11], v[44:47]\n"
"v_exp_f32 v38, v38\n"
"v_mfma_f32_16x16x32_bf16 v[44:47], a[124:127], v[12:15], v[44:47]\n"
"v_exp_f32 v39, v39\n"
"v_mfma_f32_16x16x32_bf16 a[12:15], v[60:63], v[52:55], a[12:15]\n"
"v_cvt_pk_bf16_f32 v48, v32, v33\n"
"v_cvt_pk_bf16_f32 v49, v34, v35\n"
"v_cvt_pk_bf16_f32 v50, v36, v37\n"
"v_cvt_pk_bf16_f32 v51, v38, v39\n"
"ds_read_b128 v[0:3], v96 offset:28672\n"
"ds_read_b128 v[4:7], v97 offset:28672\n"
"ds_read_b128 v[8:11], v98 offset:28672\n"
"ds_read_b128 v[12:15], v99 offset:28672\n"
"ds_write_b32 v109, a8 offset:2048\n"
"ds_write_b128 v108, v[48:51] offset:12288\n"
"s_waitcnt lgkmcnt(6)\n"
"s_barrier\n"
"v_mfma_f32_16x16x32_bf16 v[32:35], a[96:99], v[16:19], -4.0\n"
"v_exp_f32 v40, v40\n"
"v_mfma_f32_16x16x32_bf16 v[32:35], a[100:103], v[20:23], v[32:35]\n"
"v_exp_f32 v41, v41\n"
"v_mfma_f32_16x16x32_bf16 v[32:35], a[104:107], v[24:27], v[32:35]\n"
"v_exp_f32 v42, v42\n"
"v_mfma_f32_16x16x32_bf16 v[32:35], a[108:111], v[28:31], v[32:35]\n"
"v_exp_f32 v43, v43\n"
"v_mfma_f32_16x16x32_bf16 v[36:39], a[112:115], v[16:19], -4.0\n"
"v_exp_f32 v44, v44\n"
"v_mfma_f32_16x16x32_bf16 v[36:39], a[116:119], v[20:23], v[36:39]\n"
"v_exp_f32 v45, v45\n"
"v_mfma_f32_16x16x32_bf16 v[36:39], a[120:123], v[24:27], v[36:39]\n"
"v_exp_f32 v46, v46\n"
"v_mfma_f32_16x16x32_bf16 v[36:39], a[124:127], v[28:31], v[36:39]\n"
"v_exp_f32 v47, v47\n"
"v_mfma_f32_16x16x32_bf16 a[16:19], v[60:63], v[48:51], a[16:19]\n"
"v_cvt_pk_bf16_f32 v52, v40, v41\n"
"v_cvt_pk_bf16_f32 v53, v42, v43\n"
"v_cvt_pk_bf16_f32 v54, v44, v45\n"
"v_cvt_pk_bf16_f32 v55, v46, v47\n"
"ds_read_b128 v[16:19], v96 offset:45056\n"
"ds_read_b128 v[20:23], v97 offset:45056\n"
"ds_read_b128 v[24:27], v98 offset:45056\n"
"ds_read_b128 v[28:31], v99 offset:45056\n"
"ds_write_b32 v109, a12 offset:3072\n"
"ds_write_b128 v108, v[52:55] offset:16384\n"
"s_waitcnt lgkmcnt(8)\n"
"v_mfma_f32_16x16x32_bf16 v[40:43], a[96:99], v[0:3], -4.0\n"
"v_exp_f32 v32, v32\n"
"v_mfma_f32_16x16x32_bf16 v[40:43], a[100:103], v[4:7], v[40:43]\n"
"v_exp_f32 v33, v33\n"
"v_mfma_f32_16x16x32_bf16 v[40:43], a[104:107], v[8:11], v[40:43]\n"
"v_exp_f32 v34, v34\n"
"v_mfma_f32_16x16x32_bf16 v[40:43], a[108:111], v[12:15], v[40:43]\n"
"v_exp_f32 v35, v35\n"
"v_mfma_f32_16x16x32_bf16 v[44:47], a[112:115], v[0:3], -4.0\n"
"v_exp_f32 v36, v36\n"
"v_mfma_f32_16x16x32_bf16 v[44:47], a[116:119], v[4:7], v[44:47]\n"
"v_exp_f32 v37, v37\n"
"v_mfma_f32_16x16x32_bf16 v[44:47], a[120:123], v[8:11], v[44:47]\n"
"v_exp_f32 v38, v38\n"
"v_mfma_f32_16x16x32_bf16 v[44:47], a[124:127], v[12:15], v[44:47]\n"
"v_exp_f32 v39, v39\n"
"v_mfma_f32_16x16x32_bf16 a[20:23], v[60:63], v[52:55], a[20:23]\n"
"v_cvt_pk_bf16_f32 v48, v32, v33\n"
"v_cvt_pk_bf16_f32 v49, v34, v35\n"
"v_cvt_pk_bf16_f32 v50, v36, v37\n"
"v_cvt_pk_bf16_f32 v51, v38, v39\n"
"ds_write_b32 v109, a16 offset:4096\n"
"ds_write_b128 v108, v[48:51] offset:20480\n"
"s_waitcnt lgkmcnt(4)\n"
"v_mfma_f32_16x16x32_bf16 v[32:35], v[64:67], v[16:19], -4.0\n"
"v_exp_f32 v40, v40\n"
"v_mfma_f32_16x16x32_bf16 v[32:35], v[68:71], v[20:23], v[32:35]\n"
"v_exp_f32 v41, v41\n"
"v_mfma_f32_16x16x32_bf16 v[32:35], v[72:75], v[24:27], v[32:35]\n"
"v_exp_f32 v42, v42\n"
"v_mfma_f32_16x16x32_bf16 v[32:35], v[76:79], v[28:31], v[32:35]\n"
"v_exp_f32 v43, v43\n"
"v_mfma_f32_16x16x32_bf16 v[36:39], v[80:83], v[16:19], -4.0\n"
"v_exp_f32 v44, v44\n"
"v_mfma_f32_16x16x32_bf16 v[36:39], v[84:87], v[20:23], v[36:39]\n"
"v_exp_f32 v45, v45\n"
"v_mfma_f32_16x16x32_bf16 v[36:39], v[88:91], v[24:27], v[36:39]\n"
"v_exp_f32 v46, v46\n"
"v_mfma_f32_16x16x32_bf16 v[36:39], v[92:95], v[28:31], v[36:39]\n"
"v_exp_f32 v47, v47\n"
"v_mfma_f32_16x16x32_bf16 a[24:27], v[60:63], v[48:51], a[24:27]\n"
"v_cvt_pk_bf16_f32 v52, v40, v41\n"
"v_cvt_pk_bf16_f32 v53, v42, v43\n"
"v_cvt_pk_bf16_f32 v54, v44, v45\n"
"v_cvt_pk_bf16_f32 v55, v46, v47\n"
"ds_write_b32 v109, a20 offset:5120\n"
"ds_write_b128 v108, v[52:55] offset:24576\n"
"v_mfma_f32_16x16x32_bf16 v[40:43], a[32:35], v[16:19], -4.0\n"
"v_exp_f32 v32, v32\n"
"v_mfma_f32_16x16x32_bf16 v[40:43], a[36:39], v[20:23], v[40:43]\n"
"v_exp_f32 v33, v33\n"
"v_mfma_f32_16x16x32_bf16 v[40:43], a[40:43], v[24:27], v[40:43]\n"
"v_exp_f32 v34, v34\n"
"v_mfma_f32_16x16x32_bf16 v[40:43], a[44:47], v[28:31], v[40:43]\n"
"v_exp_f32 v35, v35\n"
"v_mfma_f32_16x16x32_bf16 v[44:47], a[48:51], v[16:19], -4.0\n"
"v_exp_f32 v36, v36\n"
"v_mfma_f32_16x16x32_bf16 v[44:47], a[52:55], v[20:23], v[44:47]\n"
"v_exp_f32 v37, v37\n"
"v_mfma_f32_16x16x32_bf16 v[44:47], a[56:59], v[24:27], v[44:47]\n"
"v_exp_f32 v38, v38\n"
"v_mfma_f32_16x16x32_bf16 v[44:47], a[60:63], v[28:31], v[44:47]\n"
"v_exp_f32 v39, v39\n"
"v_mfma_f32_16x16x32_bf16 a[28:31], v[60:63], v[52:55], a[28:31]\n"
"v_cvt_pk_bf16_f32 v48, v32, v33\n"
"v_cvt_pk_bf16_f32 v49, v34, v35\n"
"v_cvt_pk_bf16_f32 v50, v36, v37\n"
"v_cvt_pk_bf16_f32 v51, v38, v39\n"
"ds_write_b32 v109, a24 offset:6144\n"
"ds_write_b128 v108, v[48:51] offset:28672\n"
"s_waitcnt lgkmcnt(2)\n"
"s_barrier\n"
"v_mfma_f32_16x16x32_bf16 v[32:35], a[64:67], v[16:19], -4.0\n"
"v_exp_f32 v40, v40\n"
"v_mfma_f32_16x16x32_bf16 v[32:35], a[68:71], v[20:23], v[32:35]\n"
"v_exp_f32 v41, v41\n"
"v_mfma_f32_16x16x32_bf16 v[32:35], a[72:75], v[24:27], v[32:35]\n"
"v_exp_f32 v42, v42\n"
"v_mfma_f32_16x16x32_bf16 v[32:35], a[76:79], v[28:31], v[32:35]\n"
"v_exp_f32 v43, v43\n"
"v_mfma_f32_16x16x32_bf16 v[36:39], a[80:83], v[16:19], -4.0\n"
"v_exp_f32 v44, v44\n"
"v_mfma_f32_16x16x32_bf16 v[36:39], a[84:87], v[20:23], v[36:39]\n"
"v_exp_f32 v45, v45\n"
"v_mfma_f32_16x16x32_bf16 v[36:39], a[88:91], v[24:27], v[36:39]\n"
"v_exp_f32 v46, v46\n"
"v_mfma_f32_16x16x32_bf16 v[36:39], a[92:95], v[28:31], v[36:39]\n"
"v_exp_f32 v47, v47\n"
"v_mfma_f32_16x16x32_bf16 v[116:119], v[60:63], v[48:51], 0\n"
"v_cvt_pk_bf16_f32 v52, v40, v41\n"
"v_cvt_pk_bf16_f32 v53, v42, v43\n"
"v_cvt_pk_bf16_f32 v54, v44, v45\n"
"v_cvt_pk_bf16_f32 v55, v46, v47\n"
"ds_read_b128 v[0:3], v96 offset:32768\n"
"ds_read_b128 v[4:7], v97 offset:32768\n"
"ds_write_b128 v108, v[52:55] offset:32768\n"
"v_mfma_f32_16x16x32_bf16 v[40:43], a[96:99], v[16:19], -4.0\n"
"v_exp_f32 v32, v32\n"
"v_mfma_f32_16x16x32_bf16 v[40:43], a[100:103], v[20:23], v[40:43]\n"
"v_exp_f32 v33, v33\n"
"v_mfma_f32_16x16x32_bf16 v[40:43], a[104:107], v[24:27], v[40:43]\n"
"v_exp_f32 v34, v34\n"
"v_mfma_f32_16x16x32_bf16 v[40:43], a[108:111], v[28:31], v[40:43]\n"
"v_exp_f32 v35, v35\n"
"v_mfma_f32_16x16x32_bf16 v[44:47], a[112:115], v[16:19], -4.0\n"
"v_exp_f32 v36, v36\n"
"v_mfma_f32_16x16x32_bf16 v[44:47], a[116:119], v[20:23], v[44:47]\n"
"v_exp_f32 v37, v37\n"
"v_mfma_f32_16x16x32_bf16 v[44:47], a[120:123], v[24:27], v[44:47]\n"
"v_exp_f32 v38, v38\n"
"v_mfma_f32_16x16x32_bf16 v[44:47], a[124:127], v[28:31], v[44:47]\n"
"v_exp_f32 v39, v39\n"
"v_mfma_f32_16x16x32_bf16 v[116:119], v[60:63], v[52:55], v[116:119]\n"
"v_cvt_pk_bf16_f32 v48, v32, v33\n"
"v_cvt_pk_bf16_f32 v49, v34, v35\n"
"v_cvt_pk_bf16_f32 v50, v36, v37\n"
"v_cvt_pk_bf16_f32 v51, v38, v39\n"
"ds_read_b128 v[8:11], v98 offset:32768\n"
"ds_read_b128 v[12:15], v99 offset:32768\n"
"ds_write_b128 v108, v[48:51] offset:0\n"
"s_waitcnt lgkmcnt(1)\n"
"v_mfma_f32_16x16x32_bf16 v[32:35], v[64:67], v[0:3], -4.0\n"
"v_exp_f32 v40, v40\n"
"v_mfma_f32_16x16x32_bf16 v[32:35], v[68:71], v[4:7], v[32:35]\n"
"v_exp_f32 v41, v41\n"
"v_mfma_f32_16x16x32_bf16 v[32:35], v[72:75], v[8:11], v[32:35]\n"
"v_exp_f32 v42, v42\n"
"v_mfma_f32_16x16x32_bf16 v[32:35], v[76:79], v[12:15], v[32:35]\n"
"v_exp_f32 v43, v43\n"
"v_mfma_f32_16x16x32_bf16 v[36:39], v[80:83], v[0:3], -4.0\n"
"v_exp_f32 v44, v44\n"
"v_mfma_f32_16x16x32_bf16 v[36:39], v[84:87], v[4:7], v[36:39]\n"
"v_exp_f32 v45, v45\n"
"v_mfma_f32_16x16x32_bf16 v[36:39], v[88:91], v[8:11], v[36:39]\n"
"v_exp_f32 v46, v46\n"
"v_mfma_f32_16x16x32_bf16 v[36:39], v[92:95], v[12:15], v[36:39]\n"
"v_exp_f32 v47, v47\n"
"v_mfma_f32_16x16x32_bf16 v[116:119], v[60:63], v[48:51], v[116:119]\n"
"v_cvt_pk_bf16_f32 v52, v40, v41\n"
"v_cvt_pk_bf16_f32 v53, v42, v43\n"
"v_cvt_pk_bf16_f32 v54, v44, v45\n"
"v_cvt_pk_bf16_f32 v55, v46, v47\n"
"ds_write_b32 v109, a28 offset:7168\n"
"ds_write_b128 v108, v[52:55] offset:4096\n"
"v_mfma_f32_16x16x32_bf16 v[40:43], a[32:35], v[0:3], -4.0\n"
"v_exp_f32 v32, v32\n"
"v_mfma_f32_16x16x32_bf16 v[40:43], a[36:39], v[4:7], v[40:43]\n"
"v_exp_f32 v33, v33\n"
"v_mfma_f32_16x16x32_bf16 v[40:43], a[40:43], v[8:11], v[40:43]\n"
"v_exp_f32 v34, v34\n"
"v_mfma_f32_16x16x32_bf16 v[40:43], a[44:47], v[12:15], v[40:43]\n"
"v_exp_f32 v35, v35\n"
"v_mfma_f32_16x16x32_bf16 v[44:47], a[48:51], v[0:3], -4.0\n"
"v_exp_f32 v36, v36\n"
"v_mfma_f32_16x16x32_bf16 v[44:47], a[52:55], v[4:7], v[44:47]\n"
"v_exp_f32 v37, v37\n"
"v_mfma_f32_16x16x32_bf16 v[44:47], a[56:59], v[8:11], v[44:47]\n"
"v_exp_f32 v38, v38\n"
"v_mfma_f32_16x16x32_bf16 v[44:47], a[60:63], v[12:15], v[44:47]\n"
"v_exp_f32 v39, v39\n"
"v_mfma_f32_16x16x32_bf16 v[116:119], v[60:63], v[52:55], v[116:119]\n"
"v_cvt_pk_bf16_f32 v48, v32, v33\n"
"v_cvt_pk_bf16_f32 v49, v34, v35\n"
"v_cvt_pk_bf16_f32 v50, v36, v37\n"
"v_cvt_pk_bf16_f32 v51, v38, v39\n"
"ds_write_b128 v108, v[48:51] offset:8192\n"
"s_waitcnt lgkmcnt(1)\n"
"s_barrier\n"
"v_mfma_f32_16x16x32_bf16 v[32:35], a[64:67], v[0:3], -4.0\n"
"v_exp_f32 v40, v40\n"
"v_mfma_f32_16x16x32_bf16 v[32:35], a[68:71], v[4:7], v[32:35]\n"
"v_exp_f32 v41, v41\n"
"v_mfma_f32_16x16x32_bf16 v[32:35], a[72:75], v[8:11], v[32:35]\n"
"v_exp_f32 v42, v42\n"
"v_mfma_f32_16x16x32_bf16 v[32:35], a[76:79], v[12:15], v[32:35]\n"
"v_exp_f32 v43, v43\n"
"v_mfma_f32_16x16x32_bf16 v[36:39], a[80:83], v[0:3], -4.0\n"
"v_exp_f32 v44, v44\n"
"v_mfma_f32_16x16x32_bf16 v[36:39], a[84:87], v[4:7], v[36:39]\n"
"v_exp_f32 v45, v45\n"
"v_mfma_f32_16x16x32_bf16 v[36:39], a[88:91], v[8:11], v[36:39]\n"
"v_exp_f32 v46, v46\n"
"v_mfma_f32_16x16x32_bf16 v[36:39], a[92:95], v[12:15], v[36:39]\n"
"v_exp_f32 v47, v47\n"
"v_mfma_f32_16x16x32_bf16 v[56:59], v[60:63], v[48:51], 0\n"
"v_cvt_pk_bf16_f32 v52, v40, v41\n"
"v_cvt_pk_bf16_f32 v53, v42, v43\n"
"v_cvt_pk_bf16_f32 v54, v44, v45\n"
"v_cvt_pk_bf16_f32 v55, v46, v47\n"
"ds_read_b128 v[16:19], v96 offset:36864\n"
"ds_read_b128 v[20:23], v97 offset:36864\n"
"ds_write_b128 v108, v[52:55] offset:12288\n"
"v_mfma_f32_16x16x32_bf16 v[40:43], a[96:99], v[0:3], -4.0\n"
"v_exp_f32 v32, v32\n"
"v_mfma_f32_16x16x32_bf16 v[40:43], a[100:103], v[4:7], v[40:43]\n"
"v_exp_f32 v33, v33\n"
"v_mfma_f32_16x16x32_bf16 v[40:43], a[104:107], v[8:11], v[40:43]\n"
"v_exp_f32 v34, v34\n"
"v_mfma_f32_16x16x32_bf16 v[40:43], a[108:111], v[12:15], v[40:43]\n"
"v_exp_f32 v35, v35\n"
"v_mfma_f32_16x16x32_bf16 v[44:47], a[112:115], v[0:3], -4.0\n"
"v_exp_f32 v36, v36\n"
"v_mfma_f32_16x16x32_bf16 v[44:47], a[116:119], v[4:7], v[44:47]\n"
"v_exp_f32 v37, v37\n"
"v_mfma_f32_16x16x32_bf16 v[44:47], a[120:123], v[8:11], v[44:47]\n"
"v_exp_f32 v38, v38\n"
"v_mfma_f32_16x16x32_bf16 v[44:47], a[124:127], v[12:15], v[44:47]\n"
"v_exp_f32 v39, v39\n"
"v_mfma_f32_16x16x32_bf16 v[56:59], v[60:63], v[52:55], v[56:59]\n"
"v_cvt_pk_bf16_f32 v48, v32, v33\n"
"v_cvt_pk_bf16_f32 v49, v34, v35\n"
"v_cvt_pk_bf16_f32 v50, v36, v37\n"
"v_cvt_pk_bf16_f32 v51, v38, v39\n"
"ds_read_b128 v[24:27], v98 offset:36864\n"
"ds_read_b128 v[28:31], v99 offset:36864\n"
"ds_write_b128 v108, v[48:51] offset:16384\n"
"s_waitcnt lgkmcnt(1)\n"
"v_mfma_f32_16x16x32_bf16 v[32:35], v[64:67], v[16:19], -4.0\n"
"v_exp_f32 v40, v40\n"
"v_mfma_f32_16x16x32_bf16 v[32:35], v[68:71], v[20:23], v[32:35]\n"
"v_exp_f32 v41, v41\n"
"v_mfma_f32_16x16x32_bf16 v[32:35], v[72:75], v[24:27], v[32:35]\n"
"v_exp_f32 v42, v42\n"
"v_mfma_f32_16x16x32_bf16 v[32:35], v[76:79], v[28:31], v[32:35]\n"
"v_exp_f32 v43, v43\n"
"v_mfma_f32_16x16x32_bf16 v[36:39], v[80:83], v[16:19], -4.0\n"
"v_exp_f32 v44, v44\n"
"v_mfma_f32_16x16x32_bf16 v[36:39], v[84:87], v[20:23], v[36:39]\n"
"v_exp_f32 v45, v45\n"
"v_mfma_f32_16x16x32_bf16 v[36:39], v[88:91], v[24:27], v[36:39]\n"
"v_exp_f32 v46, v46\n"
"v_mfma_f32_16x16x32_bf16 v[36:39], v[92:95], v[28:31], v[36:39]\n"
"v_exp_f32 v47, v47\n"
"v_mfma_f32_16x16x32_bf16 v[56:59], v[60:63], v[48:51], v[56:59]\n"
"v_cvt_pk_bf16_f32 v52, v40, v41\n"
"v_cvt_pk_bf16_f32 v53, v42, v43\n"
"v_cvt_pk_bf16_f32 v54, v44, v45\n"
"v_cvt_pk_bf16_f32 v55, v46, v47\n"
"ds_write_b32 v109, v116 offset:9216\n"
"ds_write_b128 v108, v[52:55] offset:20480\n"
"v_mfma_f32_16x16x32_bf16 v[40:43], a[32:35], v[16:19], -4.0\n"
"v_exp_f32 v32, v32\n"
"v_mfma_f32_16x16x32_bf16 v[40:43], a[36:39], v[20:23], v[40:43]\n"
"v_exp_f32 v33, v33\n"
"v_mfma_f32_16x16x32_bf16 v[40:43], a[40:43], v[24:27], v[40:43]\n"
"v_exp_f32 v34, v34\n"
"v_mfma_f32_16x16x32_bf16 v[40:43], a[44:47], v[28:31], v[40:43]\n"
"v_exp_f32 v35, v35\n"
"v_mfma_f32_16x16x32_bf16 v[44:47], a[48:51], v[16:19], -4.0\n"
"v_exp_f32 v36, v36\n"
"v_mfma_f32_16x16x32_bf16 v[44:47], a[52:55], v[20:23], v[44:47]\n"
"v_exp_f32 v37, v37\n"
"v_mfma_f32_16x16x32_bf16 v[44:47], a[56:59], v[24:27], v[44:47]\n"
"v_exp_f32 v38, v38\n"
"v_mfma_f32_16x16x32_bf16 v[44:47], a[60:63], v[28:31], v[44:47]\n"
"v_exp_f32 v39, v39\n"
"v_mfma_f32_16x16x32_bf16 v[56:59], v[60:63], v[52:55], v[56:59]\n"
"v_cvt_pk_bf16_f32 v48, v32, v33\n"
"v_cvt_pk_bf16_f32 v49, v34, v35\n"
"v_cvt_pk_bf16_f32 v50, v36, v37\n"
"v_cvt_pk_bf16_f32 v51, v38, v39\n"
"ds_write_b128 v108, v[48:51] offset:24576\n"
"s_waitcnt lgkmcnt(1)\n"
"s_barrier\n"
"v_mfma_f32_16x16x32_bf16 v[32:35], a[64:67], v[16:19], -4.0\n"
"v_exp_f32 v40, v40\n"
"v_mfma_f32_16x16x32_bf16 v[32:35], a[68:71], v[20:23], v[32:35]\n"
"v_exp_f32 v41, v41\n"
"v_mfma_f32_16x16x32_bf16 v[32:35], a[72:75], v[24:27], v[32:35]\n"
"v_exp_f32 v42, v42\n"
"v_mfma_f32_16x16x32_bf16 v[32:35], a[76:79], v[28:31], v[32:35]\n"
"v_exp_f32 v43, v43\n"
"v_mfma_f32_16x16x32_bf16 v[36:39], a[80:83], v[16:19], -4.0\n"
"v_exp_f32 v44, v44\n"
"v_mfma_f32_16x16x32_bf16 v[36:39], a[84:87], v[20:23], v[36:39]\n"
"v_exp_f32 v45, v45\n"
"v_mfma_f32_16x16x32_bf16 v[36:39], a[88:91], v[24:27], v[36:39]\n"
"v_exp_f32 v46, v46\n"
"v_mfma_f32_16x16x32_bf16 v[36:39], a[92:95], v[28:31], v[36:39]\n"
"v_exp_f32 v47, v47\n"
"v_mfma_f32_16x16x32_bf16 v[116:119], v[60:63], v[48:51], 0\n"
"v_cvt_pk_bf16_f32 v52, v40, v41\n"
"v_cvt_pk_bf16_f32 v53, v42, v43\n"
"v_cvt_pk_bf16_f32 v54, v44, v45\n"
"v_cvt_pk_bf16_f32 v55, v46, v47\n"
"ds_read_b128 v[0:3], v96 offset:40960\n"
"ds_read_b128 v[4:7], v97 offset:40960\n"
"ds_write_b128 v108, v[52:55] offset:28672\n"
"v_mfma_f32_16x16x32_bf16 v[40:43], a[96:99], v[16:19], -4.0\n"
"v_exp_f32 v32, v32\n"
"v_mfma_f32_16x16x32_bf16 v[40:43], a[100:103], v[20:23], v[40:43]\n"
"v_exp_f32 v33, v33\n"
"v_mfma_f32_16x16x32_bf16 v[40:43], a[104:107], v[24:27], v[40:43]\n"
"v_exp_f32 v34, v34\n"
"v_mfma_f32_16x16x32_bf16 v[40:43], a[108:111], v[28:31], v[40:43]\n"
"v_exp_f32 v35, v35\n"
"v_mfma_f32_16x16x32_bf16 v[44:47], a[112:115], v[16:19], -4.0\n"
"v_exp_f32 v36, v36\n"
"v_mfma_f32_16x16x32_bf16 v[44:47], a[116:119], v[20:23], v[44:47]\n"
"v_exp_f32 v37, v37\n"
"v_mfma_f32_16x16x32_bf16 v[44:47], a[120:123], v[24:27], v[44:47]\n"
"v_exp_f32 v38, v38\n"
"v_mfma_f32_16x16x32_bf16 v[44:47], a[124:127], v[28:31], v[44:47]\n"
"v_exp_f32 v39, v39\n"
"v_mfma_f32_16x16x32_bf16 v[116:119], v[60:63], v[52:55], v[116:119]\n"
"v_cvt_pk_bf16_f32 v48, v32, v33\n"
"v_cvt_pk_bf16_f32 v49, v34, v35\n"
"v_cvt_pk_bf16_f32 v50, v36, v37\n"
"v_cvt_pk_bf16_f32 v51, v38, v39\n"
"ds_read_b128 v[8:11], v98 offset:40960\n"
"ds_read_b128 v[12:15], v99 offset:40960\n"
"ds_write_b128 v108, v[48:51] offset:32768\n"
"s_waitcnt lgkmcnt(1)\n"
"v_mfma_f32_16x16x32_bf16 v[32:35], v[64:67], v[0:3], -4.0\n"
"v_exp_f32 v40, v40\n"
"v_mfma_f32_16x16x32_bf16 v[32:35], v[68:71], v[4:7], v[32:35]\n"
"v_exp_f32 v41, v41\n"
"v_mfma_f32_16x16x32_bf16 v[32:35], v[72:75], v[8:11], v[32:35]\n"
"v_exp_f32 v42, v42\n"
"v_mfma_f32_16x16x32_bf16 v[32:35], v[76:79], v[12:15], v[32:35]\n"
"v_exp_f32 v43, v43\n"
"v_mfma_f32_16x16x32_bf16 v[36:39], v[80:83], v[0:3], -4.0\n"
"v_exp_f32 v44, v44\n"
"v_mfma_f32_16x16x32_bf16 v[36:39], v[84:87], v[4:7], v[36:39]\n"
"v_exp_f32 v45, v45\n"
"v_mfma_f32_16x16x32_bf16 v[36:39], v[88:91], v[8:11], v[36:39]\n"
"v_exp_f32 v46, v46\n"
"v_mfma_f32_16x16x32_bf16 v[36:39], v[92:95], v[12:15], v[36:39]\n"
"v_exp_f32 v47, v47\n"
"v_mfma_f32_16x16x32_bf16 v[116:119], v[60:63], v[48:51], v[116:119]\n"
"v_cvt_pk_bf16_f32 v52, v40, v41\n"
"v_cvt_pk_bf16_f32 v53, v42, v43\n"
"v_cvt_pk_bf16_f32 v54, v44, v45\n"
"v_cvt_pk_bf16_f32 v55, v46, v47\n"
"ds_write_b32 v109, v56 offset:10240\n"
"ds_write_b128 v108, v[52:55] offset:0\n"
"v_mfma_f32_16x16x32_bf16 v[40:43], a[32:35], v[0:3], -4.0\n"
"v_exp_f32 v32, v32\n"
"v_mfma_f32_16x16x32_bf16 v[40:43], a[36:39], v[4:7], v[40:43]\n"
"v_exp_f32 v33, v33\n"
"v_mfma_f32_16x16x32_bf16 v[40:43], a[40:43], v[8:11], v[40:43]\n"
"v_exp_f32 v34, v34\n"
"v_mfma_f32_16x16x32_bf16 v[40:43], a[44:47], v[12:15], v[40:43]\n"
"v_exp_f32 v35, v35\n"
"v_mfma_f32_16x16x32_bf16 v[44:47], a[48:51], v[0:3], -4.0\n"
"v_exp_f32 v36, v36\n"
"v_mfma_f32_16x16x32_bf16 v[44:47], a[52:55], v[4:7], v[44:47]\n"
"v_exp_f32 v37, v37\n"
"v_mfma_f32_16x16x32_bf16 v[44:47], a[56:59], v[8:11], v[44:47]\n"
"v_exp_f32 v38, v38\n"
"v_mfma_f32_16x16x32_bf16 v[44:47], a[60:63], v[12:15], v[44:47]\n"
"v_exp_f32 v39, v39\n"
"v_mfma_f32_16x16x32_bf16 v[116:119], v[60:63], v[52:55], v[116:119]\n"
"v_cvt_pk_bf16_f32 v48, v32, v33\n"
"v_cvt_pk_bf16_f32 v49, v34, v35\n"
"v_cvt_pk_bf16_f32 v50, v36, v37\n"
"v_cvt_pk_bf16_f32 v51, v38, v39\n"
"ds_write_b128 v108, v[48:51] offset:4096\n"
"s_waitcnt lgkmcnt(1)\n"
"s_barrier\n"
"v_mfma_f32_16x16x32_bf16 v[32:35], a[64:67], v[0:3], -4.0\n"
"v_exp_f32 v40, v40\n"
"v_mfma_f32_16x16x32_bf16 v[32:35], a[68:71], v[4:7], v[32:35]\n"
"v_exp_f32 v41, v41\n"
"v_mfma_f32_16x16x32_bf16 v[32:35], a[72:75], v[8:11], v[32:35]\n"
"v_exp_f32 v42, v42\n"
"v_mfma_f32_16x16x32_bf16 v[32:35], a[76:79], v[12:15], v[32:35]\n"
"v_exp_f32 v43, v43\n"
"v_mfma_f32_16x16x32_bf16 v[36:39], a[80:83], v[0:3], -4.0\n"
"v_exp_f32 v44, v44\n"
"v_mfma_f32_16x16x32_bf16 v[36:39], a[84:87], v[4:7], v[36:39]\n"
"v_exp_f32 v45, v45\n"
"v_mfma_f32_16x16x32_bf16 v[36:39], a[88:91], v[8:11], v[36:39]\n"
"v_exp_f32 v46, v46\n"
"v_mfma_f32_16x16x32_bf16 v[36:39], a[92:95], v[12:15], v[36:39]\n"
"v_exp_f32 v47, v47\n"
"v_mfma_f32_16x16x32_bf16 v[56:59], v[60:63], v[48:51], 0\n"
"v_cvt_pk_bf16_f32 v52, v40, v41\n"
"v_cvt_pk_bf16_f32 v53, v42, v43\n"
"v_cvt_pk_bf16_f32 v54, v44, v45\n"
"v_cvt_pk_bf16_f32 v55, v46, v47\n"
"ds_read_b128 v[16:19], v96 offset:45056\n"
"ds_read_b128 v[20:23], v97 offset:45056\n"
"ds_write_b128 v108, v[52:55] offset:8192\n"
"v_mfma_f32_16x16x32_bf16 v[40:43], a[96:99], v[0:3], -4.0\n"
"v_exp_f32 v32, v32\n"
"v_mfma_f32_16x16x32_bf16 v[40:43], a[100:103], v[4:7], v[40:43]\n"
"v_exp_f32 v33, v33\n"
"v_mfma_f32_16x16x32_bf16 v[40:43], a[104:107], v[8:11], v[40:43]\n"
"v_exp_f32 v34, v34\n"
"v_mfma_f32_16x16x32_bf16 v[40:43], a[108:111], v[12:15], v[40:43]\n"
"v_exp_f32 v35, v35\n"
"v_mfma_f32_16x16x32_bf16 v[44:47], a[112:115], v[0:3], -4.0\n"
"v_exp_f32 v36, v36\n"
"v_mfma_f32_16x16x32_bf16 v[44:47], a[116:119], v[4:7], v[44:47]\n"
"v_exp_f32 v37, v37\n"
"v_mfma_f32_16x16x32_bf16 v[44:47], a[120:123], v[8:11], v[44:47]\n"
"v_exp_f32 v38, v38\n"
"v_mfma_f32_16x16x32_bf16 v[44:47], a[124:127], v[12:15], v[44:47]\n"
"v_exp_f32 v39, v39\n"
"v_mfma_f32_16x16x32_bf16 v[56:59], v[60:63], v[52:55], v[56:59]\n"
"v_cvt_pk_bf16_f32 v48, v32, v33\n"
"v_cvt_pk_bf16_f32 v49, v34, v35\n"
"v_cvt_pk_bf16_f32 v50, v36, v37\n"
"v_cvt_pk_bf16_f32 v51, v38, v39\n"
"ds_read_b128 v[24:27], v98 offset:45056\n"
"ds_read_b128 v[28:31], v99 offset:45056\n"
"ds_write_b128 v108, v[48:51] offset:12288\n"
"s_waitcnt lgkmcnt(1)\n"
"v_mfma_f32_16x16x32_bf16 v[32:35], v[64:67], v[16:19], -4.0\n"
"v_exp_f32 v40, v40\n"
"v_mfma_f32_16x16x32_bf16 v[32:35], v[68:71], v[20:23], v[32:35]\n"
"v_exp_f32 v41, v41\n"
"v_mfma_f32_16x16x32_bf16 v[32:35], v[72:75], v[24:27], v[32:35]\n"
"v_exp_f32 v42, v42\n"
"v_mfma_f32_16x16x32_bf16 v[32:35], v[76:79], v[28:31], v[32:35]\n"
"v_exp_f32 v43, v43\n"
"v_mfma_f32_16x16x32_bf16 v[36:39], v[80:83], v[16:19], -4.0\n"
"v_exp_f32 v44, v44\n"
"v_mfma_f32_16x16x32_bf16 v[36:39], v[84:87], v[20:23], v[36:39]\n"
"v_exp_f32 v45, v45\n"
"v_mfma_f32_16x16x32_bf16 v[36:39], v[88:91], v[24:27], v[36:39]\n"
"v_exp_f32 v46, v46\n"
"v_mfma_f32_16x16x32_bf16 v[36:39], v[92:95], v[28:31], v[36:39]\n"
"v_exp_f32 v47, v47\n"
"v_mfma_f32_16x16x32_bf16 v[56:59], v[60:63], v[48:51], v[56:59]\n"
"v_cvt_pk_bf16_f32 v52, v40, v41\n"
"v_cvt_pk_bf16_f32 v53, v42, v43\n"
"v_cvt_pk_bf16_f32 v54, v44, v45\n"
"v_cvt_pk_bf16_f32 v55, v46, v47\n"
"ds_write_b32 v109, v116 offset:8192\n"
"ds_write_b128 v108, v[52:55] offset:16384\n"
"v_mfma_f32_16x16x32_bf16 v[40:43], a[32:35], v[16:19], -4.0\n"
"v_exp_f32 v32, v32\n"
"v_mfma_f32_16x16x32_bf16 v[40:43], a[36:39], v[20:23], v[40:43]\n"
"v_exp_f32 v33, v33\n"
"v_mfma_f32_16x16x32_bf16 v[40:43], a[40:43], v[24:27], v[40:43]\n"
"v_exp_f32 v34, v34\n"
"v_mfma_f32_16x16x32_bf16 v[40:43], a[44:47], v[28:31], v[40:43]\n"
"v_exp_f32 v35, v35\n"
"v_mfma_f32_16x16x32_bf16 v[44:47], a[48:51], v[16:19], -4.0\n"
"v_exp_f32 v36, v36\n"
"v_mfma_f32_16x16x32_bf16 v[44:47], a[52:55], v[20:23], v[44:47]\n"
"v_exp_f32 v37, v37\n"
"v_mfma_f32_16x16x32_bf16 v[44:47], a[56:59], v[24:27], v[44:47]\n"
"v_exp_f32 v38, v38\n"
"v_mfma_f32_16x16x32_bf16 v[44:47], a[60:63], v[28:31], v[44:47]\n"
"v_exp_f32 v39, v39\n"
"v_mfma_f32_16x16x32_bf16 v[56:59], v[60:63], v[52:55], v[56:59]\n"
"v_cvt_pk_bf16_f32 v48, v32, v33\n"
"v_cvt_pk_bf16_f32 v49, v34, v35\n"
"v_cvt_pk_bf16_f32 v50, v36, v37\n"
"v_cvt_pk_bf16_f32 v51, v38, v39\n"
"ds_write_b128 v108, v[48:51] offset:20480\n"
"s_waitcnt lgkmcnt(1)\n"
"s_barrier\n"
"v_mfma_f32_16x16x32_bf16 v[32:35], a[64:67], v[16:19], -4.0\n"
"v_exp_f32 v40, v40\n"
"v_mfma_f32_16x16x32_bf16 v[32:35], a[68:71], v[20:23], v[32:35]\n"
"v_exp_f32 v41, v41\n"
"v_mfma_f32_16x16x32_bf16 v[32:35], a[72:75], v[24:27], v[32:35]\n"
"v_exp_f32 v42, v42\n"
"v_mfma_f32_16x16x32_bf16 v[32:35], a[76:79], v[28:31], v[32:35]\n"
"v_exp_f32 v43, v43\n"
"v_mfma_f32_16x16x32_bf16 v[36:39], a[80:83], v[16:19], -4.0\n"
"v_exp_f32 v44, v44\n"
"v_mfma_f32_16x16x32_bf16 v[36:39], a[84:87], v[20:23], v[36:39]\n"
"v_exp_f32 v45, v45\n"
"v_mfma_f32_16x16x32_bf16 v[36:39], a[88:91], v[24:27], v[36:39]\n"
"v_exp_f32 v46, v46\n"
"v_mfma_f32_16x16x32_bf16 v[36:39], a[92:95], v[28:31], v[36:39]\n"
"v_exp_f32 v47, v47\n"
"v_mfma_f32_16x16x32_bf16 v[116:119], v[60:63], v[48:51], 0\n"
"v_cvt_pk_bf16_f32 v52, v40, v41\n"
"v_cvt_pk_bf16_f32 v53, v42, v43\n"
"v_cvt_pk_bf16_f32 v54, v44, v45\n"
"v_cvt_pk_bf16_f32 v55, v46, v47\n"
"ds_read_b128 v[0:3], v96 offset:32768\n"
"ds_read_b128 v[4:7], v97 offset:32768\n"
"ds_write_b128 v108, v[52:55] offset:24576\n"
"v_mfma_f32_16x16x32_bf16 v[40:43], a[96:99], v[16:19], -4.0\n"
"v_exp_f32 v32, v32\n"
"v_mfma_f32_16x16x32_bf16 v[40:43], a[100:103], v[20:23], v[40:43]\n"
"v_exp_f32 v33, v33\n"
"v_mfma_f32_16x16x32_bf16 v[40:43], a[104:107], v[24:27], v[40:43]\n"
"v_exp_f32 v34, v34\n"
"v_mfma_f32_16x16x32_bf16 v[40:43], a[108:111], v[28:31], v[40:43]\n"
"v_exp_f32 v35, v35\n"
"v_mfma_f32_16x16x32_bf16 v[44:47], a[112:115], v[16:19], -4.0\n"
"v_exp_f32 v36, v36\n"
"v_mfma_f32_16x16x32_bf16 v[44:47], a[116:119], v[20:23], v[44:47]\n"
"v_exp_f32 v37, v37\n"
"v_mfma_f32_16x16x32_bf16 v[44:47], a[120:123], v[24:27], v[44:47]\n"
"v_exp_f32 v38, v38\n"
"v_mfma_f32_16x16x32_bf16 v[44:47], a[124:127], v[28:31], v[44:47]\n"
"v_exp_f32 v39, v39\n"
"v_mfma_f32_16x16x32_bf16 v[116:119], v[60:63], v[52:55], v[116:119]\n"
"v_cvt_pk_bf16_f32 v48, v32, v33\n"
"v_cvt_pk_bf16_f32 v49, v34, v35\n"
"v_cvt_pk_bf16_f32 v50, v36, v37\n"
"v_cvt_pk_bf16_f32 v51, v38, v39\n"
"ds_read_b128 v[8:11], v98 offset:32768\n"
"ds_read_b128 v[12:15], v99 offset:32768\n"
"ds_write_b128 v108, v[48:51] offset:28672\n"
"s_waitcnt lgkmcnt(1)\n"
"v_mfma_f32_16x16x32_bf16 v[32:35], v[64:67], v[0:3], -4.0\n"
"v_exp_f32 v40, v40\n"
"v_mfma_f32_16x16x32_bf16 v[32:35], v[68:71], v[4:7], v[32:35]\n"
"v_exp_f32 v41, v41\n"
"v_mfma_f32_16x16x32_bf16 v[32:35], v[72:75], v[8:11], v[32:35]\n"
"v_exp_f32 v42, v42\n"
"v_mfma_f32_16x16x32_bf16 v[32:35], v[76:79], v[12:15], v[32:35]\n"
"v_exp_f32 v43, v43\n"
"v_mfma_f32_16x16x32_bf16 v[36:39], v[80:83], v[0:3], -4.0\n"
"v_exp_f32 v44, v44\n"
"v_mfma_f32_16x16x32_bf16 v[36:39], v[84:87], v[4:7], v[36:39]\n"
"v_exp_f32 v45, v45\n"
"v_mfma_f32_16x16x32_bf16 v[36:39], v[88:91], v[8:11], v[36:39]\n"
"v_exp_f32 v46, v46\n"
"v_mfma_f32_16x16x32_bf16 v[36:39], v[92:95], v[12:15], v[36:39]\n"
"v_exp_f32 v47, v47\n"
"v_mfma_f32_16x16x32_bf16 v[116:119], v[60:63], v[48:51], v[116:119]\n"
"v_cvt_pk_bf16_f32 v52, v40, v41\n"
"v_cvt_pk_bf16_f32 v53, v42, v43\n"
"v_cvt_pk_bf16_f32 v54, v44, v45\n"
"v_cvt_pk_bf16_f32 v55, v46, v47\n"
"ds_write_b32 v109, v56 offset:9216\n"
"ds_write_b128 v108, v[52:55] offset:32768\n"
"v_mfma_f32_16x16x32_bf16 v[40:43], a[32:35], v[0:3], -4.0\n"
"v_exp_f32 v32, v32\n"
"v_mfma_f32_16x16x32_bf16 v[40:43], a[36:39], v[4:7], v[40:43]\n"
"v_exp_f32 v33, v33\n"
"v_mfma_f32_16x16x32_bf16 v[40:43], a[40:43], v[8:11], v[40:43]\n"
"v_exp_f32 v34, v34\n"
"v_mfma_f32_16x16x32_bf16 v[40:43], a[44:47], v[12:15], v[40:43]\n"
"v_exp_f32 v35, v35\n"
"v_mfma_f32_16x16x32_bf16 v[44:47], a[48:51], v[0:3], -4.0\n"
"v_exp_f32 v36, v36\n"
"v_mfma_f32_16x16x32_bf16 v[44:47], a[52:55], v[4:7], v[44:47]\n"
"v_exp_f32 v37, v37\n"
"v_mfma_f32_16x16x32_bf16 v[44:47], a[56:59], v[8:11], v[44:47]\n"
"v_exp_f32 v38, v38\n"
"v_mfma_f32_16x16x32_bf16 v[44:47], a[60:63], v[12:15], v[44:47]\n"
"v_exp_f32 v39, v39\n"
"v_mfma_f32_16x16x32_bf16 v[116:119], v[60:63], v[52:55], v[116:119]\n"
"v_cvt_pk_bf16_f32 v48, v32, v33\n"
"v_cvt_pk_bf16_f32 v49, v34, v35\n"
"v_cvt_pk_bf16_f32 v50, v36, v37\n"
"v_cvt_pk_bf16_f32 v51, v38, v39\n"
"ds_write_b128 v108, v[48:51] offset:0\n"
"s_waitcnt lgkmcnt(1)\n"
"s_barrier\n"
"v_mfma_f32_16x16x32_bf16 v[32:35], a[64:67], v[0:3], -4.0\n"
"v_exp_f32 v40, v40\n"
"v_mfma_f32_16x16x32_bf16 v[32:35], a[68:71], v[4:7], v[32:35]\n"
"v_exp_f32 v41, v41\n"
"v_mfma_f32_16x16x32_bf16 v[32:35], a[72:75], v[8:11], v[32:35]\n"
"v_exp_f32 v42, v42\n"
"v_mfma_f32_16x16x32_bf16 v[32:35], a[76:79], v[12:15], v[32:35]\n"
"v_exp_f32 v43, v43\n"
"v_mfma_f32_16x16x32_bf16 v[36:39], a[80:83], v[0:3], -4.0\n"
"v_exp_f32 v44, v44\n"
"v_mfma_f32_16x16x32_bf16 v[36:39], a[84:87], v[4:7], v[36:39]\n"
"v_exp_f32 v45, v45\n"
"v_mfma_f32_16x16x32_bf16 v[36:39], a[88:91], v[8:11], v[36:39]\n"
"v_exp_f32 v46, v46\n"
"v_mfma_f32_16x16x32_bf16 v[36:39], a[92:95], v[12:15], v[36:39]\n"
"v_exp_f32 v47, v47\n"
"v_mfma_f32_16x16x32_bf16 v[56:59], v[60:63], v[48:51], 0\n"
"v_cvt_pk_bf16_f32 v52, v40, v41\n"
"v_cvt_pk_bf16_f32 v53, v42, v43\n"
"v_cvt_pk_bf16_f32 v54, v44, v45\n"
"v_cvt_pk_bf16_f32 v55, v46, v47\n"
"ds_read_b128 v[16:19], v96 offset:36864\n"
"ds_read_b128 v[20:23], v97 offset:36864\n"
"ds_write_b128 v108, v[52:55] offset:4096\n"
"v_mfma_f32_16x16x32_bf16 v[40:43], a[96:99], v[0:3], -4.0\n"
"v_exp_f32 v32, v32\n"
"v_mfma_f32_16x16x32_bf16 v[40:43], a[100:103], v[4:7], v[40:43]\n"
"v_exp_f32 v33, v33\n"
"v_mfma_f32_16x16x32_bf16 v[40:43], a[104:107], v[8:11], v[40:43]\n"
"v_exp_f32 v34, v34\n"
"v_mfma_f32_16x16x32_bf16 v[40:43], a[108:111], v[12:15], v[40:43]\n"
"v_exp_f32 v35, v35\n"
"v_mfma_f32_16x16x32_bf16 v[44:47], a[112:115], v[0:3], -4.0\n"
"v_exp_f32 v36, v36\n"
"v_mfma_f32_16x16x32_bf16 v[44:47], a[116:119], v[4:7], v[44:47]\n"
"v_exp_f32 v37, v37\n"
"v_mfma_f32_16x16x32_bf16 v[44:47], a[120:123], v[8:11], v[44:47]\n"
"v_exp_f32 v38, v38\n"
"v_mfma_f32_16x16x32_bf16 v[44:47], a[124:127], v[12:15], v[44:47]\n"
"v_exp_f32 v39, v39\n"
"v_mfma_f32_16x16x32_bf16 v[56:59], v[60:63], v[52:55], v[56:59]\n"
"v_cvt_pk_bf16_f32 v48, v32, v33\n"
"v_cvt_pk_bf16_f32 v49, v34, v35\n"
"v_cvt_pk_bf16_f32 v50, v36, v37\n"
"v_cvt_pk_bf16_f32 v51, v38, v39\n"
"ds_read_b128 v[24:27], v98 offset:36864\n"
"ds_read_b128 v[28:31], v99 offset:36864\n"
"ds_write_b128 v108, v[48:51] offset:8192\n"
"s_waitcnt lgkmcnt(1)\n"
"v_mfma_f32_16x16x32_bf16 v[32:35], v[64:67], v[16:19], -4.0\n"
"v_exp_f32 v40, v40\n"
"v_mfma_f32_16x16x32_bf16 v[32:35], v[68:71], v[20:23], v[32:35]\n"
"v_exp_f32 v41, v41\n"
"v_mfma_f32_16x16x32_bf16 v[32:35], v[72:75], v[24:27], v[32:35]\n"
"v_exp_f32 v42, v42\n"
"v_mfma_f32_16x16x32_bf16 v[32:35], v[76:79], v[28:31], v[32:35]\n"
"v_exp_f32 v43, v43\n"
"v_mfma_f32_16x16x32_bf16 v[36:39], v[80:83], v[16:19], -4.0\n"
"v_exp_f32 v44, v44\n"
"v_mfma_f32_16x16x32_bf16 v[36:39], v[84:87], v[20:23], v[36:39]\n"
"v_exp_f32 v45, v45\n"
"v_mfma_f32_16x16x32_bf16 v[36:39], v[88:91], v[24:27], v[36:39]\n"
"v_exp_f32 v46, v46\n"
"v_mfma_f32_16x16x32_bf16 v[36:39], v[92:95], v[28:31], v[36:39]\n"
"v_exp_f32 v47, v47\n"
"v_mfma_f32_16x16x32_bf16 v[56:59], v[60:63], v[48:51], v[56:59]\n"
"v_cvt_pk_bf16_f32 v52, v40, v41\n"
"v_cvt_pk_bf16_f32 v53, v42, v43\n"
"v_cvt_pk_bf16_f32 v54, v44, v45\n"
"v_cvt_pk_bf16_f32 v55, v46, v47\n"
"ds_write_b32 v109, v116 offset:10240\n"
"ds_write_b128 v108, v[52:55] offset:12288\n"
"v_mfma_f32_16x16x32_bf16 v[40:43], a[32:35], v[16:19], -4.0\n"
"v_exp_f32 v32, v32\n"
"v_mfma_f32_16x16x32_bf16 v[40:43], a[36:39], v[20:23], v[40:43]\n"
"v_exp_f32 v33, v33\n"
"v_mfma_f32_16x16x32_bf16 v[40:43], a[40:43], v[24:27], v[40:43]\n"
"v_exp_f32 v34, v34\n"
"v_mfma_f32_16x16x32_bf16 v[40:43], a[44:47], v[28:31], v[40:43]\n"
"v_exp_f32 v35, v35\n"
"v_mfma_f32_16x16x32_bf16 v[44:47], a[48:51], v[16:19], -4.0\n"
"v_exp_f32 v36, v36\n"
"v_mfma_f32_16x16x32_bf16 v[44:47], a[52:55], v[20:23], v[44:47]\n"
"v_exp_f32 v37, v37\n"
"v_mfma_f32_16x16x32_bf16 v[44:47], a[56:59], v[24:27], v[44:47]\n"
"v_exp_f32 v38, v38\n"
"v_mfma_f32_16x16x32_bf16 v[44:47], a[60:63], v[28:31], v[44:47]\n"
"v_exp_f32 v39, v39\n"
"v_mfma_f32_16x16x32_bf16 v[56:59], v[60:63], v[52:55], v[56:59]\n"
"v_cvt_pk_bf16_f32 v48, v32, v33\n"
"v_cvt_pk_bf16_f32 v49, v34, v35\n"
"v_cvt_pk_bf16_f32 v50, v36, v37\n"
"v_cvt_pk_bf16_f32 v51, v38, v39\n"
"ds_write_b128 v108, v[48:51] offset:16384\n"
"s_waitcnt lgkmcnt(1)\n"
"s_barrier\n"
"v_mfma_f32_16x16x32_bf16 v[32:35], a[64:67], v[16:19], -4.0\n"
"v_exp_f32 v40, v40\n"
"v_mfma_f32_16x16x32_bf16 v[32:35], a[68:71], v[20:23], v[32:35]\n"
"v_exp_f32 v41, v41\n"
"v_mfma_f32_16x16x32_bf16 v[32:35], a[72:75], v[24:27], v[32:35]\n"
"v_exp_f32 v42, v42\n"
"v_mfma_f32_16x16x32_bf16 v[32:35], a[76:79], v[28:31], v[32:35]\n"
"v_exp_f32 v43, v43\n"
"v_mfma_f32_16x16x32_bf16 v[36:39], a[80:83], v[16:19], -4.0\n"
"v_exp_f32 v44, v44\n"
"v_mfma_f32_16x16x32_bf16 v[36:39], a[84:87], v[20:23], v[36:39]\n"
"v_exp_f32 v45, v45\n"
"v_mfma_f32_16x16x32_bf16 v[36:39], a[88:91], v[24:27], v[36:39]\n"
"v_exp_f32 v46, v46\n"
"v_mfma_f32_16x16x32_bf16 v[36:39], a[92:95], v[28:31], v[36:39]\n"
"v_exp_f32 v47, v47\n"
"v_mfma_f32_16x16x32_bf16 v[116:119], v[60:63], v[48:51], 0\n"
"v_cvt_pk_bf16_f32 v52, v40, v41\n"
"v_cvt_pk_bf16_f32 v53, v42, v43\n"
"v_cvt_pk_bf16_f32 v54, v44, v45\n"
"v_cvt_pk_bf16_f32 v55, v46, v47\n"
"ds_read_b128 v[0:3], v96 offset:40960\n"
"ds_read_b128 v[4:7], v97 offset:40960\n"
"ds_write_b128 v108, v[52:55] offset:20480\n"
"v_mfma_f32_16x16x32_bf16 v[40:43], a[96:99], v[16:19], -4.0\n"
"v_exp_f32 v32, v32\n"
"v_mfma_f32_16x16x32_bf16 v[40:43], a[100:103], v[20:23], v[40:43]\n"
"v_exp_f32 v33, v33\n"
"v_mfma_f32_16x16x32_bf16 v[40:43], a[104:107], v[24:27], v[40:43]\n"
"v_exp_f32 v34, v34\n"
"v_mfma_f32_16x16x32_bf16 v[40:43], a[108:111], v[28:31], v[40:43]\n"
"v_exp_f32 v35, v35\n"
"v_mfma_f32_16x16x32_bf16 v[44:47], a[112:115], v[16:19], -4.0\n"
"v_exp_f32 v36, v36\n"
"v_mfma_f32_16x16x32_bf16 v[44:47], a[116:119], v[20:23], v[44:47]\n"
"v_exp_f32 v37, v37\n"
"v_mfma_f32_16x16x32_bf16 v[44:47], a[120:123], v[24:27], v[44:47]\n"
"v_exp_f32 v38, v38\n"
"v_mfma_f32_16x16x32_bf16 v[44:47], a[124:127], v[28:31], v[44:47]\n"
"v_exp_f32 v39, v39\n"
"v_mfma_f32_16x16x32_bf16 v[116:119], v[60:63], v[52:55], v[116:119]\n"
"v_cvt_pk_bf16_f32 v48, v32, v33\n"
"v_cvt_pk_bf16_f32 v49, v34, v35\n"
"v_cvt_pk_bf16_f32 v50, v36, v37\n"
"v_cvt_pk_bf16_f32 v51, v38, v39\n"
"ds_read_b128 v[8:11], v98 offset:40960\n"
"ds_read_b128 v[12:15], v99 offset:40960\n"
"ds_write_b128 v108, v[48:51] offset:24576\n"
"s_waitcnt lgkmcnt(1)\n"
"v_mfma_f32_16x16x32_bf16 v[32:35], v[64:67], v[0:3], -4.0\n"
"v_exp_f32 v40, v40\n"
"v_mfma_f32_16x16x32_bf16 v[32:35], v[68:71], v[4:7], v[32:35]\n"
"v_exp_f32 v41, v41\n"
"v_mfma_f32_16x16x32_bf16 v[32:35], v[72:75], v[8:11], v[32:35]\n"
"v_exp_f32 v42, v42\n"
"v_mfma_f32_16x16x32_bf16 v[32:35], v[76:79], v[12:15], v[32:35]\n"
"v_exp_f32 v43, v43\n"
"v_mfma_f32_16x16x32_bf16 v[36:39], v[80:83], v[0:3], -4.0\n"
"v_exp_f32 v44, v44\n"
"v_mfma_f32_16x16x32_bf16 v[36:39], v[84:87], v[4:7], v[36:39]\n"
"v_exp_f32 v45, v45\n"
"v_mfma_f32_16x16x32_bf16 v[36:39], v[88:91], v[8:11], v[36:39]\n"
"v_exp_f32 v46, v46\n"
"v_mfma_f32_16x16x32_bf16 v[36:39], v[92:95], v[12:15], v[36:39]\n"
"v_exp_f32 v47, v47\n"
"v_mfma_f32_16x16x32_bf16 v[116:119], v[60:63], v[48:51], v[116:119]\n"
"v_cvt_pk_bf16_f32 v52, v40, v41\n"
"v_cvt_pk_bf16_f32 v53, v42, v43\n"
"v_cvt_pk_bf16_f32 v54, v44, v45\n"
"v_cvt_pk_bf16_f32 v55, v46, v47\n"
"ds_write_b32 v109, v56 offset:8192\n"
"ds_write_b128 v108, v[52:55] offset:28672\n"
"v_mfma_f32_16x16x32_bf16 v[40:43], a[32:35], v[0:3], -4.0\n"
"v_exp_f32 v32, v32\n"
"v_mfma_f32_16x16x32_bf16 v[40:43], a[36:39], v[4:7], v[40:43]\n"
"v_exp_f32 v33, v33\n"
"v_mfma_f32_16x16x32_bf16 v[40:43], a[40:43], v[8:11], v[40:43]\n"
"v_exp_f32 v34, v34\n"
"v_mfma_f32_16x16x32_bf16 v[40:43], a[44:47], v[12:15], v[40:43]\n"
"v_exp_f32 v35, v35\n"
"v_mfma_f32_16x16x32_bf16 v[44:47], a[48:51], v[0:3], -4.0\n"
"v_exp_f32 v36, v36\n"
"v_mfma_f32_16x16x32_bf16 v[44:47], a[52:55], v[4:7], v[44:47]\n"
"v_exp_f32 v37, v37\n"
"v_mfma_f32_16x16x32_bf16 v[44:47], a[56:59], v[8:11], v[44:47]\n"
"v_exp_f32 v38, v38\n"
"v_mfma_f32_16x16x32_bf16 v[44:47], a[60:63], v[12:15], v[44:47]\n"
"v_exp_f32 v39, v39\n"
"v_mfma_f32_16x16x32_bf16 v[116:119], v[60:63], v[52:55], v[116:119]\n"
"v_cvt_pk_bf16_f32 v48, v32, v33\n"
"v_cvt_pk_bf16_f32 v49, v34, v35\n"
"v_cvt_pk_bf16_f32 v50, v36, v37\n"
"v_cvt_pk_bf16_f32 v51, v38, v39\n"
"ds_write_b128 v108, v[48:51] offset:32768\n"
"s_waitcnt lgkmcnt(1)\n"
"s_barrier\n"
"v_mfma_f32_16x16x32_bf16 v[32:35], a[64:67], v[0:3], -4.0\n"
"v_exp_f32 v40, v40\n"
"v_mfma_f32_16x16x32_bf16 v[32:35], a[68:71], v[4:7], v[32:35]\n"
"v_exp_f32 v41, v41\n"
"v_mfma_f32_16x16x32_bf16 v[32:35], a[72:75], v[8:11], v[32:35]\n"
"v_exp_f32 v42, v42\n"
"v_mfma_f32_16x16x32_bf16 v[32:35], a[76:79], v[12:15], v[32:35]\n"
"v_exp_f32 v43, v43\n"
"v_mfma_f32_16x16x32_bf16 v[36:39], a[80:83], v[0:3], -4.0\n"
"v_exp_f32 v44, v44\n"
"v_mfma_f32_16x16x32_bf16 v[36:39], a[84:87], v[4:7], v[36:39]\n"
"v_exp_f32 v45, v45\n"
"v_mfma_f32_16x16x32_bf16 v[36:39], a[88:91], v[8:11], v[36:39]\n"
"v_exp_f32 v46, v46\n"
"v_mfma_f32_16x16x32_bf16 v[36:39], a[92:95], v[12:15], v[36:39]\n"
"v_exp_f32 v47, v47\n"
"v_mfma_f32_16x16x32_bf16 v[56:59], v[60:63], v[48:51], 0\n"
"v_cvt_pk_bf16_f32 v52, v40, v41\n"
"v_cvt_pk_bf16_f32 v53, v42, v43\n"
"v_cvt_pk_bf16_f32 v54, v44, v45\n"
"v_cvt_pk_bf16_f32 v55, v46, v47\n"
"ds_read_b128 v[16:19], v96 offset:45056\n"
"ds_read_b128 v[20:23], v97 offset:45056\n"
"ds_write_b128 v108, v[52:55] offset:0\n"
"v_mfma_f32_16x16x32_bf16 v[40:43], a[96:99], v[0:3], -4.0\n"
"v_exp_f32 v32, v32\n"
"v_mfma_f32_16x16x32_bf16 v[40:43], a[100:103], v[4:7], v[40:43]\n"
"v_exp_f32 v33, v33\n"
"v_mfma_f32_16x16x32_bf16 v[40:43], a[104:107], v[8:11], v[40:43]\n"
"v_exp_f32 v34, v34\n"
"v_mfma_f32_16x16x32_bf16 v[40:43], a[108:111], v[12:15], v[40:43]\n"
"v_exp_f32 v35, v35\n"
"v_mfma_f32_16x16x32_bf16 v[44:47], a[112:115], v[0:3], -4.0\n"
"v_exp_f32 v36, v36\n"
"v_mfma_f32_16x16x32_bf16 v[44:47], a[116:119], v[4:7], v[44:47]\n"
"v_exp_f32 v37, v37\n"
"v_mfma_f32_16x16x32_bf16 v[44:47], a[120:123], v[8:11], v[44:47]\n"
"v_exp_f32 v38, v38\n"
"v_mfma_f32_16x16x32_bf16 v[44:47], a[124:127], v[12:15], v[44:47]\n"
"v_exp_f32 v39, v39\n"
"v_mfma_f32_16x16x32_bf16 v[56:59], v[60:63], v[52:55], v[56:59]\n"
"v_cvt_pk_bf16_f32 v48, v32, v33\n"
"v_cvt_pk_bf16_f32 v49, v34, v35\n"
"v_cvt_pk_bf16_f32 v50, v36, v37\n"
"v_cvt_pk_bf16_f32 v51, v38, v39\n"
"ds_read_b128 v[24:27], v98 offset:45056\n"
"ds_read_b128 v[28:31], v99 offset:45056\n"
"ds_write_b128 v108, v[48:51] offset:4096\n"
"s_waitcnt lgkmcnt(1)\n"
"v_mfma_f32_16x16x32_bf16 v[32:35], v[64:67], v[16:19], -4.0\n"
"v_exp_f32 v40, v40\n"
"v_mfma_f32_16x16x32_bf16 v[32:35], v[68:71], v[20:23], v[32:35]\n"
"v_exp_f32 v41, v41\n"
"v_mfma_f32_16x16x32_bf16 v[32:35], v[72:75], v[24:27], v[32:35]\n"
"v_exp_f32 v42, v42\n"
"v_mfma_f32_16x16x32_bf16 v[32:35], v[76:79], v[28:31], v[32:35]\n"
"v_exp_f32 v43, v43\n"
"v_mfma_f32_16x16x32_bf16 v[36:39], v[80:83], v[16:19], -4.0\n"
"v_exp_f32 v44, v44\n"
"v_mfma_f32_16x16x32_bf16 v[36:39], v[84:87], v[20:23], v[36:39]\n"
"v_exp_f32 v45, v45\n"
"v_mfma_f32_16x16x32_bf16 v[36:39], v[88:91], v[24:27], v[36:39]\n"
"v_exp_f32 v46, v46\n"
"v_mfma_f32_16x16x32_bf16 v[36:39], v[92:95], v[28:31], v[36:39]\n"
"v_exp_f32 v47, v47\n"
"v_mfma_f32_16x16x32_bf16 v[56:59], v[60:63], v[48:51], v[56:59]\n"
"v_cvt_pk_bf16_f32 v52, v40, v41\n"
"v_cvt_pk_bf16_f32 v53, v42, v43\n"
"v_cvt_pk_bf16_f32 v54, v44, v45\n"
"v_cvt_pk_bf16_f32 v55, v46, v47\n"
"ds_write_b32 v109, v116 offset:9216\n"
"ds_write_b128 v108, v[52:55] offset:8192\n"
"v_mfma_f32_16x16x32_bf16 v[40:43], a[32:35], v[16:19], -4.0\n"
"v_exp_f32 v32, v32\n"
"v_mfma_f32_16x16x32_bf16 v[40:43], a[36:39], v[20:23], v[40:43]\n"
"v_exp_f32 v33, v33\n"
"v_mfma_f32_16x16x32_bf16 v[40:43], a[40:43], v[24:27], v[40:43]\n"
"v_exp_f32 v34, v34\n"
"v_mfma_f32_16x16x32_bf16 v[40:43], a[44:47], v[28:31], v[40:43]\n"
"v_exp_f32 v35, v35\n"
"v_mfma_f32_16x16x32_bf16 v[44:47], a[48:51], v[16:19], -4.0\n"
"v_exp_f32 v36, v36\n"
"v_mfma_f32_16x16x32_bf16 v[44:47], a[52:55], v[20:23], v[44:47]\n"
"v_exp_f32 v37, v37\n"
"v_mfma_f32_16x16x32_bf16 v[44:47], a[56:59], v[24:27], v[44:47]\n"
"v_exp_f32 v38, v38\n"
"v_mfma_f32_16x16x32_bf16 v[44:47], a[60:63], v[28:31], v[44:47]\n"
"v_exp_f32 v39, v39\n"
"v_mfma_f32_16x16x32_bf16 v[56:59], v[60:63], v[52:55], v[56:59]\n"
"v_cvt_pk_bf16_f32 v48, v32, v33\n"
"v_cvt_pk_bf16_f32 v49, v34, v35\n"
"v_cvt_pk_bf16_f32 v50, v36, v37\n"
"v_cvt_pk_bf16_f32 v51, v38, v39\n"
"ds_write_b128 v108, v[48:51] offset:12288\n"
"s_waitcnt lgkmcnt(1)\n"
"s_barrier\n"
"v_mfma_f32_16x16x32_bf16 v[32:35], a[64:67], v[16:19], -4.0\n"
"v_exp_f32 v40, v40\n"
"v_mfma_f32_16x16x32_bf16 v[32:35], a[68:71], v[20:23], v[32:35]\n"
"v_exp_f32 v41, v41\n"
"v_mfma_f32_16x16x32_bf16 v[32:35], a[72:75], v[24:27], v[32:35]\n"
"v_exp_f32 v42, v42\n"
"v_mfma_f32_16x16x32_bf16 v[32:35], a[76:79], v[28:31], v[32:35]\n"
"v_exp_f32 v43, v43\n"
"v_mfma_f32_16x16x32_bf16 v[36:39], a[80:83], v[16:19], -4.0\n"
"v_exp_f32 v44, v44\n"
"v_mfma_f32_16x16x32_bf16 v[36:39], a[84:87], v[20:23], v[36:39]\n"
"v_exp_f32 v45, v45\n"
"v_mfma_f32_16x16x32_bf16 v[36:39], a[88:91], v[24:27], v[36:39]\n"
"v_exp_f32 v46, v46\n"
"v_mfma_f32_16x16x32_bf16 v[36:39], a[92:95], v[28:31], v[36:39]\n"
"v_exp_f32 v47, v47\n"
"v_mfma_f32_16x16x32_bf16 v[116:119], v[60:63], v[48:51], 0\n"
"v_cvt_pk_bf16_f32 v52, v40, v41\n"
"v_cvt_pk_bf16_f32 v53, v42, v43\n"
"v_cvt_pk_bf16_f32 v54, v44, v45\n"
"v_cvt_pk_bf16_f32 v55, v46, v47\n"
"ds_read_b128 v[0:3], v96 offset:32768\n"
"ds_read_b128 v[4:7], v97 offset:32768\n"
"ds_write_b128 v108, v[52:55] offset:16384\n"
"v_mfma_f32_16x16x32_bf16 v[40:43], a[96:99], v[16:19], -4.0\n"
"v_exp_f32 v32, v32\n"
"v_mfma_f32_16x16x32_bf16 v[40:43], a[100:103], v[20:23], v[40:43]\n"
"v_exp_f32 v33, v33\n"
"v_mfma_f32_16x16x32_bf16 v[40:43], a[104:107], v[24:27], v[40:43]\n"
"v_exp_f32 v34, v34\n"
"v_mfma_f32_16x16x32_bf16 v[40:43], a[108:111], v[28:31], v[40:43]\n"
"v_exp_f32 v35, v35\n"
"v_mfma_f32_16x16x32_bf16 v[44:47], a[112:115], v[16:19], -4.0\n"
"v_exp_f32 v36, v36\n"
"v_mfma_f32_16x16x32_bf16 v[44:47], a[116:119], v[20:23], v[44:47]\n"
"v_exp_f32 v37, v37\n"
"v_mfma_f32_16x16x32_bf16 v[44:47], a[120:123], v[24:27], v[44:47]\n"
"v_exp_f32 v38, v38\n"
"v_mfma_f32_16x16x32_bf16 v[44:47], a[124:127], v[28:31], v[44:47]\n"
"v_exp_f32 v39, v39\n"
"v_mfma_f32_16x16x32_bf16 v[116:119], v[60:63], v[52:55], v[116:119]\n"
"v_cvt_pk_bf16_f32 v48, v32, v33\n"
"v_cvt_pk_bf16_f32 v49, v34, v35\n"
"v_cvt_pk_bf16_f32 v50, v36, v37\n"
"v_cvt_pk_bf16_f32 v51, v38, v39\n"
"ds_read_b128 v[8:11], v98 offset:32768\n"
"ds_read_b128 v[12:15], v99 offset:32768\n"
"ds_write_b128 v108, v[48:51] offset:20480\n"
"s_waitcnt lgkmcnt(1)\n"
"v_mfma_f32_16x16x32_bf16 v[32:35], v[64:67], v[0:3], -4.0\n"
"v_exp_f32 v40, v40\n"
"v_mfma_f32_16x16x32_bf16 v[32:35], v[68:71], v[4:7], v[32:35]\n"
"v_exp_f32 v41, v41\n"
"v_mfma_f32_16x16x32_bf16 v[32:35], v[72:75], v[8:11], v[32:35]\n"
"v_exp_f32 v42, v42\n"
"v_mfma_f32_16x16x32_bf16 v[32:35], v[76:79], v[12:15], v[32:35]\n"
"v_exp_f32 v43, v43\n"
"v_mfma_f32_16x16x32_bf16 v[36:39], v[80:83], v[0:3], -4.0\n"
"v_exp_f32 v44, v44\n"
"v_mfma_f32_16x16x32_bf16 v[36:39], v[84:87], v[4:7], v[36:39]\n"
"v_exp_f32 v45, v45\n"
"v_mfma_f32_16x16x32_bf16 v[36:39], v[88:91], v[8:11], v[36:39]\n"
"v_exp_f32 v46, v46\n"
"v_mfma_f32_16x16x32_bf16 v[36:39], v[92:95], v[12:15], v[36:39]\n"
"v_exp_f32 v47, v47\n"
"v_mfma_f32_16x16x32_bf16 v[116:119], v[60:63], v[48:51], v[116:119]\n"
"v_cvt_pk_bf16_f32 v52, v40, v41\n"
"v_cvt_pk_bf16_f32 v53, v42, v43\n"
"v_cvt_pk_bf16_f32 v54, v44, v45\n"
"v_cvt_pk_bf16_f32 v55, v46, v47\n"
"ds_write_b32 v109, v56 offset:10240\n"
"ds_write_b128 v108, v[52:55] offset:24576\n"
"v_mfma_f32_16x16x32_bf16 v[40:43], a[32:35], v[0:3], -4.0\n"
"v_exp_f32 v32, v32\n"
"v_mfma_f32_16x16x32_bf16 v[40:43], a[36:39], v[4:7], v[40:43]\n"
"v_exp_f32 v33, v33\n"
"v_mfma_f32_16x16x32_bf16 v[40:43], a[40:43], v[8:11], v[40:43]\n"
"v_exp_f32 v34, v34\n"
"v_mfma_f32_16x16x32_bf16 v[40:43], a[44:47], v[12:15], v[40:43]\n"
"v_exp_f32 v35, v35\n"
"v_mfma_f32_16x16x32_bf16 v[44:47], a[48:51], v[0:3], -4.0\n"
"v_exp_f32 v36, v36\n"
"v_mfma_f32_16x16x32_bf16 v[44:47], a[52:55], v[4:7], v[44:47]\n"
"v_exp_f32 v37, v37\n"
"v_mfma_f32_16x16x32_bf16 v[44:47], a[56:59], v[8:11], v[44:47]\n"
"v_exp_f32 v38, v38\n"
"v_mfma_f32_16x16x32_bf16 v[44:47], a[60:63], v[12:15], v[44:47]\n"
"v_exp_f32 v39, v39\n"
"v_mfma_f32_16x16x32_bf16 v[116:119], v[60:63], v[52:55], v[116:119]\n"
"v_cvt_pk_bf16_f32 v48, v32, v33\n"
"v_cvt_pk_bf16_f32 v49, v34, v35\n"
"v_cvt_pk_bf16_f32 v50, v36, v37\n"
"v_cvt_pk_bf16_f32 v51, v38, v39\n"
"ds_write_b128 v108, v[48:51] offset:28672\n"
"s_waitcnt lgkmcnt(1)\n"
"s_barrier\n"
"v_mfma_f32_16x16x32_bf16 v[32:35], a[64:67], v[0:3], -4.0\n"
"v_exp_f32 v40, v40\n"
"v_mfma_f32_16x16x32_bf16 v[32:35], a[68:71], v[4:7], v[32:35]\n"
"v_exp_f32 v41, v41\n"
"v_mfma_f32_16x16x32_bf16 v[32:35], a[72:75], v[8:11], v[32:35]\n"
"v_exp_f32 v42, v42\n"
"v_mfma_f32_16x16x32_bf16 v[32:35], a[76:79], v[12:15], v[32:35]\n"
"v_exp_f32 v43, v43\n"
"v_mfma_f32_16x16x32_bf16 v[36:39], a[80:83], v[0:3], -4.0\n"
"v_exp_f32 v44, v44\n"
"v_mfma_f32_16x16x32_bf16 v[36:39], a[84:87], v[4:7], v[36:39]\n"
"v_exp_f32 v45, v45\n"
"v_mfma_f32_16x16x32_bf16 v[36:39], a[88:91], v[8:11], v[36:39]\n"
"v_exp_f32 v46, v46\n"
"v_mfma_f32_16x16x32_bf16 v[36:39], a[92:95], v[12:15], v[36:39]\n"
"v_exp_f32 v47, v47\n"
"v_mfma_f32_16x16x32_bf16 v[56:59], v[60:63], v[48:51], 0\n"
"v_cvt_pk_bf16_f32 v52, v40, v41\n"
"v_cvt_pk_bf16_f32 v53, v42, v43\n"
"v_cvt_pk_bf16_f32 v54, v44, v45\n"
"v_cvt_pk_bf16_f32 v55, v46, v47\n"
"ds_read_b128 v[16:19], v96 offset:36864\n"
"ds_read_b128 v[20:23], v97 offset:36864\n"
"ds_write_b128 v108, v[52:55] offset:32768\n"
"v_mfma_f32_16x16x32_bf16 v[40:43], a[96:99], v[0:3], -4.0\n"
"v_exp_f32 v32, v32\n"
"v_mfma_f32_16x16x32_bf16 v[40:43], a[100:103], v[4:7], v[40:43]\n"
"v_exp_f32 v33, v33\n"
"v_mfma_f32_16x16x32_bf16 v[40:43], a[104:107], v[8:11], v[40:43]\n"
"v_exp_f32 v34, v34\n"
"v_mfma_f32_16x16x32_bf16 v[40:43], a[108:111], v[12:15], v[40:43]\n"
"v_exp_f32 v35, v35\n"
"v_mfma_f32_16x16x32_bf16 v[44:47], a[112:115], v[0:3], -4.0\n"
"v_exp_f32 v36, v36\n"
"v_mfma_f32_16x16x32_bf16 v[44:47], a[116:119], v[4:7], v[44:47]\n"
"v_exp_f32 v37, v37\n"
"v_mfma_f32_16x16x32_bf16 v[44:47], a[120:123], v[8:11], v[44:47]\n"
"v_exp_f32 v38, v38\n"
"v_mfma_f32_16x16x32_bf16 v[44:47], a[124:127], v[12:15], v[44:47]\n"
"v_exp_f32 v39, v39\n"
"v_mfma_f32_16x16x32_bf16 v[56:59], v[60:63], v[52:55], v[56:59]\n"
"v_cvt_pk_bf16_f32 v48, v32, v33\n"
"v_cvt_pk_bf16_f32 v49, v34, v35\n"
"v_cvt_pk_bf16_f32 v50, v36, v37\n"
"v_cvt_pk_bf16_f32 v51, v38, v39\n"
"ds_read_b128 v[24:27], v98 offset:36864\n"
"ds_read_b128 v[28:31], v99 offset:36864\n"
"ds_write_b128 v108, v[48:51] offset:0\n"
"s_waitcnt lgkmcnt(1)\n"
"v_mfma_f32_16x16x32_bf16 v[32:35], v[64:67], v[16:19], -4.0\n"
"v_exp_f32 v40, v40\n"
"v_mfma_f32_16x16x32_bf16 v[32:35], v[68:71], v[20:23], v[32:35]\n"
"v_exp_f32 v41, v41\n"
"v_mfma_f32_16x16x32_bf16 v[32:35], v[72:75], v[24:27], v[32:35]\n"
"v_exp_f32 v42, v42\n"
"v_mfma_f32_16x16x32_bf16 v[32:35], v[76:79], v[28:31], v[32:35]\n"
"v_exp_f32 v43, v43\n"
"v_mfma_f32_16x16x32_bf16 v[36:39], v[80:83], v[16:19], -4.0\n"
"v_exp_f32 v44, v44\n"
"v_mfma_f32_16x16x32_bf16 v[36:39], v[84:87], v[20:23], v[36:39]\n"
"v_exp_f32 v45, v45\n"
"v_mfma_f32_16x16x32_bf16 v[36:39], v[88:91], v[24:27], v[36:39]\n"
"v_exp_f32 v46, v46\n"
"v_mfma_f32_16x16x32_bf16 v[36:39], v[92:95], v[28:31], v[36:39]\n"
"v_exp_f32 v47, v47\n"
"v_mfma_f32_16x16x32_bf16 v[56:59], v[60:63], v[48:51], v[56:59]\n"
"v_cvt_pk_bf16_f32 v52, v40, v41\n"
"v_cvt_pk_bf16_f32 v53, v42, v43\n"
"v_cvt_pk_bf16_f32 v54, v44, v45\n"
"v_cvt_pk_bf16_f32 v55, v46, v47\n"
"ds_write_b32 v109, v116 offset:8192\n"
"ds_write_b128 v108, v[52:55] offset:4096\n"
"v_mfma_f32_16x16x32_bf16 v[40:43], a[32:35], v[16:19], -4.0\n"
"v_exp_f32 v32, v32\n"
"v_mfma_f32_16x16x32_bf16 v[40:43], a[36:39], v[20:23], v[40:43]\n"
"v_exp_f32 v33, v33\n"
"v_mfma_f32_16x16x32_bf16 v[40:43], a[40:43], v[24:27], v[40:43]\n"
"v_exp_f32 v34, v34\n"
"v_mfma_f32_16x16x32_bf16 v[40:43], a[44:47], v[28:31], v[40:43]\n"
"v_exp_f32 v35, v35\n"
"v_mfma_f32_16x16x32_bf16 v[44:47], a[48:51], v[16:19], -4.0\n"
"v_exp_f32 v36, v36\n"
"v_mfma_f32_16x16x32_bf16 v[44:47], a[52:55], v[20:23], v[44:47]\n"
"v_exp_f32 v37, v37\n"
"v_mfma_f32_16x16x32_bf16 v[44:47], a[56:59], v[24:27], v[44:47]\n"
"v_exp_f32 v38, v38\n"
"v_mfma_f32_16x16x32_bf16 v[44:47], a[60:63], v[28:31], v[44:47]\n"
"v_exp_f32 v39, v39\n"
"v_mfma_f32_16x16x32_bf16 v[56:59], v[60:63], v[52:55], v[56:59]\n"
"v_cvt_pk_bf16_f32 v48, v32, v33\n"
"v_cvt_pk_bf16_f32 v49, v34, v35\n"
"v_cvt_pk_bf16_f32 v50, v36, v37\n"
"v_cvt_pk_bf16_f32 v51, v38, v39\n"
"ds_write_b128 v108, v[48:51] offset:8192\n"
"s_waitcnt lgkmcnt(1)\n"
"s_barrier\n"
"v_mfma_f32_16x16x32_bf16 v[32:35], a[64:67], v[16:19], -4.0\n"
"v_exp_f32 v40, v40\n"
"v_mfma_f32_16x16x32_bf16 v[32:35], a[68:71], v[20:23], v[32:35]\n"
"v_exp_f32 v41, v41\n"
"v_mfma_f32_16x16x32_bf16 v[32:35], a[72:75], v[24:27], v[32:35]\n"
"v_exp_f32 v42, v42\n"
"v_mfma_f32_16x16x32_bf16 v[32:35], a[76:79], v[28:31], v[32:35]\n"
"v_exp_f32 v43, v43\n"
"v_mfma_f32_16x16x32_bf16 v[36:39], a[80:83], v[16:19], -4.0\n"
"v_exp_f32 v44, v44\n"
"v_mfma_f32_16x16x32_bf16 v[36:39], a[84:87], v[20:23], v[36:39]\n"
"v_exp_f32 v45, v45\n"
"v_mfma_f32_16x16x32_bf16 v[36:39], a[88:91], v[24:27], v[36:39]\n"
"v_exp_f32 v46, v46\n"
"v_mfma_f32_16x16x32_bf16 v[36:39], a[92:95], v[28:31], v[36:39]\n"
"v_exp_f32 v47, v47\n"
"v_mfma_f32_16x16x32_bf16 v[116:119], v[60:63], v[48:51], 0\n"
"v_cvt_pk_bf16_f32 v52, v40, v41\n"
"v_cvt_pk_bf16_f32 v53, v42, v43\n"
"v_cvt_pk_bf16_f32 v54, v44, v45\n"
"v_cvt_pk_bf16_f32 v55, v46, v47\n"
"ds_read_b128 v[0:3], v96 offset:40960\n"
"ds_read_b128 v[4:7], v97 offset:40960\n"
"ds_write_b128 v108, v[52:55] offset:12288\n"
"v_mfma_f32_16x16x32_bf16 v[40:43], a[96:99], v[16:19], -4.0\n"
"v_exp_f32 v32, v32\n"
"v_mfma_f32_16x16x32_bf16 v[40:43], a[100:103], v[20:23], v[40:43]\n"
"v_exp_f32 v33, v33\n"
"v_mfma_f32_16x16x32_bf16 v[40:43], a[104:107], v[24:27], v[40:43]\n"
"v_exp_f32 v34, v34\n"
"v_mfma_f32_16x16x32_bf16 v[40:43], a[108:111], v[28:31], v[40:43]\n"
"v_exp_f32 v35, v35\n"
"v_mfma_f32_16x16x32_bf16 v[44:47], a[112:115], v[16:19], -4.0\n"
"v_exp_f32 v36, v36\n"
"v_mfma_f32_16x16x32_bf16 v[44:47], a[116:119], v[20:23], v[44:47]\n"
"v_exp_f32 v37, v37\n"
"v_mfma_f32_16x16x32_bf16 v[44:47], a[120:123], v[24:27], v[44:47]\n"
"v_exp_f32 v38, v38\n"
"v_mfma_f32_16x16x32_bf16 v[44:47], a[124:127], v[28:31], v[44:47]\n"
"v_exp_f32 v39, v39\n"
"v_mfma_f32_16x16x32_bf16 v[116:119], v[60:63], v[52:55], v[116:119]\n"
"v_cvt_pk_bf16_f32 v48, v32, v33\n"
"v_cvt_pk_bf16_f32 v49, v34, v35\n"
"v_cvt_pk_bf16_f32 v50, v36, v37\n"
"v_cvt_pk_bf16_f32 v51, v38, v39\n"
"ds_read_b128 v[8:11], v98 offset:40960\n"
"ds_read_b128 v[12:15], v99 offset:40960\n"
"ds_write_b128 v108, v[48:51] offset:16384\n"
"s_waitcnt lgkmcnt(1)\n"
"v_mfma_f32_16x16x32_bf16 v[32:35], v[64:67], v[0:3], -4.0\n"
"v_exp_f32 v40, v40\n"
"v_mfma_f32_16x16x32_bf16 v[32:35], v[68:71], v[4:7], v[32:35]\n"
"v_exp_f32 v41, v41\n"
"v_mfma_f32_16x16x32_bf16 v[32:35], v[72:75], v[8:11], v[32:35]\n"
"v_exp_f32 v42, v42\n"
"v_mfma_f32_16x16x32_bf16 v[32:35], v[76:79], v[12:15], v[32:35]\n"
"v_exp_f32 v43, v43\n"
"v_mfma_f32_16x16x32_bf16 v[36:39], v[80:83], v[0:3], -4.0\n"
"v_exp_f32 v44, v44\n"
"v_mfma_f32_16x16x32_bf16 v[36:39], v[84:87], v[4:7], v[36:39]\n"
"v_exp_f32 v45, v45\n"
"v_mfma_f32_16x16x32_bf16 v[36:39], v[88:91], v[8:11], v[36:39]\n"
"v_exp_f32 v46, v46\n"
"v_mfma_f32_16x16x32_bf16 v[36:39], v[92:95], v[12:15], v[36:39]\n"
"v_exp_f32 v47, v47\n"
"v_mfma_f32_16x16x32_bf16 v[116:119], v[60:63], v[48:51], v[116:119]\n"
"v_cvt_pk_bf16_f32 v52, v40, v41\n"
"v_cvt_pk_bf16_f32 v53, v42, v43\n"
"v_cvt_pk_bf16_f32 v54, v44, v45\n"
"v_cvt_pk_bf16_f32 v55, v46, v47\n"
"ds_write_b32 v109, v56 offset:9216\n"
"ds_write_b128 v108, v[52:55] offset:20480\n"
"v_mfma_f32_16x16x32_bf16 v[40:43], a[32:35], v[0:3], -4.0\n"
"v_exp_f32 v32, v32\n"
"v_mfma_f32_16x16x32_bf16 v[40:43], a[36:39], v[4:7], v[40:43]\n"
"v_exp_f32 v33, v33\n"
"v_mfma_f32_16x16x32_bf16 v[40:43], a[40:43], v[8:11], v[40:43]\n"
"v_exp_f32 v34, v34\n"
"v_mfma_f32_16x16x32_bf16 v[40:43], a[44:47], v[12:15], v[40:43]\n"
"v_exp_f32 v35, v35\n"
"v_mfma_f32_16x16x32_bf16 v[44:47], a[48:51], v[0:3], -4.0\n"
"v_exp_f32 v36, v36\n"
"v_mfma_f32_16x16x32_bf16 v[44:47], a[52:55], v[4:7], v[44:47]\n"
"v_exp_f32 v37, v37\n"
"v_mfma_f32_16x16x32_bf16 v[44:47], a[56:59], v[8:11], v[44:47]\n"
"v_exp_f32 v38, v38\n"
"v_mfma_f32_16x16x32_bf16 v[44:47], a[60:63], v[12:15], v[44:47]\n"
"v_exp_f32 v39, v39\n"
"v_mfma_f32_16x16x32_bf16 v[116:119], v[60:63], v[52:55], v[116:119]\n"
"v_cvt_pk_bf16_f32 v48, v32, v33\n"
"v_cvt_pk_bf16_f32 v49, v34, v35\n"
"v_cvt_pk_bf16_f32 v50, v36, v37\n"
"v_cvt_pk_bf16_f32 v51, v38, v39\n"
"ds_write_b128 v108, v[48:51] offset:24576\n"
"s_waitcnt lgkmcnt(1)\n"
"s_barrier\n"
"v_mfma_f32_16x16x32_bf16 v[32:35], a[64:67], v[0:3], -4.0\n"
"v_exp_f32 v40, v40\n"
"v_mfma_f32_16x16x32_bf16 v[32:35], a[68:71], v[4:7], v[32:35]\n"
"v_exp_f32 v41, v41\n"
"v_mfma_f32_16x16x32_bf16 v[32:35], a[72:75], v[8:11], v[32:35]\n"
"v_exp_f32 v42, v42\n"
"v_mfma_f32_16x16x32_bf16 v[32:35], a[76:79], v[12:15], v[32:35]\n"
"v_exp_f32 v43, v43\n"
"v_mfma_f32_16x16x32_bf16 v[36:39], a[80:83], v[0:3], -4.0\n"
"v_exp_f32 v44, v44\n"
"v_mfma_f32_16x16x32_bf16 v[36:39], a[84:87], v[4:7], v[36:39]\n"
"v_exp_f32 v45, v45\n"
"v_mfma_f32_16x16x32_bf16 v[36:39], a[88:91], v[8:11], v[36:39]\n"
"v_exp_f32 v46, v46\n"
"v_mfma_f32_16x16x32_bf16 v[36:39], a[92:95], v[12:15], v[36:39]\n"
"v_exp_f32 v47, v47\n"
"v_mfma_f32_16x16x32_bf16 v[56:59], v[60:63], v[48:51], 0\n"
"v_cvt_pk_bf16_f32 v52, v40, v41\n"
"v_cvt_pk_bf16_f32 v53, v42, v43\n"
"v_cvt_pk_bf16_f32 v54, v44, v45\n"
"v_cvt_pk_bf16_f32 v55, v46, v47\n"
"ds_read_b128 v[16:19], v96 offset:45056\n"
"ds_read_b128 v[20:23], v97 offset:45056\n"
"ds_write_b128 v108, v[52:55] offset:28672\n"
"v_mfma_f32_16x16x32_bf16 v[40:43], a[96:99], v[0:3], -4.0\n"
"v_exp_f32 v32, v32\n"
"v_mfma_f32_16x16x32_bf16 v[40:43], a[100:103], v[4:7], v[40:43]\n"
"v_exp_f32 v33, v33\n"
"v_mfma_f32_16x16x32_bf16 v[40:43], a[104:107], v[8:11], v[40:43]\n"
"v_exp_f32 v34, v34\n"
"v_mfma_f32_16x16x32_bf16 v[40:43], a[108:111], v[12:15], v[40:43]\n"
"v_exp_f32 v35, v35\n"
"v_mfma_f32_16x16x32_bf16 v[44:47], a[112:115], v[0:3], -4.0\n"
"v_exp_f32 v36, v36\n"
"v_mfma_f32_16x16x32_bf16 v[44:47], a[116:119], v[4:7], v[44:47]\n"
"v_exp_f32 v37, v37\n"
"v_mfma_f32_16x16x32_bf16 v[44:47], a[120:123], v[8:11], v[44:47]\n"
"v_exp_f32 v38, v38\n"
"v_mfma_f32_16x16x32_bf16 v[44:47], a[124:127], v[12:15], v[44:47]\n"
"v_exp_f32 v39, v39\n"
"v_mfma_f32_16x16x32_bf16 v[56:59], v[60:63], v[52:55], v[56:59]\n"
"v_cvt_pk_bf16_f32 v48, v32, v33\n"
"v_cvt_pk_bf16_f32 v49, v34, v35\n"
"v_cvt_pk_bf16_f32 v50, v36, v37\n"
"v_cvt_pk_bf16_f32 v51, v38, v39\n"
"ds_read_b128 v[24:27], v98 offset:45056\n"
"ds_read_b128 v[28:31], v99 offset:45056\n"
"ds_write_b128 v108, v[48:51] offset:32768\n"
"s_waitcnt lgkmcnt(1)\n"
"v_mfma_f32_16x16x32_bf16 v[32:35], v[64:67], v[16:19], -4.0\n"
"v_exp_f32 v40, v40\n"
"v_mfma_f32_16x16x32_bf16 v[32:35], v[68:71], v[20:23], v[32:35]\n"
"v_exp_f32 v41, v41\n"
"v_mfma_f32_16x16x32_bf16 v[32:35], v[72:75], v[24:27], v[32:35]\n"
"v_exp_f32 v42, v42\n"
"v_mfma_f32_16x16x32_bf16 v[32:35], v[76:79], v[28:31], v[32:35]\n"
"v_exp_f32 v43, v43\n"
"v_mfma_f32_16x16x32_bf16 v[36:39], v[80:83], v[16:19], -4.0\n"
"v_exp_f32 v44, v44\n"
"v_mfma_f32_16x16x32_bf16 v[36:39], v[84:87], v[20:23], v[36:39]\n"
"v_exp_f32 v45, v45\n"
"v_mfma_f32_16x16x32_bf16 v[36:39], v[88:91], v[24:27], v[36:39]\n"
"v_exp_f32 v46, v46\n"
"v_mfma_f32_16x16x32_bf16 v[36:39], v[92:95], v[28:31], v[36:39]\n"
"v_exp_f32 v47, v47\n"
"v_mfma_f32_16x16x32_bf16 v[56:59], v[60:63], v[48:51], v[56:59]\n"
"v_cvt_pk_bf16_f32 v52, v40, v41\n"
"v_cvt_pk_bf16_f32 v53, v42, v43\n"
"v_cvt_pk_bf16_f32 v54, v44, v45\n"
"v_cvt_pk_bf16_f32 v55, v46, v47\n"
"ds_write_b32 v109, v116 offset:10240\n"
"ds_write_b128 v108, v[52:55] offset:0\n"
"v_mfma_f32_16x16x32_bf16 v[40:43], a[32:35], v[16:19], -4.0\n"
"v_exp_f32 v32, v32\n"
"v_mfma_f32_16x16x32_bf16 v[40:43], a[36:39], v[20:23], v[40:43]\n"
"v_exp_f32 v33, v33\n"
"v_mfma_f32_16x16x32_bf16 v[40:43], a[40:43], v[24:27], v[40:43]\n"
"v_exp_f32 v34, v34\n"
"v_mfma_f32_16x16x32_bf16 v[40:43], a[44:47], v[28:31], v[40:43]\n"
"v_exp_f32 v35, v35\n"
"v_mfma_f32_16x16x32_bf16 v[44:47], a[48:51], v[16:19], -4.0\n"
"v_exp_f32 v36, v36\n"
"v_mfma_f32_16x16x32_bf16 v[44:47], a[52:55], v[20:23], v[44:47]\n"
"v_exp_f32 v37, v37\n"
"v_mfma_f32_16x16x32_bf16 v[44:47], a[56:59], v[24:27], v[44:47]\n"
"v_exp_f32 v38, v38\n"
"v_mfma_f32_16x16x32_bf16 v[44:47], a[60:63], v[28:31], v[44:47]\n"
"v_exp_f32 v39, v39\n"
"v_mfma_f32_16x16x32_bf16 v[56:59], v[60:63], v[52:55], v[56:59]\n"
"v_cvt_pk_bf16_f32 v48, v32, v33\n"
"v_cvt_pk_bf16_f32 v49, v34, v35\n"
"v_cvt_pk_bf16_f32 v50, v36, v37\n"
"v_cvt_pk_bf16_f32 v51, v38, v39\n"
"ds_write_b128 v108, v[48:51] offset:4096\n"
"s_waitcnt lgkmcnt(1)\n"
"s_barrier\n"
"v_mfma_f32_16x16x32_bf16 v[32:35], a[64:67], v[16:19], -4.0\n"
"v_exp_f32 v40, v40\n"
"v_mfma_f32_16x16x32_bf16 v[32:35], a[68:71], v[20:23], v[32:35]\n"
"v_exp_f32 v41, v41\n"
"v_mfma_f32_16x16x32_bf16 v[32:35], a[72:75], v[24:27], v[32:35]\n"
"v_exp_f32 v42, v42\n"
"v_mfma_f32_16x16x32_bf16 v[32:35], a[76:79], v[28:31], v[32:35]\n"
"v_exp_f32 v43, v43\n"
"v_mfma_f32_16x16x32_bf16 v[36:39], a[80:83], v[16:19], -4.0\n"
"v_exp_f32 v44, v44\n"
"v_mfma_f32_16x16x32_bf16 v[36:39], a[84:87], v[20:23], v[36:39]\n"
"v_exp_f32 v45, v45\n"
"v_mfma_f32_16x16x32_bf16 v[36:39], a[88:91], v[24:27], v[36:39]\n"
"v_exp_f32 v46, v46\n"
"v_mfma_f32_16x16x32_bf16 v[36:39], a[92:95], v[28:31], v[36:39]\n"
"v_exp_f32 v47, v47\n"
"v_mfma_f32_16x16x32_bf16 v[116:119], v[60:63], v[48:51], 0\n"
"v_cvt_pk_bf16_f32 v52, v40, v41\n"
"v_cvt_pk_bf16_f32 v53, v42, v43\n"
"v_cvt_pk_bf16_f32 v54, v44, v45\n"
"v_cvt_pk_bf16_f32 v55, v46, v47\n"
"ds_read_b128 v[0:3], v96 offset:32768\n"
"ds_read_b128 v[4:7], v97 offset:32768\n"
"ds_write_b128 v108, v[52:55] offset:8192\n"
"v_mfma_f32_16x16x32_bf16 v[40:43], a[96:99], v[16:19], -4.0\n"
"v_exp_f32 v32, v32\n"
"v_mfma_f32_16x16x32_bf16 v[40:43], a[100:103], v[20:23], v[40:43]\n"
"v_exp_f32 v33, v33\n"
"v_mfma_f32_16x16x32_bf16 v[40:43], a[104:107], v[24:27], v[40:43]\n"
"v_exp_f32 v34, v34\n"
"v_mfma_f32_16x16x32_bf16 v[40:43], a[108:111], v[28:31], v[40:43]\n"
"v_exp_f32 v35, v35\n"
"v_mfma_f32_16x16x32_bf16 v[44:47], a[112:115], v[16:19], -4.0\n"
"v_exp_f32 v36, v36\n"
"v_mfma_f32_16x16x32_bf16 v[44:47], a[116:119], v[20:23], v[44:47]\n"
"v_exp_f32 v37, v37\n"
"v_mfma_f32_16x16x32_bf16 v[44:47], a[120:123], v[24:27], v[44:47]\n"
"v_exp_f32 v38, v38\n"
"v_mfma_f32_16x16x32_bf16 v[44:47], a[124:127], v[28:31], v[44:47]\n"
"v_exp_f32 v39, v39\n"
"v_mfma_f32_16x16x32_bf16 v[116:119], v[60:63], v[52:55], v[116:119]\n"
"v_cvt_pk_bf16_f32 v48, v32, v33\n"
"v_cvt_pk_bf16_f32 v49, v34, v35\n"
"v_cvt_pk_bf16_f32 v50, v36, v37\n"
"v_cvt_pk_bf16_f32 v51, v38, v39\n"
"ds_read_b128 v[8:11], v98 offset:32768\n"
"ds_read_b128 v[12:15], v99 offset:32768\n"
"ds_write_b128 v108, v[48:51] offset:12288\n"
"s_waitcnt lgkmcnt(1)\n"
"v_mfma_f32_16x16x32_bf16 v[32:35], v[64:67], v[0:3], -4.0\n"
"v_exp_f32 v40, v40\n"
"v_mfma_f32_16x16x32_bf16 v[32:35], v[68:71], v[4:7], v[32:35]\n"
"v_exp_f32 v41, v41\n"
"v_mfma_f32_16x16x32_bf16 v[32:35], v[72:75], v[8:11], v[32:35]\n"
"v_exp_f32 v42, v42\n"
"v_mfma_f32_16x16x32_bf16 v[32:35], v[76:79], v[12:15], v[32:35]\n"
"v_exp_f32 v43, v43\n"
"v_mfma_f32_16x16x32_bf16 v[36:39], v[80:83], v[0:3], -4.0\n"
"v_exp_f32 v44, v44\n"
"v_mfma_f32_16x16x32_bf16 v[36:39], v[84:87], v[4:7], v[36:39]\n"
"v_exp_f32 v45, v45\n"
"v_mfma_f32_16x16x32_bf16 v[36:39], v[88:91], v[8:11], v[36:39]\n"
"v_exp_f32 v46, v46\n"
"v_mfma_f32_16x16x32_bf16 v[36:39], v[92:95], v[12:15], v[36:39]\n"
"v_exp_f32 v47, v47\n"
"v_mfma_f32_16x16x32_bf16 v[116:119], v[60:63], v[48:51], v[116:119]\n"
"v_cvt_pk_bf16_f32 v52, v40, v41\n"
"v_cvt_pk_bf16_f32 v53, v42, v43\n"
"v_cvt_pk_bf16_f32 v54, v44, v45\n"
"v_cvt_pk_bf16_f32 v55, v46, v47\n"
"ds_write_b32 v109, v56 offset:8192\n"
"ds_write_b128 v108, v[52:55] offset:16384\n"
"v_mfma_f32_16x16x32_bf16 v[40:43], a[32:35], v[0:3], -4.0\n"
"v_exp_f32 v32, v32\n"
"v_mfma_f32_16x16x32_bf16 v[40:43], a[36:39], v[4:7], v[40:43]\n"
"v_exp_f32 v33, v33\n"
"v_mfma_f32_16x16x32_bf16 v[40:43], a[40:43], v[8:11], v[40:43]\n"
"v_exp_f32 v34, v34\n"
"v_mfma_f32_16x16x32_bf16 v[40:43], a[44:47], v[12:15], v[40:43]\n"
"v_exp_f32 v35, v35\n"
"v_mfma_f32_16x16x32_bf16 v[44:47], a[48:51], v[0:3], -4.0\n"
"v_exp_f32 v36, v36\n"
"v_mfma_f32_16x16x32_bf16 v[44:47], a[52:55], v[4:7], v[44:47]\n"
"v_exp_f32 v37, v37\n"
"v_mfma_f32_16x16x32_bf16 v[44:47], a[56:59], v[8:11], v[44:47]\n"
"v_exp_f32 v38, v38\n"
"v_mfma_f32_16x16x32_bf16 v[44:47], a[60:63], v[12:15], v[44:47]\n"
"v_exp_f32 v39, v39\n"
"v_mfma_f32_16x16x32_bf16 v[116:119], v[60:63], v[52:55], v[116:119]\n"
"v_cvt_pk_bf16_f32 v48, v32, v33\n"
"v_cvt_pk_bf16_f32 v49, v34, v35\n"
"v_cvt_pk_bf16_f32 v50, v36, v37\n"
"v_cvt_pk_bf16_f32 v51, v38, v39\n"
"ds_write_b128 v108, v[48:51] offset:20480\n"
"s_waitcnt lgkmcnt(1)\n"
"s_barrier\n"
"v_mfma_f32_16x16x32_bf16 v[32:35], a[64:67], v[0:3], -4.0\n"
"v_exp_f32 v40, v40\n"
"v_mfma_f32_16x16x32_bf16 v[32:35], a[68:71], v[4:7], v[32:35]\n"
"v_exp_f32 v41, v41\n"
"v_mfma_f32_16x16x32_bf16 v[32:35], a[72:75], v[8:11], v[32:35]\n"
"v_exp_f32 v42, v42\n"
"v_mfma_f32_16x16x32_bf16 v[32:35], a[76:79], v[12:15], v[32:35]\n"
"v_exp_f32 v43, v43\n"
"v_mfma_f32_16x16x32_bf16 v[36:39], a[80:83], v[0:3], -4.0\n"
"v_exp_f32 v44, v44\n"
"v_mfma_f32_16x16x32_bf16 v[36:39], a[84:87], v[4:7], v[36:39]\n"
"v_exp_f32 v45, v45\n"
"v_mfma_f32_16x16x32_bf16 v[36:39], a[88:91], v[8:11], v[36:39]\n"
"v_exp_f32 v46, v46\n"
"v_mfma_f32_16x16x32_bf16 v[36:39], a[92:95], v[12:15], v[36:39]\n"
"v_exp_f32 v47, v47\n"
"v_mfma_f32_16x16x32_bf16 v[56:59], v[60:63], v[48:51], 0\n"
"v_cvt_pk_bf16_f32 v52, v40, v41\n"
"v_cvt_pk_bf16_f32 v53, v42, v43\n"
"v_cvt_pk_bf16_f32 v54, v44, v45\n"
"v_cvt_pk_bf16_f32 v55, v46, v47\n"
"ds_read_b128 v[16:19], v96 offset:36864\n"
"ds_read_b128 v[20:23], v97 offset:36864\n"
"ds_write_b32 v109, v116 offset:9216\n"
"ds_write_b128 v108, v[52:55] offset:24576\n"
"v_mfma_f32_16x16x32_bf16 v[40:43], a[96:99], v[0:3], -4.0\n"
"v_exp_f32 v32, v32\n"
"v_mfma_f32_16x16x32_bf16 v[40:43], a[100:103], v[4:7], v[40:43]\n"
"v_exp_f32 v33, v33\n"
"v_mfma_f32_16x16x32_bf16 v[40:43], a[104:107], v[8:11], v[40:43]\n"
"v_exp_f32 v34, v34\n"
"v_mfma_f32_16x16x32_bf16 v[40:43], a[108:111], v[12:15], v[40:43]\n"
"v_exp_f32 v35, v35\n"
"v_mfma_f32_16x16x32_bf16 v[44:47], a[112:115], v[0:3], -4.0\n"
"v_exp_f32 v36, v36\n"
"v_mfma_f32_16x16x32_bf16 v[44:47], a[116:119], v[4:7], v[44:47]\n"
"v_exp_f32 v37, v37\n"
"v_mfma_f32_16x16x32_bf16 v[44:47], a[120:123], v[8:11], v[44:47]\n"
"v_exp_f32 v38, v38\n"
"v_mfma_f32_16x16x32_bf16 v[44:47], a[124:127], v[12:15], v[44:47]\n"
"v_exp_f32 v39, v39\n"
"v_mfma_f32_16x16x32_bf16 v[56:59], v[60:63], v[52:55], v[56:59]\n"
"v_cvt_pk_bf16_f32 v48, v32, v33\n"
"v_cvt_pk_bf16_f32 v49, v34, v35\n"
"v_cvt_pk_bf16_f32 v50, v36, v37\n"
"v_cvt_pk_bf16_f32 v51, v38, v39\n"
"ds_read_b128 v[24:27], v98 offset:36864\n"
"ds_read_b128 v[28:31], v99 offset:36864\n"
"ds_write_b128 v108, v[48:51] offset:28672\n"
"s_waitcnt lgkmcnt(1)\n"
"v_mfma_f32_16x16x32_bf16 v[32:35], v[64:67], v[16:19], -4.0\n"
"v_exp_f32 v40, v40\n"
"v_mfma_f32_16x16x32_bf16 v[32:35], v[68:71], v[20:23], v[32:35]\n"
"v_exp_f32 v41, v41\n"
"v_mfma_f32_16x16x32_bf16 v[32:35], v[72:75], v[24:27], v[32:35]\n"
"v_exp_f32 v42, v42\n"
"v_mfma_f32_16x16x32_bf16 v[32:35], v[76:79], v[28:31], v[32:35]\n"
"v_exp_f32 v43, v43\n"
"v_mfma_f32_16x16x32_bf16 v[36:39], v[80:83], v[16:19], -4.0\n"
"v_exp_f32 v44, v44\n"
"v_mfma_f32_16x16x32_bf16 v[36:39], v[84:87], v[20:23], v[36:39]\n"
"v_exp_f32 v45, v45\n"
"v_mfma_f32_16x16x32_bf16 v[36:39], v[88:91], v[24:27], v[36:39]\n"
"v_exp_f32 v46, v46\n"
"v_mfma_f32_16x16x32_bf16 v[36:39], v[92:95], v[28:31], v[36:39]\n"
"v_exp_f32 v47, v47\n"
"v_mfma_f32_16x16x32_bf16 v[56:59], v[60:63], v[48:51], v[56:59]\n"
"v_cvt_pk_bf16_f32 v52, v40, v41\n"
"v_cvt_pk_bf16_f32 v53, v42, v43\n"
"v_cvt_pk_bf16_f32 v54, v44, v45\n"
"v_cvt_pk_bf16_f32 v55, v46, v47\n"
"ds_write_b128 v108, v[52:55] offset:32768\n"
"v_mfma_f32_16x16x32_bf16 v[40:43], a[32:35], v[16:19], -4.0\n"
"v_exp_f32 v32, v32\n"
"v_mfma_f32_16x16x32_bf16 v[40:43], a[36:39], v[20:23], v[40:43]\n"
"v_exp_f32 v33, v33\n"
"v_mfma_f32_16x16x32_bf16 v[40:43], a[40:43], v[24:27], v[40:43]\n"
"v_exp_f32 v34, v34\n"
"v_mfma_f32_16x16x32_bf16 v[40:43], a[44:47], v[28:31], v[40:43]\n"
"v_exp_f32 v35, v35\n"
"v_mfma_f32_16x16x32_bf16 v[44:47], a[48:51], v[16:19], -4.0\n"
"v_exp_f32 v36, v36\n"
"v_mfma_f32_16x16x32_bf16 v[44:47], a[52:55], v[20:23], v[44:47]\n"
"v_exp_f32 v37, v37\n"
"v_mfma_f32_16x16x32_bf16 v[44:47], a[56:59], v[24:27], v[44:47]\n"
"v_exp_f32 v38, v38\n"
"v_mfma_f32_16x16x32_bf16 v[44:47], a[60:63], v[28:31], v[44:47]\n"
"v_exp_f32 v39, v39\n"
"v_mfma_f32_16x16x32_bf16 v[56:59], v[60:63], v[52:55], v[56:59]\n"
"v_cvt_pk_bf16_f32 v48, v32, v33\n"
"v_cvt_pk_bf16_f32 v49, v34, v35\n"
"v_cvt_pk_bf16_f32 v50, v36, v37\n"
"v_cvt_pk_bf16_f32 v51, v38, v39\n"
"ds_write_b128 v108, v[48:51] offset:0\n"
"s_waitcnt lgkmcnt(1)\n"
"s_barrier\n"
"v_mfma_f32_16x16x32_bf16 v[32:35], a[64:67], v[16:19], -4.0\n"
"v_exp_f32 v40, v40\n"
"v_mfma_f32_16x16x32_bf16 v[32:35], a[68:71], v[20:23], v[32:35]\n"
"v_exp_f32 v41, v41\n"
"v_mfma_f32_16x16x32_bf16 v[32:35], a[72:75], v[24:27], v[32:35]\n"
"v_exp_f32 v42, v42\n"
"v_mfma_f32_16x16x32_bf16 v[32:35], a[76:79], v[28:31], v[32:35]\n"
"v_exp_f32 v43, v43\n"
"v_mfma_f32_16x16x32_bf16 v[36:39], a[80:83], v[16:19], -4.0\n"
"v_exp_f32 v44, v44\n"
"v_mfma_f32_16x16x32_bf16 v[36:39], a[84:87], v[20:23], v[36:39]\n"
"v_exp_f32 v45, v45\n"
"v_mfma_f32_16x16x32_bf16 v[36:39], a[88:91], v[24:27], v[36:39]\n"
"v_exp_f32 v46, v46\n"
"v_mfma_f32_16x16x32_bf16 v[36:39], a[92:95], v[28:31], v[36:39]\n"
"v_exp_f32 v47, v47\n"
"v_mfma_f32_16x16x32_bf16 v[116:119], v[60:63], v[48:51], 0\n"
"v_cvt_pk_bf16_f32 v52, v40, v41\n"
"v_cvt_pk_bf16_f32 v53, v42, v43\n"
"v_cvt_pk_bf16_f32 v54, v44, v45\n"
"v_cvt_pk_bf16_f32 v55, v46, v47\n"
"ds_write_b32 v109, v56 offset:10240\n"
"ds_write_b128 v108, v[52:55] offset:4096\n"
"v_mfma_f32_16x16x32_bf16 v[40:43], a[96:99], v[16:19], -4.0\n"
"v_exp_f32 v32, v32\n"
"v_mfma_f32_16x16x32_bf16 v[40:43], a[100:103], v[20:23], v[40:43]\n"
"v_exp_f32 v33, v33\n"
"v_mfma_f32_16x16x32_bf16 v[40:43], a[104:107], v[24:27], v[40:43]\n"
"v_exp_f32 v34, v34\n"
"v_mfma_f32_16x16x32_bf16 v[40:43], a[108:111], v[28:31], v[40:43]\n"
"v_exp_f32 v35, v35\n"
"v_mfma_f32_16x16x32_bf16 v[44:47], a[112:115], v[16:19], -4.0\n"
"v_exp_f32 v36, v36\n"
"v_mfma_f32_16x16x32_bf16 v[44:47], a[116:119], v[20:23], v[44:47]\n"
"v_exp_f32 v37, v37\n"
"v_mfma_f32_16x16x32_bf16 v[44:47], a[120:123], v[24:27], v[44:47]\n"
"v_exp_f32 v38, v38\n"
"v_mfma_f32_16x16x32_bf16 v[44:47], a[124:127], v[28:31], v[44:47]\n"
"v_exp_f32 v39, v39\n"
"v_mfma_f32_16x16x32_bf16 v[116:119], v[60:63], v[52:55], v[116:119]\n"
"v_cvt_pk_bf16_f32 v48, v32, v33\n"
"v_cvt_pk_bf16_f32 v49, v34, v35\n"
"v_cvt_pk_bf16_f32 v50, v36, v37\n"
"v_cvt_pk_bf16_f32 v51, v38, v39\n"
"ds_write_b128 v108, v[48:51] offset:8192\n"
"s_nop 0\n"
"v_mfma_f32_16x16x32_bf16 v[116:119], v[60:63], v[48:51], v[116:119]\n"
"v_exp_f32 v40, v40\n"
"v_exp_f32 v41, v41\n"
"v_exp_f32 v42, v42\n"
"v_exp_f32 v43, v43\n"
"v_exp_f32 v44, v44\n"
"v_exp_f32 v45, v45\n"
"v_exp_f32 v46, v46\n"
"v_exp_f32 v47, v47\n"
"v_cvt_pk_bf16_f32 v52, v40, v41\n"
"v_cvt_pk_bf16_f32 v53, v42, v43\n"
"v_cvt_pk_bf16_f32 v54, v44, v45\n"
"v_cvt_pk_bf16_f32 v55, v46, v47\n"
"ds_write_b128 v108, v[52:55] offset:12288\n"
"s_nop 0\n"
"v_mfma_f32_16x16x32_bf16 v[116:119], v[60:63], v[52:55], v[116:119]\n"
"s_nop 7\n"
"ds_write_b32 v109, v116 offset:0\n"
"s_waitcnt lgkmcnt(0)\n"
"s_barrier\n"
"s_mov_b32 s58, 0\n"
"s_branch L_done\n"
".p2align 6\n"
"L_pv_role:\n"
"s_sleep 8\n"
"s_add_u32 s40, s51, 0x4000\n"
"buffer_load_dwordx4 v[64:67], v124, s[20:23], s40 offen nt\n"
"s_add_u32 s40, s51, 0x5000\n"
"buffer_load_dwordx4 v[68:71], v124, s[20:23], s40 offen nt\n"
"s_add_u32 s40, s51, 0x6000\n"
"buffer_load_dwordx4 v[72:75], v124, s[20:23], s40 offen nt\n"
"s_add_u32 s40, s51, 0x7000\n"
"buffer_load_dwordx4 v[76:79], v124, s[20:23], s40 offen nt\n"
"s_lshl_b32 s48, s38, 6\n"
"s_add_u32 s59, s53, s48\n"
"s_add_u32 s39, s59, 0x20000\n"
"s_lshl_b32 s48, s38, 13\n"
"s_add_u32 s55, s48, 0x20000\n"
"v_and_b32 v113, 48, v124\n"
"v_lshrrev_b32 v121, 6, v124\n"
"v_lshl_add_u32 v121, v121, 8, v113\n"
"v_and_b32 v113, 15, v112\n"
"v_lshrrev_b32 v114, 4, v112\n"
"v_add_u32 v125, s55, v124\n"
"s_lshl_b32 s48, s38, 6\n"
"v_lshl_add_u32 v127, v114, 4, s48\n"
"v_lshl_add_u32 v127, v113, 8, v127\n"
"s_lshl_b32 s48, s38, 2\n"
"v_add_u32 v114, s48, v114\n"
"v_xor_b32 v113, v113, v114\n"
"v_lshlrev_b32 v113, 4, v113\n"
"v_lshl_add_u32 v126, v114, 8, v113\n"
"v_add_u32 v126, 0xc000, v126\n"
"s_mov_b32 s44, 0x3e0293ee\n"
"s_mov_b32 s45, 0xffff0000\n"
"s_mov_b32 s46, 0x39800000\n"
"s_mov_b32 s47, 0x1c000000\n"
"v_mov_b32 v122, 0\n"
"v_mov_b32 v123, 0\n"
"s_waitcnt vmcnt(7)\n"
"v_lshlrev_b32 v112, 16, v96\n"
"s_xor_b32 s43, s62, 0\n"
"s_add_u32 m0, s55, 0\n"
"s_add_u32 s43, s43, s59\n"
"buffer_load_dwordx4 v121, s[28:31], s43 offen offset:0 lds\n"
"s_xor_b32 s43, s62, 0x1000\n"
"s_add_u32 m0, s55, 0x400\n"
"s_add_u32 s43, s43, s59\n"
"buffer_load_dwordx4 v121, s[28:31], s43 offen offset:0 lds\n"
"v_and_b32 v113, s45, v96\n"
"v_mul_f32 v112, s44, v112\n"
"v_mul_f32 v113, s44, v113\n"
"v_cvt_pk_bf16_f32 v96, v112, v113\n"
"v_lshlrev_b32 v112, 16, v97\n"
"v_and_b32 v113, s45, v97\n"
"v_mul_f32 v112, s44, v112\n"
"v_mul_f32 v113, s44, v113\n"
"v_cvt_pk_bf16_f32 v97, v112, v113\n"
"v_lshlrev_b32 v112, 16, v98\n"
"v_and_b32 v113, s45, v98\n"
"v_mul_f32 v112, s44, v112\n"
"v_mul_f32 v113, s44, v113\n"
"v_cvt_pk_bf16_f32 v98, v112, v113\n"
"v_lshlrev_b32 v112, 16, v99\n"
"v_and_b32 v113, s45, v99\n"
"v_mul_f32 v112, s44, v112\n"
"v_mul_f32 v113, s44, v113\n"
"v_cvt_pk_bf16_f32 v99, v112, v113\n"
"ds_write_b128 v126, v[96:99] offset:0\n"
"s_waitcnt vmcnt(8)\n"
"v_lshlrev_b32 v112, 16, v100\n"
"s_xor_b32 s43, s62, 0x2000\n"
"s_add_u32 m0, s55, 0x800\n"
"s_add_u32 s43, s43, s59\n"
"buffer_load_dwordx4 v121, s[28:31], s43 offen offset:0 lds\n"
"s_xor_b32 s43, s62, 0x3000\n"
"s_add_u32 m0, s55, 0xc00\n"
"s_add_u32 s43, s43, s59\n"
"buffer_load_dwordx4 v121, s[28:31], s43 offen offset:0 lds\n"
"v_and_b32 v113, s45, v100\n"
"v_mul_f32 v112, s44, v112\n"
"v_mul_f32 v113, s44, v113\n"
"v_cvt_pk_bf16_f32 v100, v112, v113\n"
"v_lshlrev_b32 v112, 16, v101\n"
"v_and_b32 v113, s45, v101\n"
"v_mul_f32 v112, s44, v112\n"
"v_mul_f32 v113, s44, v113\n"
"v_cvt_pk_bf16_f32 v101, v112, v113\n"
"v_lshlrev_b32 v112, 16, v102\n"
"v_and_b32 v113, s45, v102\n"
"v_mul_f32 v112, s44, v112\n"
"v_mul_f32 v113, s44, v113\n"
"v_cvt_pk_bf16_f32 v102, v112, v113\n"
"v_lshlrev_b32 v112, 16, v103\n"
"v_and_b32 v113, s45, v103\n"
"v_mul_f32 v112, s44, v112\n"
"v_mul_f32 v113, s44, v113\n"
"v_cvt_pk_bf16_f32 v103, v112, v113\n"
"ds_write_b128 v126, v[100:103] offset:4096\n"
"s_waitcnt vmcnt(9)\n"
"v_lshlrev_b32 v112, 16, v104\n"
"s_xor_b32 s43, s62, 0x4000\n"
"s_add_u32 m0, s55, 0x1000\n"
"s_add_u32 s43, s43, s59\n"
"buffer_load_dwordx4 v121, s[28:31], s43 offen offset:0 lds\n"
"s_xor_b32 s43, s62, 0x5000\n"
"s_add_u32 m0, s55, 0x1400\n"
"s_add_u32 s43, s43, s59\n"
"buffer_load_dwordx4 v121, s[28:31], s43 offen offset:0 lds\n"
"v_and_b32 v113, s45, v104\n"
"v_mul_f32 v112, s44, v112\n"
"v_mul_f32 v113, s44, v113\n"
"v_cvt_pk_bf16_f32 v104, v112, v113\n"
"v_lshlrev_b32 v112, 16, v105\n"
"v_and_b32 v113, s45, v105\n"
"v_mul_f32 v112, s44, v112\n"
"v_mul_f32 v113, s44, v113\n"
"v_cvt_pk_bf16_f32 v105, v112, v113\n"
"v_lshlrev_b32 v112, 16, v106\n"
"v_and_b32 v113, s45, v106\n"
"v_mul_f32 v112, s44, v112\n"
"v_mul_f32 v113, s44, v113\n"
"v_cvt_pk_bf16_f32 v106, v112, v113\n"
"v_lshlrev_b32 v112, 16, v107\n"
"v_and_b32 v113, s45, v107\n"
"v_mul_f32 v112, s44, v112\n"
"v_mul_f32 v113, s44, v113\n"
"v_cvt_pk_bf16_f32 v107, v112, v113\n"
"ds_write_b128 v126, v[104:107] offset:8192\n"
"s_waitcnt vmcnt(10)\n"
"v_lshlrev_b32 v112, 16, v108\n"
"s_xor_b32 s43, s62, 0x6000\n"
"s_add_u32 m0, s55, 0x1800\n"
"s_add_u32 s43, s43, s59\n"
"buffer_load_dwordx4 v121, s[28:31], s43 offen offset:0 lds\n"
"s_xor_b32 s43, s62, 0x7000\n"
"s_add_u32 m0, s55, 0x1c00\n"
"s_add_u32 s43, s43, s59\n"
"buffer_load_dwordx4 v121, s[28:31], s43 offen offset:0 lds\n"
"v_and_b32 v113, s45, v108\n"
"v_mul_f32 v112, s44, v112\n"
"v_mul_f32 v113, s44, v113\n"
"v_cvt_pk_bf16_f32 v108, v112, v113\n"
"v_lshlrev_b32 v112, 16, v109\n"
"v_and_b32 v113, s45, v109\n"
"v_mul_f32 v112, s44, v112\n"
"v_mul_f32 v113, s44, v113\n"
"v_cvt_pk_bf16_f32 v109, v112, v113\n"
"v_lshlrev_b32 v112, 16, v110\n"
"v_and_b32 v113, s45, v110\n"
"v_mul_f32 v112, s44, v112\n"
"v_mul_f32 v113, s44, v113\n"
"v_cvt_pk_bf16_f32 v110, v112, v113\n"
"v_lshlrev_b32 v112, 16, v111\n"
"v_and_b32 v113, s45, v111\n"
"v_mul_f32 v112, s44, v112\n"
"v_mul_f32 v113, s44, v113\n"
"v_cvt_pk_bf16_f32 v111, v112, v113\n"
"ds_write_b128 v126, v[108:111] offset:12288\n"
"s_waitcnt lgkmcnt(0)\n"
"s_barrier\n"
"s_waitcnt vmcnt(11)\n"
"v_lshlrev_b32 v112, 16, v64\n"
"v_and_b32 v113, s45, v64\n"
"v_mul_f32 v112, s44, v112\n"
"v_mul_f32 v113, s44, v113\n"
"v_cvt_pk_bf16_f32 v64, v112, v113\n"
"v_lshlrev_b32 v112, 16, v65\n"
"v_and_b32 v113, s45, v65\n"
"v_mul_f32 v112, s44, v112\n"
"v_mul_f32 v113, s44, v113\n"
"v_cvt_pk_bf16_f32 v65, v112, v113\n"
"v_lshlrev_b32 v112, 16, v66\n"
"v_and_b32 v113, s45, v66\n"
"v_mul_f32 v112, s44, v112\n"
"v_mul_f32 v113, s44, v113\n"
"v_cvt_pk_bf16_f32 v66, v112, v113\n"
"v_lshlrev_b32 v112, 16, v67\n"
"v_and_b32 v113, s45, v67\n"
"v_mul_f32 v112, s44, v112\n"
"v_mul_f32 v113, s44, v113\n"
"v_cvt_pk_bf16_f32 v67, v112, v113\n"
"ds_write_b128 v126, v[64:67] offset:16384\n"
"s_waitcnt vmcnt(10)\n"
"v_lshlrev_b32 v112, 16, v68\n"
"v_and_b32 v113, s45, v68\n"
"v_mul_f32 v112, s44, v112\n"
"v_mul_f32 v113, s44, v113\n"
"v_cvt_pk_bf16_f32 v68, v112, v113\n"
"v_lshlrev_b32 v112, 16, v69\n"
"v_and_b32 v113, s45, v69\n"
"v_mul_f32 v112, s44, v112\n"
"v_mul_f32 v113, s44, v113\n"
"v_cvt_pk_bf16_f32 v69, v112, v113\n"
"v_lshlrev_b32 v112, 16, v70\n"
"v_and_b32 v113, s45, v70\n"
"v_mul_f32 v112, s44, v112\n"
"v_mul_f32 v113, s44, v113\n"
"v_cvt_pk_bf16_f32 v70, v112, v113\n"
"v_lshlrev_b32 v112, 16, v71\n"
"v_and_b32 v113, s45, v71\n"
"v_mul_f32 v112, s44, v112\n"
"v_mul_f32 v113, s44, v113\n"
"v_cvt_pk_bf16_f32 v71, v112, v113\n"
"ds_write_b128 v126, v[68:71] offset:20480\n"
"s_waitcnt vmcnt(9)\n"
"v_lshlrev_b32 v112, 16, v72\n"
"v_and_b32 v113, s45, v72\n"
"v_mul_f32 v112, s44, v112\n"
"v_mul_f32 v113, s44, v113\n"
"v_cvt_pk_bf16_f32 v72, v112, v113\n"
"v_lshlrev_b32 v112, 16, v73\n"
"v_and_b32 v113, s45, v73\n"
"v_mul_f32 v112, s44, v112\n"
"v_mul_f32 v113, s44, v113\n"
"v_cvt_pk_bf16_f32 v73, v112, v113\n"
"v_lshlrev_b32 v112, 16, v74\n"
"v_and_b32 v113, s45, v74\n"
"v_mul_f32 v112, s44, v112\n"
"v_mul_f32 v113, s44, v113\n"
"v_cvt_pk_bf16_f32 v74, v112, v113\n"
"v_lshlrev_b32 v112, 16, v75\n"
"v_and_b32 v113, s45, v75\n"
"v_mul_f32 v112, s44, v112\n"
"v_mul_f32 v113, s44, v113\n"
"v_cvt_pk_bf16_f32 v75, v112, v113\n"
"ds_write_b128 v126, v[72:75] offset:24576\n"
"s_waitcnt vmcnt(8)\n"
"v_lshlrev_b32 v112, 16, v76\n"
"v_and_b32 v113, s45, v76\n"
"v_mul_f32 v112, s44, v112\n"
"v_mul_f32 v113, s44, v113\n"
"v_cvt_pk_bf16_f32 v76, v112, v113\n"
"v_lshlrev_b32 v112, 16, v77\n"
"v_and_b32 v113, s45, v77\n"
"v_mul_f32 v112, s44, v112\n"
"v_mul_f32 v113, s44, v113\n"
"v_cvt_pk_bf16_f32 v77, v112, v113\n"
"v_lshlrev_b32 v112, 16, v78\n"
"v_and_b32 v113, s45, v78\n"
"v_mul_f32 v112, s44, v112\n"
"v_mul_f32 v113, s44, v113\n"
"v_cvt_pk_bf16_f32 v78, v112, v113\n"
"v_lshlrev_b32 v112, 16, v79\n"
"v_and_b32 v113, s45, v79\n"
"v_mul_f32 v112, s44, v112\n"
"v_mul_f32 v113, s44, v113\n"
"v_cvt_pk_bf16_f32 v79, v112, v113\n"
"ds_write_b128 v126, v[76:79] offset:28672\n"
"s_waitcnt lgkmcnt(0)\n"
"s_barrier\n"
"s_waitcnt vmcnt(7)\n"
"ds_read_b64_tr_b16 a[0:1], v125 offset:0\n"
"ds_read_b64_tr_b16 a[4:5], v125 offset:8\n"
"s_waitcnt vmcnt(6)\n"
"ds_read_b64_tr_b16 a[2:3], v125 offset:1024\n"
"ds_read_b64_tr_b16 a[6:7], v125 offset:1032\n"
"s_waitcnt vmcnt(5)\n"
"ds_read_b64_tr_b16 a[8:9], v125 offset:2048\n"
"ds_read_b64_tr_b16 a[12:13], v125 offset:2056\n"
"s_waitcnt lgkmcnt(4)\n"
"s_xor_b32 s43, s62, 0x8000\n"
"s_add_u32 m0, s55, 0\n"
"s_add_u32 s43, s43, s59\n"
"buffer_load_dwordx4 v121, s[28:31], s43 offen offset:0 lds\n"
"s_waitcnt vmcnt(5)\n"
"ds_read_b64_tr_b16 a[10:11], v125 offset:3072\n"
"ds_read_b64_tr_b16 a[14:15], v125 offset:3080\n"
"s_waitcnt lgkmcnt(4)\n"
"s_xor_b32 s43, s62, 0x9000\n"
"s_add_u32 m0, s55, 0x400\n"
"s_add_u32 s43, s43, s59\n"
"buffer_load_dwordx4 v121, s[28:31], s43 offen offset:0 lds\n"
"s_waitcnt vmcnt(5)\n"
"ds_read_b64_tr_b16 a[16:17], v125 offset:4096\n"
"ds_read_b64_tr_b16 a[20:21], v125 offset:4104\n"
"s_waitcnt lgkmcnt(4)\n"
"s_xor_b32 s43, s62, 0xa000\n"
"s_add_u32 m0, s55, 0x800\n"
"s_add_u32 s43, s43, s59\n"
"buffer_load_dwordx4 v121, s[28:31], s43 offen offset:0 lds\n"
"s_waitcnt vmcnt(5)\n"
"ds_read_b64_tr_b16 a[18:19], v125 offset:5120\n"
"ds_read_b64_tr_b16 a[22:23], v125 offset:5128\n"
"s_waitcnt lgkmcnt(4)\n"
"s_xor_b32 s43, s62, 0xb000\n"
"s_add_u32 m0, s55, 0xc00\n"
"s_add_u32 s43, s43, s59\n"
"buffer_load_dwordx4 v121, s[28:31], s43 offen offset:0 lds\n"
"s_waitcnt vmcnt(5)\n"
"ds_read_b64_tr_b16 a[24:25], v125 offset:6144\n"
"ds_read_b64_tr_b16 a[28:29], v125 offset:6152\n"
"s_waitcnt lgkmcnt(4)\n"
"s_xor_b32 s43, s62, 0xc000\n"
"s_add_u32 m0, s55, 0x1000\n"
"s_add_u32 s43, s43, s59\n"
"buffer_load_dwordx4 v121, s[28:31], s43 offen offset:0 lds\n"
"s_waitcnt vmcnt(5)\n"
"ds_read_b64_tr_b16 a[26:27], v125 offset:7168\n"
"ds_read_b64_tr_b16 a[30:31], v125 offset:7176\n"
"s_waitcnt lgkmcnt(4)\n"
"s_xor_b32 s43, s62, 0xd000\n"
"s_add_u32 m0, s55, 0x1400\n"
"s_add_u32 s43, s43, s59\n"
"buffer_load_dwordx4 v121, s[28:31], s43 offen offset:0 lds\n"
"s_waitcnt lgkmcnt(2)\n"
"s_xor_b32 s43, s62, 0xe000\n"
"s_add_u32 m0, s55, 0x1800\n"
"s_add_u32 s43, s43, s59\n"
"buffer_load_dwordx4 v121, s[28:31], s43 offen offset:0 lds\n"
"s_waitcnt lgkmcnt(0)\n"
"s_xor_b32 s43, s62, 0xf000\n"
"s_add_u32 m0, s55, 0x1c00\n"
"s_add_u32 s43, s43, s59\n"
"buffer_load_dwordx4 v121, s[28:31], s43 offen offset:0 lds\n"
"s_barrier\n"
"ds_read_b128 v[64:67], v124 offset:0\n"
"ds_read_b128 v[68:71], v124 offset:1024\n"
"ds_read_b128 v[72:75], v124 offset:2048\n"
"ds_read_b128 v[76:79], v124 offset:3072\n"
"ds_read_b128 v[80:83], v124 offset:4096\n"
"ds_read_b128 v[84:87], v124 offset:5120\n"
"ds_read_b128 v[88:91], v124 offset:6144\n"
"ds_read_b128 v[92:95], v124 offset:7168\n"
"s_waitcnt lgkmcnt(4)\n"
"v_mfma_f32_16x16x32_bf16 v[0:3], a[0:3], v[64:67], 0\n"
"s_setprio 2\n"
"v_mfma_f32_16x16x32_bf16 v[0:3], a[8:11], v[68:71], v[0:3]\n"
"v_mfma_f32_16x16x32_bf16 v[0:3], a[16:19], v[72:75], v[0:3]\n"
"v_mfma_f32_16x16x32_bf16 v[0:3], a[24:27], v[76:79], v[0:3]\n"
"v_mfma_f32_16x16x32_bf16 v[4:7], a[4:7], v[64:67], 0\n"
"v_mfma_f32_16x16x32_bf16 v[4:7], a[12:15], v[68:71], v[4:7]\n"
"v_mfma_f32_16x16x32_bf16 v[4:7], a[20:23], v[72:75], v[4:7]\n"
"v_mfma_f32_16x16x32_bf16 v[4:7], a[28:31], v[76:79], v[4:7]\n"
"s_setprio 0\n"
"ds_read_b128 v[64:67], v124 offset:8192\n"
"ds_read_b128 v[68:71], v124 offset:9216\n"
"ds_read_b128 v[72:75], v124 offset:10240\n"
"ds_read_b128 v[76:79], v124 offset:11264\n"
"s_waitcnt lgkmcnt(4)\n"
"v_mfma_f32_16x16x32_bf16 v[8:11], a[0:3], v[80:83], 0\n"
"s_setprio 2\n"
"v_mfma_f32_16x16x32_bf16 v[8:11], a[8:11], v[84:87], v[8:11]\n"
"v_mfma_f32_16x16x32_bf16 v[8:11], a[16:19], v[88:91], v[8:11]\n"
"v_mfma_f32_16x16x32_bf16 v[8:11], a[24:27], v[92:95], v[8:11]\n"
"v_mfma_f32_16x16x32_bf16 v[12:15], a[4:7], v[80:83], 0\n"
"v_mfma_f32_16x16x32_bf16 v[12:15], a[12:15], v[84:87], v[12:15]\n"
"v_mfma_f32_16x16x32_bf16 v[12:15], a[20:23], v[88:91], v[12:15]\n"
"v_mfma_f32_16x16x32_bf16 v[12:15], a[28:31], v[92:95], v[12:15]\n"
"s_setprio 0\n"
"ds_read_b128 v[80:83], v124 offset:12288\n"
"ds_read_b128 v[84:87], v124 offset:13312\n"
"ds_read_b128 v[88:91], v124 offset:14336\n"
"ds_read_b128 v[92:95], v124 offset:15360\n"
"s_waitcnt lgkmcnt(4)\n"
"v_mfma_f32_16x16x32_bf16 v[16:19], a[0:3], v[64:67], 0\n"
"s_setprio 2\n"
"v_mfma_f32_16x16x32_bf16 v[16:19], a[8:11], v[68:71], v[16:19]\n"
"v_mfma_f32_16x16x32_bf16 v[16:19], a[16:19], v[72:75], v[16:19]\n"
"v_mfma_f32_16x16x32_bf16 v[16:19], a[24:27], v[76:79], v[16:19]\n"
"v_mfma_f32_16x16x32_bf16 v[20:23], a[4:7], v[64:67], 0\n"
"v_mfma_f32_16x16x32_bf16 v[20:23], a[12:15], v[68:71], v[20:23]\n"
"v_mfma_f32_16x16x32_bf16 v[20:23], a[20:23], v[72:75], v[20:23]\n"
"v_mfma_f32_16x16x32_bf16 v[20:23], a[28:31], v[76:79], v[20:23]\n"
"s_setprio 0\n"
"ds_read_b128 v[64:67], v124 offset:16384\n"
"ds_read_b128 v[68:71], v124 offset:17408\n"
"ds_read_b128 v[72:75], v124 offset:18432\n"
"ds_read_b128 v[76:79], v124 offset:19456\n"
"s_waitcnt vmcnt(7)\n"
"ds_read_b64_tr_b16 a[32:33], v125 offset:0\n"
"ds_read_b64_tr_b16 a[36:37], v125 offset:8\n"
"s_waitcnt lgkmcnt(6)\n"
"v_mfma_f32_16x16x32_bf16 v[24:27], a[0:3], v[80:83], 0\n"
"s_setprio 2\n"
"v_mfma_f32_16x16x32_bf16 v[24:27], a[8:11], v[84:87], v[24:27]\n"
"v_mfma_f32_16x16x32_bf16 v[24:27], a[16:19], v[88:91], v[24:27]\n"
"v_mfma_f32_16x16x32_bf16 v[24:27], a[24:27], v[92:95], v[24:27]\n"
"v_mfma_f32_16x16x32_bf16 v[28:31], a[4:7], v[80:83], 0\n"
"v_mfma_f32_16x16x32_bf16 v[28:31], a[12:15], v[84:87], v[28:31]\n"
"v_mfma_f32_16x16x32_bf16 v[28:31], a[20:23], v[88:91], v[28:31]\n"
"v_mfma_f32_16x16x32_bf16 v[28:31], a[28:31], v[92:95], v[28:31]\n"
"s_setprio 0\n"
"ds_read_b128 v[80:83], v124 offset:20480\n"
"ds_read_b128 v[84:87], v124 offset:21504\n"
"ds_read_b128 v[88:91], v124 offset:22528\n"
"ds_read_b128 v[92:95], v124 offset:23552\n"
"s_waitcnt vmcnt(6)\n"
"ds_read_b64_tr_b16 a[34:35], v125 offset:1024\n"
"ds_read_b64_tr_b16 a[38:39], v125 offset:1032\n"
"s_waitcnt lgkmcnt(6)\n"
"s_xor_b32 s43, s62, 0x10000\n"
"s_add_u32 m0, s55, 0\n"
"s_add_u32 s43, s43, s59\n"
"buffer_load_dwordx4 v121, s[28:31], s43 offen offset:0 lds\n"
"v_mfma_f32_16x16x32_bf16 v[32:35], a[0:3], v[64:67], 0\n"
"s_setprio 2\n"
"v_mfma_f32_16x16x32_bf16 v[32:35], a[8:11], v[68:71], v[32:35]\n"
"v_mfma_f32_16x16x32_bf16 v[32:35], a[16:19], v[72:75], v[32:35]\n"
"v_mfma_f32_16x16x32_bf16 v[32:35], a[24:27], v[76:79], v[32:35]\n"
"v_mfma_f32_16x16x32_bf16 v[36:39], a[4:7], v[64:67], 0\n"
"v_mfma_f32_16x16x32_bf16 v[36:39], a[12:15], v[68:71], v[36:39]\n"
"v_mfma_f32_16x16x32_bf16 v[36:39], a[20:23], v[72:75], v[36:39]\n"
"v_mfma_f32_16x16x32_bf16 v[36:39], a[28:31], v[76:79], v[36:39]\n"
"s_setprio 0\n"
"ds_read_b128 v[64:67], v124 offset:24576\n"
"ds_read_b128 v[68:71], v124 offset:25600\n"
"ds_read_b128 v[72:75], v124 offset:26624\n"
"ds_read_b128 v[76:79], v124 offset:27648\n"
"s_waitcnt vmcnt(6)\n"
"ds_read_b64_tr_b16 a[40:41], v125 offset:2048\n"
"ds_read_b64_tr_b16 a[44:45], v125 offset:2056\n"
"s_waitcnt lgkmcnt(6)\n"
"s_xor_b32 s43, s62, 0x11000\n"
"s_add_u32 m0, s55, 0x400\n"
"s_add_u32 s43, s43, s59\n"
"buffer_load_dwordx4 v121, s[28:31], s43 offen offset:0 lds\n"
"v_mfma_f32_16x16x32_bf16 v[40:43], a[0:3], v[80:83], 0\n"
"s_setprio 2\n"
"v_mfma_f32_16x16x32_bf16 v[40:43], a[8:11], v[84:87], v[40:43]\n"
"v_mfma_f32_16x16x32_bf16 v[40:43], a[16:19], v[88:91], v[40:43]\n"
"v_mfma_f32_16x16x32_bf16 v[40:43], a[24:27], v[92:95], v[40:43]\n"
"v_mfma_f32_16x16x32_bf16 v[44:47], a[4:7], v[80:83], 0\n"
"v_mfma_f32_16x16x32_bf16 v[44:47], a[12:15], v[84:87], v[44:47]\n"
"v_mfma_f32_16x16x32_bf16 v[44:47], a[20:23], v[88:91], v[44:47]\n"
"s_waitcnt lgkmcnt(0)\n"
"s_setprio 0\n"
"v_mfma_f32_16x16x32_bf16 v[44:47], a[28:31], v[92:95], v[44:47]\n"
"s_barrier\n"
"ds_read_b128 v[80:83], v124 offset:28672\n"
"ds_read_b128 v[84:87], v124 offset:29696\n"
"ds_read_b128 v[88:91], v124 offset:30720\n"
"ds_read_b128 v[92:95], v124 offset:31744\n"
"s_waitcnt vmcnt(6)\n"
"ds_read_b64_tr_b16 a[42:43], v125 offset:3072\n"
"ds_read_b64_tr_b16 a[46:47], v125 offset:3080\n"
"s_xor_b32 s43, s62, 0x12000\n"
"s_add_u32 m0, s55, 0x800\n"
"s_add_u32 s43, s43, s59\n"
"buffer_load_dwordx4 v121, s[28:31], s43 offen offset:0 lds\n"
"v_mfma_f32_16x16x32_bf16 v[48:51], a[0:3], v[64:67], 0\n"
"s_setprio 2\n"
"v_mfma_f32_16x16x32_bf16 v[48:51], a[8:11], v[68:71], v[48:51]\n"
"v_mfma_f32_16x16x32_bf16 v[48:51], a[16:19], v[72:75], v[48:51]\n"
"v_mfma_f32_16x16x32_bf16 v[48:51], a[24:27], v[76:79], v[48:51]\n"
"v_mfma_f32_16x16x32_bf16 v[52:55], a[4:7], v[64:67], 0\n"
"v_mfma_f32_16x16x32_bf16 v[52:55], a[12:15], v[68:71], v[52:55]\n"
"v_mfma_f32_16x16x32_bf16 v[52:55], a[20:23], v[72:75], v[52:55]\n"
"s_waitcnt lgkmcnt(0)\n"
"s_setprio 0\n"
"v_mfma_f32_16x16x32_bf16 v[52:55], a[28:31], v[76:79], v[52:55]\n"
"s_barrier\n"
"ds_read_b128 v[64:67], v124 offset:32768\n"
"ds_read_b128 v[68:71], v124 offset:33792\n"
"ds_read_b128 v[72:75], v124 offset:34816\n"
"ds_read_b128 v[76:79], v124 offset:35840\n"
"s_waitcnt vmcnt(6)\n"
"ds_read_b64_tr_b16 a[48:49], v125 offset:4096\n"
"ds_read_b64_tr_b16 a[52:53], v125 offset:4104\n"
"s_waitcnt vmcnt(5)\n"
"ds_read_b64_tr_b16 a[50:51], v125 offset:5120\n"
"ds_read_b64_tr_b16 a[54:55], v125 offset:5128\n"
"s_xor_b32 s43, s62, 0x13000\n"
"s_add_u32 m0, s55, 0xc00\n"
"s_add_u32 s43, s43, s59\n"
"buffer_load_dwordx4 v121, s[28:31], s43 offen offset:0 lds\n"
"v_mfma_f32_16x16x32_bf16 v[56:59], a[0:3], v[80:83], 0\n"
"s_setprio 2\n"
"v_mfma_f32_16x16x32_bf16 v[56:59], a[8:11], v[84:87], v[56:59]\n"
"v_mfma_f32_16x16x32_bf16 v[56:59], a[16:19], v[88:91], v[56:59]\n"
"v_mfma_f32_16x16x32_bf16 v[56:59], a[24:27], v[92:95], v[56:59]\n"
"v_mfma_f32_16x16x32_bf16 v[60:63], a[4:7], v[80:83], 0\n"
"v_mfma_f32_16x16x32_bf16 v[60:63], a[12:15], v[84:87], v[60:63]\n"
"v_mfma_f32_16x16x32_bf16 v[60:63], a[20:23], v[88:91], v[60:63]\n"
"v_mfma_f32_16x16x32_bf16 v[60:63], a[28:31], v[92:95], v[60:63]\n"
"s_setprio 0\n"
"ds_read_b128 v[80:83], v124 offset:0\n"
"ds_read_b128 v[84:87], v124 offset:1024\n"
"ds_read_b128 v[88:91], v124 offset:2048\n"
"ds_read_b128 v[92:95], v124 offset:3072\n"
"s_waitcnt vmcnt(5)\n"
"ds_read_b64_tr_b16 a[56:57], v125 offset:6144\n"
"ds_read_b64_tr_b16 a[60:61], v125 offset:6152\n"
"s_waitcnt vmcnt(4)\n"
"ds_read_b64_tr_b16 a[58:59], v125 offset:7168\n"
"ds_read_b64_tr_b16 a[62:63], v125 offset:7176\n"
"s_waitcnt lgkmcnt(10)\n"
"s_xor_b32 s43, s62, 0x14000\n"
"s_add_u32 m0, s55, 0x1000\n"
"s_add_u32 s43, s43, s59\n"
"buffer_load_dwordx4 v121, s[28:31], s43 offen offset:0 lds\n"
"s_waitcnt lgkmcnt(8)\n"
"s_xor_b32 s43, s62, 0x15000\n"
"s_add_u32 m0, s55, 0x1400\n"
"s_add_u32 s43, s43, s59\n"
"buffer_load_dwordx4 v121, s[28:31], s43 offen offset:0 lds\n"
"v_mfma_f32_16x16x32_bf16 v[0:3], a[32:35], v[64:67], v[0:3]\n"
"s_setprio 2\n"
"v_mfma_f32_16x16x32_bf16 v[0:3], a[40:43], v[68:71], v[0:3]\n"
"v_mfma_f32_16x16x32_bf16 v[0:3], a[48:51], v[72:75], v[0:3]\n"
"s_waitcnt lgkmcnt(1)\n"
"v_mfma_f32_16x16x32_bf16 v[0:3], a[56:59], v[76:79], v[0:3]\n"
"v_mfma_f32_16x16x32_bf16 v[4:7], a[36:39], v[64:67], v[4:7]\n"
"v_mfma_f32_16x16x32_bf16 v[4:7], a[44:47], v[68:71], v[4:7]\n"
"v_mfma_f32_16x16x32_bf16 v[4:7], a[52:55], v[72:75], v[4:7]\n"
"s_waitcnt lgkmcnt(0)\n"
"v_mfma_f32_16x16x32_bf16 v[4:7], a[60:63], v[76:79], v[4:7]\n"
"s_setprio 0\n"
"ds_read_b128 v[64:67], v124 offset:4096\n"
"ds_read_b128 v[68:71], v124 offset:5120\n"
"ds_read_b128 v[72:75], v124 offset:6144\n"
"ds_read_b128 v[76:79], v124 offset:7168\n"
"s_add_u32 s40, s51, 0x8000\n"
"buffer_load_dwordx4 v[96:99], v124, s[20:23], s40 offen nt\n"
"s_xor_b32 s43, s62, 0x16000\n"
"s_add_u32 m0, s55, 0x1800\n"
"s_add_u32 s43, s43, s59\n"
"buffer_load_dwordx4 v121, s[28:31], s43 offen offset:0 lds\n"
"s_xor_b32 s43, s62, 0x17000\n"
"s_add_u32 m0, s55, 0x1c00\n"
"s_add_u32 s43, s43, s59\n"
"buffer_load_dwordx4 v121, s[28:31], s43 offen offset:0 lds\n"
"v_mfma_f32_16x16x32_bf16 v[8:11], a[32:35], v[80:83], v[8:11]\n"
"s_setprio 2\n"
"v_mfma_f32_16x16x32_bf16 v[8:11], a[40:43], v[84:87], v[8:11]\n"
"v_mfma_f32_16x16x32_bf16 v[8:11], a[48:51], v[88:91], v[8:11]\n"
"v_mfma_f32_16x16x32_bf16 v[8:11], a[56:59], v[92:95], v[8:11]\n"
"v_mfma_f32_16x16x32_bf16 v[12:15], a[36:39], v[80:83], v[12:15]\n"
"v_mfma_f32_16x16x32_bf16 v[12:15], a[44:47], v[84:87], v[12:15]\n"
"v_mfma_f32_16x16x32_bf16 v[12:15], a[52:55], v[88:91], v[12:15]\n"
"v_mfma_f32_16x16x32_bf16 v[12:15], a[60:63], v[92:95], v[12:15]\n"
"s_setprio 0\n"
"ds_read_b128 v[80:83], v124 offset:8192\n"
"ds_read_b128 v[84:87], v124 offset:9216\n"
"ds_read_b128 v[88:91], v124 offset:10240\n"
"ds_read_b128 v[92:95], v124 offset:11264\n"
"s_waitcnt lgkmcnt(4)\n"
"v_mfma_f32_16x16x32_bf16 v[16:19], a[32:35], v[64:67], v[16:19]\n"
"s_setprio 2\n"
"v_mfma_f32_16x16x32_bf16 v[16:19], a[40:43], v[68:71], v[16:19]\n"
"v_mfma_f32_16x16x32_bf16 v[16:19], a[48:51], v[72:75], v[16:19]\n"
"v_mfma_f32_16x16x32_bf16 v[16:19], a[56:59], v[76:79], v[16:19]\n"
"v_mfma_f32_16x16x32_bf16 v[20:23], a[36:39], v[64:67], v[20:23]\n"
"v_mfma_f32_16x16x32_bf16 v[20:23], a[44:47], v[68:71], v[20:23]\n"
"v_mfma_f32_16x16x32_bf16 v[20:23], a[52:55], v[72:75], v[20:23]\n"
"s_waitcnt lgkmcnt(0)\n"
"s_setprio 0\n"
"v_mfma_f32_16x16x32_bf16 v[20:23], a[60:63], v[76:79], v[20:23]\n"
"s_barrier\n"
"ds_read_b128 v[64:67], v124 offset:12288\n"
"ds_read_b128 v[68:71], v124 offset:13312\n"
"ds_read_b128 v[72:75], v124 offset:14336\n"
"ds_read_b128 v[76:79], v124 offset:15360\n"
"v_mfma_f32_16x16x32_bf16 v[24:27], a[32:35], v[80:83], v[24:27]\n"
"s_setprio 2\n"
"v_mfma_f32_16x16x32_bf16 v[24:27], a[40:43], v[84:87], v[24:27]\n"
"v_mfma_f32_16x16x32_bf16 v[24:27], a[48:51], v[88:91], v[24:27]\n"
"v_mfma_f32_16x16x32_bf16 v[24:27], a[56:59], v[92:95], v[24:27]\n"
"v_mfma_f32_16x16x32_bf16 v[28:31], a[36:39], v[80:83], v[28:31]\n"
"v_mfma_f32_16x16x32_bf16 v[28:31], a[44:47], v[84:87], v[28:31]\n"
"v_mfma_f32_16x16x32_bf16 v[28:31], a[52:55], v[88:91], v[28:31]\n"
"v_mfma_f32_16x16x32_bf16 v[28:31], a[60:63], v[92:95], v[28:31]\n"
"s_setprio 0\n"
"ds_read_b128 v[80:83], v124 offset:16384\n"
"ds_read_b128 v[84:87], v124 offset:17408\n"
"ds_read_b128 v[88:91], v124 offset:18432\n"
"ds_read_b128 v[92:95], v124 offset:19456\n"
"s_waitcnt vmcnt(8)\n"
"ds_read_b64_tr_b16 a[64:65], v125 offset:0\n"
"ds_read_b64_tr_b16 a[68:69], v125 offset:8\n"
"s_waitcnt lgkmcnt(6)\n"
"v_mfma_f32_16x16x32_bf16 v[32:35], a[32:35], v[64:67], v[32:35]\n"
"s_setprio 2\n"
"v_mfma_f32_16x16x32_bf16 v[32:35], a[40:43], v[68:71], v[32:35]\n"
"v_mfma_f32_16x16x32_bf16 v[32:35], a[48:51], v[72:75], v[32:35]\n"
"v_mfma_f32_16x16x32_bf16 v[32:35], a[56:59], v[76:79], v[32:35]\n"
"v_mfma_f32_16x16x32_bf16 v[36:39], a[36:39], v[64:67], v[36:39]\n"
"v_mfma_f32_16x16x32_bf16 v[36:39], a[44:47], v[68:71], v[36:39]\n"
"v_mfma_f32_16x16x32_bf16 v[36:39], a[52:55], v[72:75], v[36:39]\n"
"v_mfma_f32_16x16x32_bf16 v[36:39], a[60:63], v[76:79], v[36:39]\n"
"s_setprio 0\n"
"ds_read_b128 v[64:67], v124 offset:20480\n"
"ds_read_b128 v[68:71], v124 offset:21504\n"
"ds_read_b128 v[72:75], v124 offset:22528\n"
"ds_read_b128 v[76:79], v124 offset:23552\n"
"s_waitcnt vmcnt(7)\n"
"ds_read_b64_tr_b16 a[66:67], v125 offset:1024\n"
"ds_read_b64_tr_b16 a[70:71], v125 offset:1032\n"
"s_add_u32 s40, s51, 0x9000\n"
"buffer_load_dwordx4 v[100:103], v124, s[20:23], s40 offen nt\n"
"s_waitcnt lgkmcnt(6)\n"
"s_xor_b32 s43, s62, 0x18000\n"
"s_add_u32 m0, s55, 0\n"
"s_add_u32 s43, s43, s59\n"
"buffer_load_dwordx4 v121, s[28:31], s43 offen offset:0 lds\n"
"v_mfma_f32_16x16x32_bf16 v[40:43], a[32:35], v[80:83], v[40:43]\n"
"s_setprio 2\n"
"v_mfma_f32_16x16x32_bf16 v[40:43], a[40:43], v[84:87], v[40:43]\n"
"v_mfma_f32_16x16x32_bf16 v[40:43], a[48:51], v[88:91], v[40:43]\n"
"v_mfma_f32_16x16x32_bf16 v[40:43], a[56:59], v[92:95], v[40:43]\n"
"v_mfma_f32_16x16x32_bf16 v[44:47], a[36:39], v[80:83], v[44:47]\n"
"v_mfma_f32_16x16x32_bf16 v[44:47], a[44:47], v[84:87], v[44:47]\n"
"v_mfma_f32_16x16x32_bf16 v[44:47], a[52:55], v[88:91], v[44:47]\n"
"v_mfma_f32_16x16x32_bf16 v[44:47], a[60:63], v[92:95], v[44:47]\n"
"s_setprio 0\n"
"ds_read_b128 v[80:83], v124 offset:24576\n"
"ds_read_b128 v[84:87], v124 offset:25600\n"
"ds_read_b128 v[88:91], v124 offset:26624\n"
"ds_read_b128 v[92:95], v124 offset:27648\n"
"s_waitcnt vmcnt(8)\n"
"ds_read_b64_tr_b16 a[72:73], v125 offset:2048\n"
"ds_read_b64_tr_b16 a[76:77], v125 offset:2056\n"
"s_waitcnt vmcnt(7)\n"
"ds_read_b64_tr_b16 a[74:75], v125 offset:3072\n"
"ds_read_b64_tr_b16 a[78:79], v125 offset:3080\n"
"s_waitcnt lgkmcnt(8)\n"
"s_xor_b32 s43, s62, 0x19000\n"
"s_add_u32 m0, s55, 0x400\n"
"s_add_u32 s43, s43, s59\n"
"buffer_load_dwordx4 v121, s[28:31], s43 offen offset:0 lds\n"
"v_mfma_f32_16x16x32_bf16 v[48:51], a[32:35], v[64:67], v[48:51]\n"
"s_setprio 2\n"
"v_mfma_f32_16x16x32_bf16 v[48:51], a[40:43], v[68:71], v[48:51]\n"
"v_mfma_f32_16x16x32_bf16 v[48:51], a[48:51], v[72:75], v[48:51]\n"
"v_mfma_f32_16x16x32_bf16 v[48:51], a[56:59], v[76:79], v[48:51]\n"
"v_mfma_f32_16x16x32_bf16 v[52:55], a[36:39], v[64:67], v[52:55]\n"
"v_mfma_f32_16x16x32_bf16 v[52:55], a[44:47], v[68:71], v[52:55]\n"
"v_mfma_f32_16x16x32_bf16 v[52:55], a[52:55], v[72:75], v[52:55]\n"
"s_waitcnt lgkmcnt(0)\n"
"s_setprio 0\n"
"v_mfma_f32_16x16x32_bf16 v[52:55], a[60:63], v[76:79], v[52:55]\n"
"s_barrier\n"
"ds_read_b128 v[64:67], v124 offset:28672\n"
"ds_read_b128 v[68:71], v124 offset:29696\n"
"ds_read_b128 v[72:75], v124 offset:30720\n"
"ds_read_b128 v[76:79], v124 offset:31744\n"
"s_waitcnt vmcnt(7)\n"
"ds_read_b64_tr_b16 a[80:81], v125 offset:4096\n"
"ds_read_b64_tr_b16 a[84:85], v125 offset:4104\n"
"s_waitcnt vmcnt(6)\n"
"ds_read_b64_tr_b16 a[82:83], v125 offset:5120\n"
"ds_read_b64_tr_b16 a[86:87], v125 offset:5128\n"
"s_xor_b32 s43, s62, 0x1a000\n"
"s_add_u32 m0, s55, 0x800\n"
"s_add_u32 s43, s43, s59\n"
"buffer_load_dwordx4 v121, s[28:31], s43 offen offset:0 lds\n"
"s_xor_b32 s43, s62, 0x1b000\n"
"s_add_u32 m0, s55, 0xc00\n"
"s_add_u32 s43, s43, s59\n"
"buffer_load_dwordx4 v121, s[28:31], s43 offen offset:0 lds\n"
"v_mfma_f32_16x16x32_bf16 v[56:59], a[32:35], v[80:83], v[56:59]\n"
"s_setprio 2\n"
"v_mfma_f32_16x16x32_bf16 v[56:59], a[40:43], v[84:87], v[56:59]\n"
"v_mfma_f32_16x16x32_bf16 v[56:59], a[48:51], v[88:91], v[56:59]\n"
"v_mfma_f32_16x16x32_bf16 v[56:59], a[56:59], v[92:95], v[56:59]\n"
"v_mfma_f32_16x16x32_bf16 v[60:63], a[36:39], v[80:83], v[60:63]\n"
"v_mfma_f32_16x16x32_bf16 v[60:63], a[44:47], v[84:87], v[60:63]\n"
"v_mfma_f32_16x16x32_bf16 v[60:63], a[52:55], v[88:91], v[60:63]\n"
"v_mfma_f32_16x16x32_bf16 v[60:63], a[60:63], v[92:95], v[60:63]\n"
"s_setprio 0\n"
"ds_read_b128 v[80:83], v124 offset:32768\n"
"ds_read_b128 v[84:87], v124 offset:33792\n"
"ds_read_b128 v[88:91], v124 offset:34816\n"
"ds_read_b128 v[92:95], v124 offset:35840\n"
"s_waitcnt vmcnt(6)\n"
"ds_read_b64_tr_b16 a[88:89], v125 offset:6144\n"
"ds_read_b64_tr_b16 a[92:93], v125 offset:6152\n"
"s_waitcnt vmcnt(5)\n"
"ds_read_b64_tr_b16 a[90:91], v125 offset:7168\n"
"ds_read_b64_tr_b16 a[94:95], v125 offset:7176\n"
"s_waitcnt lgkmcnt(10)\n"
"s_xor_b32 s43, s62, 0x1c000\n"
"s_add_u32 m0, s55, 0x1000\n"
"s_add_u32 s43, s43, s59\n"
"buffer_load_dwordx4 v121, s[28:31], s43 offen offset:0 lds\n"
"s_waitcnt lgkmcnt(8)\n"
"s_xor_b32 s43, s62, 0x1d000\n"
"s_add_u32 m0, s55, 0x1400\n"
"s_add_u32 s43, s43, s59\n"
"buffer_load_dwordx4 v121, s[28:31], s43 offen offset:0 lds\n"
"v_mfma_f32_16x16x32_bf16 v[0:3], a[64:67], v[64:67], v[0:3]\n"
"s_setprio 2\n"
"v_mfma_f32_16x16x32_bf16 v[0:3], a[72:75], v[68:71], v[0:3]\n"
"v_mfma_f32_16x16x32_bf16 v[0:3], a[80:83], v[72:75], v[0:3]\n"
"s_waitcnt lgkmcnt(1)\n"
"v_mfma_f32_16x16x32_bf16 v[0:3], a[88:91], v[76:79], v[0:3]\n"
"v_mfma_f32_16x16x32_bf16 v[4:7], a[68:71], v[64:67], v[4:7]\n"
"v_mfma_f32_16x16x32_bf16 v[4:7], a[76:79], v[68:71], v[4:7]\n"
"v_mfma_f32_16x16x32_bf16 v[4:7], a[84:87], v[72:75], v[4:7]\n"
"s_waitcnt lgkmcnt(0)\n"
"v_mfma_f32_16x16x32_bf16 v[4:7], a[92:95], v[76:79], v[4:7]\n"
"s_setprio 0\n"
"ds_read_b128 v[64:67], v124 offset:0\n"
"ds_read_b128 v[68:71], v124 offset:1024\n"
"ds_read_b128 v[72:75], v124 offset:2048\n"
"ds_read_b128 v[76:79], v124 offset:3072\n"
"s_add_u32 s40, s51, 0xa000\n"
"buffer_load_dwordx4 v[104:107], v124, s[20:23], s40 offen nt\n"
"s_xor_b32 s43, s62, 0x1e000\n"
"s_add_u32 m0, s55, 0x1800\n"
"s_add_u32 s43, s43, s59\n"
"buffer_load_dwordx4 v121, s[28:31], s43 offen offset:0 lds\n"
"s_xor_b32 s43, s62, 0x1f000\n"
"s_add_u32 m0, s55, 0x1c00\n"
"s_add_u32 s43, s43, s59\n"
"buffer_load_dwordx4 v121, s[28:31], s43 offen offset:0 lds\n"
"v_mfma_f32_16x16x32_bf16 v[8:11], a[64:67], v[80:83], v[8:11]\n"
"s_setprio 2\n"
"v_mfma_f32_16x16x32_bf16 v[8:11], a[72:75], v[84:87], v[8:11]\n"
"v_mfma_f32_16x16x32_bf16 v[8:11], a[80:83], v[88:91], v[8:11]\n"
"v_mfma_f32_16x16x32_bf16 v[8:11], a[88:91], v[92:95], v[8:11]\n"
"v_mfma_f32_16x16x32_bf16 v[12:15], a[68:71], v[80:83], v[12:15]\n"
"v_mfma_f32_16x16x32_bf16 v[12:15], a[76:79], v[84:87], v[12:15]\n"
"v_mfma_f32_16x16x32_bf16 v[12:15], a[84:87], v[88:91], v[12:15]\n"
"v_mfma_f32_16x16x32_bf16 v[12:15], a[92:95], v[92:95], v[12:15]\n"
"s_setprio 0\n"
"ds_read_b128 v[80:83], v124 offset:4096\n"
"ds_read_b128 v[84:87], v124 offset:5120\n"
"ds_read_b128 v[88:91], v124 offset:6144\n"
"ds_read_b128 v[92:95], v124 offset:7168\n"
"s_add_u32 s40, s51, 0xb000\n"
"buffer_load_dwordx4 v[108:111], v124, s[20:23], s40 offen nt\n"
"s_waitcnt lgkmcnt(4)\n"
"v_mfma_f32_16x16x32_bf16 v[16:19], a[64:67], v[64:67], v[16:19]\n"
"s_setprio 2\n"
"v_mfma_f32_16x16x32_bf16 v[16:19], a[72:75], v[68:71], v[16:19]\n"
"v_lshlrev_b32 v112, 16, v96\n"
"v_and_b32 v113, s45, v96\n"
"v_mfma_f32_16x16x32_bf16 v[16:19], a[80:83], v[72:75], v[16:19]\n"
"v_mul_f32 v112, s44, v112\n"
"v_mul_f32 v113, s44, v113\n"
"v_mfma_f32_16x16x32_bf16 v[16:19], a[88:91], v[76:79], v[16:19]\n"
"v_cvt_pk_bf16_f32 v96, v112, v113\n"
"v_lshlrev_b32 v112, 16, v97\n"
"v_mfma_f32_16x16x32_bf16 v[20:23], a[68:71], v[64:67], v[20:23]\n"
"v_and_b32 v113, s45, v97\n"
"v_mul_f32 v112, s44, v112\n"
"v_mfma_f32_16x16x32_bf16 v[20:23], a[76:79], v[68:71], v[20:23]\n"
"v_mul_f32 v113, s44, v113\n"
"v_cvt_pk_bf16_f32 v97, v112, v113\n"
"v_mfma_f32_16x16x32_bf16 v[20:23], a[84:87], v[72:75], v[20:23]\n"
"v_lshlrev_b32 v112, 16, v98\n"
"v_and_b32 v113, s45, v98\n"
"s_waitcnt lgkmcnt(0)\n"
"s_setprio 0\n"
"v_mfma_f32_16x16x32_bf16 v[20:23], a[92:95], v[76:79], v[20:23]\n"
"s_barrier\n"
"ds_read_b128 v[64:67], v124 offset:8192\n"
"ds_read_b128 v[68:71], v124 offset:9216\n"
"ds_read_b128 v[72:75], v124 offset:10240\n"
"ds_read_b128 v[76:79], v124 offset:11264\n"
"v_mfma_f32_16x16x32_bf16 v[24:27], a[64:67], v[80:83], v[24:27]\n"
"s_setprio 2\n"
"v_mfma_f32_16x16x32_bf16 v[24:27], a[72:75], v[84:87], v[24:27]\n"
"v_mul_f32 v112, s44, v112\n"
"v_mul_f32 v113, s44, v113\n"
"v_mfma_f32_16x16x32_bf16 v[24:27], a[80:83], v[88:91], v[24:27]\n"
"v_cvt_pk_bf16_f32 v98, v112, v113\n"
"v_lshlrev_b32 v112, 16, v99\n"
"v_mfma_f32_16x16x32_bf16 v[24:27], a[88:91], v[92:95], v[24:27]\n"
"v_and_b32 v113, s45, v99\n"
"v_mul_f32 v112, s44, v112\n"
"v_mfma_f32_16x16x32_bf16 v[28:31], a[68:71], v[80:83], v[28:31]\n"
"v_mul_f32 v113, s44, v113\n"
"v_mfma_f32_16x16x32_bf16 v[28:31], a[76:79], v[84:87], v[28:31]\n"
"v_cvt_pk_bf16_f32 v99, v112, v113\n"
"v_mfma_f32_16x16x32_bf16 v[28:31], a[84:87], v[88:91], v[28:31]\n"
"v_mfma_f32_16x16x32_bf16 v[28:31], a[92:95], v[92:95], v[28:31]\n"
"s_setprio 0\n"
"ds_read_b128 v[80:83], v124 offset:12288\n"
"ds_read_b128 v[84:87], v124 offset:13312\n"
"ds_read_b128 v[88:91], v124 offset:14336\n"
"ds_read_b128 v[92:95], v124 offset:15360\n"
"ds_write_b128 v126, v[96:99] offset:32768\n"
"s_waitcnt lgkmcnt(5)\n"
"v_mfma_f32_16x16x32_bf16 v[32:35], a[64:67], v[64:67], v[32:35]\n"
"s_setprio 2\n"
"v_mfma_f32_16x16x32_bf16 v[32:35], a[72:75], v[68:71], v[32:35]\n"
"v_mfma_f32_16x16x32_bf16 v[32:35], a[80:83], v[72:75], v[32:35]\n"
"v_mfma_f32_16x16x32_bf16 v[32:35], a[88:91], v[76:79], v[32:35]\n"
"v_mfma_f32_16x16x32_bf16 v[36:39], a[68:71], v[64:67], v[36:39]\n"
"v_mfma_f32_16x16x32_bf16 v[36:39], a[76:79], v[68:71], v[36:39]\n"
"v_mfma_f32_16x16x32_bf16 v[36:39], a[84:87], v[72:75], v[36:39]\n"
"v_mfma_f32_16x16x32_bf16 v[36:39], a[92:95], v[76:79], v[36:39]\n"
"s_setprio 0\n"
"ds_read_b128 v[64:67], v124 offset:16384\n"
"ds_read_b128 v[68:71], v124 offset:17408\n"
"ds_read_b128 v[72:75], v124 offset:18432\n"
"ds_read_b128 v[76:79], v124 offset:19456\n"
"s_waitcnt vmcnt(9)\n"
"ds_read_b64_tr_b16 a[96:97], v125 offset:0\n"
"ds_read_b64_tr_b16 a[100:101], v125 offset:8\n"
"s_add_u32 s40, s51, 0xc000\n"
"buffer_load_dwordx4 v[96:99], v124, s[20:23], s40 offen nt\n"
"s_waitcnt lgkmcnt(7)\n"
"v_mfma_f32_16x16x32_bf16 v[40:43], a[64:67], v[80:83], v[40:43]\n"
"s_setprio 2\n"
"v_mfma_f32_16x16x32_bf16 v[40:43], a[72:75], v[84:87], v[40:43]\n"
"v_mfma_f32_16x16x32_bf16 v[40:43], a[80:83], v[88:91], v[40:43]\n"
"v_mfma_f32_16x16x32_bf16 v[40:43], a[88:91], v[92:95], v[40:43]\n"
"v_mfma_f32_16x16x32_bf16 v[44:47], a[68:71], v[80:83], v[44:47]\n"
"v_mfma_f32_16x16x32_bf16 v[44:47], a[76:79], v[84:87], v[44:47]\n"
"v_mfma_f32_16x16x32_bf16 v[44:47], a[84:87], v[88:91], v[44:47]\n"
"v_mfma_f32_16x16x32_bf16 v[44:47], a[92:95], v[92:95], v[44:47]\n"
"s_setprio 0\n"
"ds_read_b128 v[80:83], v124 offset:20480\n"
"ds_read_b128 v[84:87], v124 offset:21504\n"
"ds_read_b128 v[88:91], v124 offset:22528\n"
"ds_read_b128 v[92:95], v124 offset:23552\n"
"s_waitcnt vmcnt(9)\n"
"ds_read_b64_tr_b16 a[98:99], v125 offset:1024\n"
"ds_read_b64_tr_b16 a[102:103], v125 offset:1032\n"
"s_waitcnt vmcnt(8)\n"
"ds_read_b64_tr_b16 a[104:105], v125 offset:2048\n"
"ds_read_b64_tr_b16 a[108:109], v125 offset:2056\n"
"s_waitcnt lgkmcnt(10)\n"
"v_mfma_f32_16x16x32_bf16 v[48:51], a[64:67], v[64:67], v[48:51]\n"
"s_setprio 2\n"
"v_mfma_f32_16x16x32_bf16 v[48:51], a[72:75], v[68:71], v[48:51]\n"
"v_lshlrev_b32 v112, 16, v100\n"
"v_and_b32 v113, s45, v100\n"
"v_mul_f32 v112, s44, v112\n"
"v_mfma_f32_16x16x32_bf16 v[48:51], a[80:83], v[72:75], v[48:51]\n"
"v_mul_f32 v113, s44, v113\n"
"v_cvt_pk_bf16_f32 v100, v112, v113\n"
"v_mfma_f32_16x16x32_bf16 v[48:51], a[88:91], v[76:79], v[48:51]\n"
"v_lshlrev_b32 v112, 16, v101\n"
"v_and_b32 v113, s45, v101\n"
"v_mfma_f32_16x16x32_bf16 v[52:55], a[68:71], v[64:67], v[52:55]\n"
"v_mul_f32 v112, s44, v112\n"
"v_mul_f32 v113, s44, v113\n"
"v_mfma_f32_16x16x32_bf16 v[52:55], a[76:79], v[68:71], v[52:55]\n"
"v_cvt_pk_bf16_f32 v101, v112, v113\n"
"v_lshlrev_b32 v112, 16, v102\n"
"v_mfma_f32_16x16x32_bf16 v[52:55], a[84:87], v[72:75], v[52:55]\n"
"v_and_b32 v113, s45, v102\n"
"v_mul_f32 v112, s44, v112\n"
"s_waitcnt lgkmcnt(0)\n"
"s_setprio 0\n"
"v_mfma_f32_16x16x32_bf16 v[52:55], a[92:95], v[76:79], v[52:55]\n"
"s_barrier\n"
"ds_read_b128 v[64:67], v124 offset:24576\n"
"ds_read_b128 v[68:71], v124 offset:25600\n"
"ds_read_b128 v[72:75], v124 offset:26624\n"
"ds_read_b128 v[76:79], v124 offset:27648\n"
"s_waitcnt vmcnt(7)\n"
"ds_read_b64_tr_b16 a[106:107], v125 offset:3072\n"
"ds_read_b64_tr_b16 a[110:111], v125 offset:3080\n"
"s_waitcnt vmcnt(6)\n"
"ds_read_b64_tr_b16 a[112:113], v125 offset:4096\n"
"ds_read_b64_tr_b16 a[116:117], v125 offset:4104\n"
"v_mfma_f32_16x16x32_bf16 v[56:59], a[64:67], v[80:83], v[56:59]\n"
"s_setprio 2\n"
"v_mfma_f32_16x16x32_bf16 v[56:59], a[72:75], v[84:87], v[56:59]\n"
"v_mul_f32 v113, s44, v113\n"
"v_cvt_pk_bf16_f32 v102, v112, v113\n"
"v_mfma_f32_16x16x32_bf16 v[56:59], a[80:83], v[88:91], v[56:59]\n"
"v_lshlrev_b32 v112, 16, v103\n"
"v_and_b32 v113, s45, v103\n"
"v_mfma_f32_16x16x32_bf16 v[56:59], a[88:91], v[92:95], v[56:59]\n"
"v_mul_f32 v112, s44, v112\n"
"v_mfma_f32_16x16x32_bf16 v[60:63], a[68:71], v[80:83], v[60:63]\n"
"v_mul_f32 v113, s44, v113\n"
"v_mfma_f32_16x16x32_bf16 v[60:63], a[76:79], v[84:87], v[60:63]\n"
"v_cvt_pk_bf16_f32 v103, v112, v113\n"
"v_mfma_f32_16x16x32_bf16 v[60:63], a[84:87], v[88:91], v[60:63]\n"
"v_mfma_f32_16x16x32_bf16 v[60:63], a[92:95], v[92:95], v[60:63]\n"
"s_setprio 0\n"
"ds_read_b128 v[80:83], v124 offset:28672\n"
"ds_read_b128 v[84:87], v124 offset:29696\n"
"ds_read_b128 v[88:91], v124 offset:30720\n"
"ds_read_b128 v[92:95], v124 offset:31744\n"
"s_waitcnt vmcnt(5)\n"
"ds_read_b64_tr_b16 a[114:115], v125 offset:5120\n"
"ds_read_b64_tr_b16 a[118:119], v125 offset:5128\n"
"s_waitcnt vmcnt(3)\n"
"ds_read_b64_tr_b16 a[120:121], v125 offset:6144\n"
"ds_read_b64_tr_b16 a[124:125], v125 offset:6152\n"
"s_waitcnt vmcnt(2)\n"
"ds_read_b64_tr_b16 a[122:123], v125 offset:7168\n"
"ds_read_b64_tr_b16 a[126:127], v125 offset:7176\n"
"ds_write_b128 v126, v[100:103] offset:36864\n"
"s_waitcnt lgkmcnt(15)\n"
"v_mfma_f32_16x16x32_bf16 v[0:3], a[96:99], v[64:67], v[0:3]\n"
"s_setprio 2\n"
"s_waitcnt lgkmcnt(14)\n"
"v_mfma_f32_16x16x32_bf16 v[0:3], a[104:107], v[68:71], v[0:3]\n"
"s_waitcnt lgkmcnt(6)\n"
"v_mfma_f32_16x16x32_bf16 v[0:3], a[112:115], v[72:75], v[0:3]\n"
"s_waitcnt lgkmcnt(2)\n"
"v_mfma_f32_16x16x32_bf16 v[0:3], a[120:123], v[76:79], v[0:3]\n"
"v_mfma_f32_16x16x32_bf16 v[4:7], a[100:103], v[64:67], v[4:7]\n"
"v_mfma_f32_16x16x32_bf16 v[4:7], a[108:111], v[68:71], v[4:7]\n"
"v_mfma_f32_16x16x32_bf16 v[4:7], a[116:119], v[72:75], v[4:7]\n"
"s_waitcnt lgkmcnt(1)\n"
"v_mfma_f32_16x16x32_bf16 v[4:7], a[124:127], v[76:79], v[4:7]\n"
"s_setprio 0\n"
"ds_read_b128 v[64:67], v124 offset:32768\n"
"ds_read_b128 v[68:71], v124 offset:33792\n"
"ds_read_b128 v[72:75], v124 offset:34816\n"
"ds_read_b128 v[76:79], v124 offset:35840\n"
"s_add_u32 s40, s51, 0xd000\n"
"buffer_load_dwordx4 v[100:103], v124, s[20:23], s40 offen nt\n"
"v_mfma_f32_16x16x32_bf16 v[8:11], a[96:99], v[80:83], v[8:11]\n"
"s_setprio 2\n"
"v_mfma_f32_16x16x32_bf16 v[8:11], a[104:107], v[84:87], v[8:11]\n"
"v_mfma_f32_16x16x32_bf16 v[8:11], a[112:115], v[88:91], v[8:11]\n"
"v_mfma_f32_16x16x32_bf16 v[8:11], a[120:123], v[92:95], v[8:11]\n"
"v_mfma_f32_16x16x32_bf16 v[12:15], a[100:103], v[80:83], v[12:15]\n"
"v_mfma_f32_16x16x32_bf16 v[12:15], a[108:111], v[84:87], v[12:15]\n"
"v_mfma_f32_16x16x32_bf16 v[12:15], a[116:119], v[88:91], v[12:15]\n"
"v_mfma_f32_16x16x32_bf16 v[12:15], a[124:127], v[92:95], v[12:15]\n"
"s_setprio 0\n"
"ds_read_b128 v[80:83], v124 offset:0\n"
"ds_read_b128 v[84:87], v124 offset:1024\n"
"ds_read_b128 v[88:91], v124 offset:2048\n"
"ds_read_b128 v[92:95], v124 offset:3072\n"
"s_waitcnt lgkmcnt(4)\n"
"v_mfma_f32_16x16x32_bf16 v[16:19], a[96:99], v[64:67], v[16:19]\n"
"s_setprio 2\n"
"v_mfma_f32_16x16x32_bf16 v[16:19], a[104:107], v[68:71], v[16:19]\n"
"v_lshlrev_b32 v112, 16, v104\n"
"v_and_b32 v113, s45, v104\n"
"v_mul_f32 v112, s44, v112\n"
"v_mfma_f32_16x16x32_bf16 v[16:19], a[112:115], v[72:75], v[16:19]\n"
"v_mul_f32 v113, s44, v113\n"
"v_cvt_pk_bf16_f32 v104, v112, v113\n"
"v_lshlrev_b32 v112, 16, v105\n"
"v_mfma_f32_16x16x32_bf16 v[16:19], a[120:123], v[76:79], v[16:19]\n"
"v_and_b32 v113, s45, v105\n"
"v_mul_f32 v112, s44, v112\n"
"v_mfma_f32_16x16x32_bf16 v[20:23], a[100:103], v[64:67], v[20:23]\n"
"v_mul_f32 v113, s44, v113\n"
"v_cvt_pk_bf16_f32 v105, v112, v113\n"
"v_mfma_f32_16x16x32_bf16 v[20:23], a[108:111], v[68:71], v[20:23]\n"
"v_lshlrev_b32 v112, 16, v106\n"
"v_and_b32 v113, s45, v106\n"
"v_mfma_f32_16x16x32_bf16 v[20:23], a[116:119], v[72:75], v[20:23]\n"
"v_mul_f32 v112, s44, v112\n"
"v_mul_f32 v113, s44, v113\n"
"s_waitcnt lgkmcnt(0)\n"
"s_setprio 0\n"
"v_mfma_f32_16x16x32_bf16 v[20:23], a[124:127], v[76:79], v[20:23]\n"
"s_barrier\n"
"ds_read_b128 v[64:67], v124 offset:4096\n"
"ds_read_b128 v[68:71], v124 offset:5120\n"
"ds_read_b128 v[72:75], v124 offset:6144\n"
"ds_read_b128 v[76:79], v124 offset:7168\n"
"s_xor_b32 s43, s62, 0\n"
"s_add_u32 m0, s55, 0\n"
"s_add_u32 s43, s43, s39\n"
"buffer_load_dwordx4 v121, s[28:31], s43 offen offset:0 lds\n"
"v_mfma_f32_16x16x32_bf16 v[24:27], a[96:99], v[80:83], v[24:27]\n"
"s_setprio 2\n"
"v_mfma_f32_16x16x32_bf16 v[24:27], a[104:107], v[84:87], v[24:27]\n"
"v_cvt_pk_bf16_f32 v106, v112, v113\n"
"v_lshlrev_b32 v112, 16, v107\n"
"v_and_b32 v113, s45, v107\n"
"v_mfma_f32_16x16x32_bf16 v[24:27], a[112:115], v[88:91], v[24:27]\n"
"v_mul_f32 v112, s44, v112\n"
"v_mul_f32 v113, s44, v113\n"
"v_cvt_pk_bf16_f32 v107, v112, v113\n"
"v_mfma_f32_16x16x32_bf16 v[24:27], a[120:123], v[92:95], v[24:27]\n"
"s_waitcnt vmcnt(3)\n"
"v_lshlrev_b32 v112, 16, v108\n"
"v_and_b32 v113, s45, v108\n"
"v_mfma_f32_16x16x32_bf16 v[28:31], a[100:103], v[80:83], v[28:31]\n"
"v_mul_f32 v112, s44, v112\n"
"v_mul_f32 v113, s44, v113\n"
"v_mfma_f32_16x16x32_bf16 v[28:31], a[108:111], v[84:87], v[28:31]\n"
"v_cvt_pk_bf16_f32 v108, v112, v113\n"
"v_lshlrev_b32 v112, 16, v109\n"
"v_mfma_f32_16x16x32_bf16 v[28:31], a[116:119], v[88:91], v[28:31]\n"
"v_and_b32 v113, s45, v109\n"
"v_mul_f32 v112, s44, v112\n"
"v_mfma_f32_16x16x32_bf16 v[28:31], a[124:127], v[92:95], v[28:31]\n"
"s_setprio 0\n"
"ds_read_b128 v[80:83], v124 offset:8192\n"
"ds_read_b128 v[84:87], v124 offset:9216\n"
"ds_read_b128 v[88:91], v124 offset:10240\n"
"ds_read_b128 v[92:95], v124 offset:11264\n"
"ds_write_b128 v126, v[104:107] offset:40960\n"
"s_waitcnt lgkmcnt(5)\n"
"v_mfma_f32_16x16x32_bf16 v[32:35], a[96:99], v[64:67], v[32:35]\n"
"s_setprio 2\n"
"v_mfma_f32_16x16x32_bf16 v[32:35], a[104:107], v[68:71], v[32:35]\n"
"v_mul_f32 v113, s44, v113\n"
"v_cvt_pk_bf16_f32 v109, v112, v113\n"
"v_mfma_f32_16x16x32_bf16 v[32:35], a[112:115], v[72:75], v[32:35]\n"
"v_lshlrev_b32 v112, 16, v110\n"
"v_and_b32 v113, s45, v110\n"
"v_mfma_f32_16x16x32_bf16 v[32:35], a[120:123], v[76:79], v[32:35]\n"
"v_mul_f32 v112, s44, v112\n"
"v_mul_f32 v113, s44, v113\n"
"v_mfma_f32_16x16x32_bf16 v[36:39], a[100:103], v[64:67], v[36:39]\n"
"v_cvt_pk_bf16_f32 v110, v112, v113\n"
"v_lshlrev_b32 v112, 16, v111\n"
"v_mfma_f32_16x16x32_bf16 v[36:39], a[108:111], v[68:71], v[36:39]\n"
"v_and_b32 v113, s45, v111\n"
"v_mul_f32 v112, s44, v112\n"
"v_mfma_f32_16x16x32_bf16 v[36:39], a[116:119], v[72:75], v[36:39]\n"
"v_mul_f32 v113, s44, v113\n"
"v_cvt_pk_bf16_f32 v111, v112, v113\n"
"v_mfma_f32_16x16x32_bf16 v[36:39], a[124:127], v[76:79], v[36:39]\n"
"s_setprio 0\n"
"ds_read_b128 v[64:67], v124 offset:12288\n"
"ds_read_b128 v[68:71], v124 offset:13312\n"
"ds_read_b128 v[72:75], v124 offset:14336\n"
"ds_read_b128 v[76:79], v124 offset:15360\n"
"ds_read_b128 v[114:117], v124 offset:39936\n"
"ds_write_b128 v126, v[108:111] offset:45056\n"
"s_add_u32 s40, s51, 0xe000\n"
"buffer_load_dwordx4 v[104:107], v124, s[20:23], s40 offen nt\n"
"s_xor_b32 s43, s62, 0x1000\n"
"s_add_u32 m0, s55, 0x400\n"
"s_add_u32 s43, s43, s39\n"
"buffer_load_dwordx4 v121, s[28:31], s43 offen offset:0 lds\n"
"s_waitcnt lgkmcnt(7)\n"
"v_mfma_f32_16x16x32_bf16 v[40:43], a[96:99], v[80:83], v[40:43]\n"
"s_setprio 2\n"
"v_mfma_f32_16x16x32_bf16 v[40:43], a[104:107], v[84:87], v[40:43]\n"
"s_waitcnt lgkmcnt(1)\n"
"v_add_f32 v114, v114, v115\n"
"v_add_f32 v116, v116, v117\n"
"v_add_f32 v114, v114, v116\n"
"v_mfma_f32_16x16x32_bf16 v[40:43], a[112:115], v[88:91], v[40:43]\n"
"v_rcp_f32 v120, v114\n"
"v_subrev_u32 v118, s46, v114\n"
"v_max_u32 v123, v123, v118\n"
"v_mfma_f32_16x16x32_bf16 v[40:43], a[120:123], v[92:95], v[40:43]\n"
"v_mul_f32 v24, v120, v24\n"
"v_mul_f32 v25, v120, v25\n"
"v_mfma_f32_16x16x32_bf16 v[44:47], a[100:103], v[80:83], v[44:47]\n"
"v_mul_f32 v26, v120, v26\n"
"v_mul_f32 v27, v120, v27\n"
"v_mfma_f32_16x16x32_bf16 v[44:47], a[108:111], v[84:87], v[44:47]\n"
"v_mul_f32 v28, v120, v28\n"
"v_mul_f32 v29, v120, v29\n"
"v_mfma_f32_16x16x32_bf16 v[44:47], a[116:119], v[88:91], v[44:47]\n"
"v_mul_f32 v30, v120, v30\n"
"v_mul_f32 v31, v120, v31\n"
"v_mfma_f32_16x16x32_bf16 v[44:47], a[124:127], v[92:95], v[44:47]\n"
"s_setprio 0\n"
"ds_read_b128 v[80:83], v124 offset:16384\n"
"ds_read_b128 v[84:87], v124 offset:17408\n"
"ds_read_b128 v[88:91], v124 offset:18432\n"
"ds_read_b128 v[92:95], v124 offset:19456\n"
"v_mfma_f32_16x16x32_bf16 v[48:51], a[96:99], v[64:67], v[48:51]\n"
"s_setprio 2\n"
"v_mfma_f32_16x16x32_bf16 v[48:51], a[104:107], v[68:71], v[48:51]\n"
"v_fmac_f32 v122, v24, v25\n"
"v_fmac_f32 v122, v26, v27\n"
"v_fmac_f32 v122, v28, v29\n"
"v_mfma_f32_16x16x32_bf16 v[48:51], a[112:115], v[72:75], v[48:51]\n"
"v_fmac_f32 v122, v30, v31\n"
"v_cvt_pk_bf16_f32 v24, v24, v25\n"
"v_cvt_pk_bf16_f32 v25, v26, v27\n"
"v_mfma_f32_16x16x32_bf16 v[48:51], a[120:123], v[76:79], v[48:51]\n"
"v_cvt_pk_bf16_f32 v26, v28, v29\n"
"v_cvt_pk_bf16_f32 v27, v30, v31\n"
"v_mfma_f32_16x16x32_bf16 v[52:55], a[100:103], v[64:67], v[52:55]\n"
"s_waitcnt vmcnt(4)\n"
"v_lshlrev_b32 v112, 16, v96\n"
"v_and_b32 v113, s45, v96\n"
"v_mfma_f32_16x16x32_bf16 v[52:55], a[108:111], v[68:71], v[52:55]\n"
"v_mul_f32 v112, s44, v112\n"
"v_mul_f32 v113, s44, v113\n"
"v_mfma_f32_16x16x32_bf16 v[52:55], a[116:119], v[72:75], v[52:55]\n"
"v_cvt_pk_bf16_f32 v96, v112, v113\n"
"v_lshlrev_b32 v112, 16, v97\n"
"s_waitcnt lgkmcnt(0)\n"
"s_setprio 0\n"
"v_mfma_f32_16x16x32_bf16 v[52:55], a[124:127], v[76:79], v[52:55]\n"
"s_barrier\n"
"ds_read_b128 v[64:67], v124 offset:20480\n"
"ds_read_b128 v[68:71], v124 offset:21504\n"
"ds_read_b128 v[72:75], v124 offset:22528\n"
"ds_read_b128 v[76:79], v124 offset:23552\n"
"s_add_u32 s41, s52, 0x3000\n"
"buffer_store_dwordx4 v[24:27], v127, s[64:67], s41 offen sc1\n"
"s_add_u32 s40, s51, 0xf000\n"
"buffer_load_dwordx4 v[108:111], v124, s[20:23], s40 offen nt\n"
"s_xor_b32 s43, s62, 0x2000\n"
"s_add_u32 m0, s55, 0x800\n"
"s_add_u32 s43, s43, s39\n"
"buffer_load_dwordx4 v121, s[28:31], s43 offen offset:0 lds\n"
"v_mfma_f32_16x16x32_bf16 v[56:59], a[96:99], v[80:83], v[56:59]\n"
"s_setprio 2\n"
"v_mfma_f32_16x16x32_bf16 v[56:59], a[104:107], v[84:87], v[56:59]\n"
"v_and_b32 v113, s45, v97\n"
"v_mul_f32 v112, s44, v112\n"
"v_mul_f32 v113, s44, v113\n"
"v_mfma_f32_16x16x32_bf16 v[56:59], a[112:115], v[88:91], v[56:59]\n"
"v_cvt_pk_bf16_f32 v97, v112, v113\n"
"v_lshlrev_b32 v112, 16, v98\n"
"v_and_b32 v113, s45, v98\n"
"v_mfma_f32_16x16x32_bf16 v[56:59], a[120:123], v[92:95], v[56:59]\n"
"v_mul_f32 v112, s44, v112\n"
"v_mul_f32 v113, s44, v113\n"
"v_mfma_f32_16x16x32_bf16 v[60:63], a[100:103], v[80:83], v[60:63]\n"
"v_cvt_pk_bf16_f32 v98, v112, v113\n"
"v_lshlrev_b32 v112, 16, v99\n"
"v_mfma_f32_16x16x32_bf16 v[60:63], a[108:111], v[84:87], v[60:63]\n"
"v_and_b32 v113, s45, v99\n"
"v_mul_f32 v112, s44, v112\n"
"v_mfma_f32_16x16x32_bf16 v[60:63], a[116:119], v[88:91], v[60:63]\n"
"v_mul_f32 v113, s44, v113\n"
"v_cvt_pk_bf16_f32 v99, v112, v113\n"
"v_mfma_f32_16x16x32_bf16 v[60:63], a[124:127], v[92:95], v[60:63]\n"
"s_setprio 0\n"
"ds_read_b128 v[80:83], v124 offset:24576\n"
"ds_read_b128 v[84:87], v124 offset:25600\n"
"ds_read_b128 v[88:91], v124 offset:26624\n"
"ds_read_b128 v[92:95], v124 offset:27648\n"
"ds_write_b128 v126, v[96:99] offset:32768\n"
"s_waitcnt lgkmcnt(5)\n"
"v_mfma_f32_16x16x32_bf16 v[24:27], a[0:3], v[64:67], 0\n"
"s_setprio 2\n"
"v_mfma_f32_16x16x32_bf16 v[24:27], a[8:11], v[68:71], v[24:27]\n"
"v_mfma_f32_16x16x32_bf16 v[24:27], a[16:19], v[72:75], v[24:27]\n"
"v_mfma_f32_16x16x32_bf16 v[24:27], a[24:27], v[76:79], v[24:27]\n"
"v_mfma_f32_16x16x32_bf16 v[28:31], a[4:7], v[64:67], 0\n"
"v_mfma_f32_16x16x32_bf16 v[28:31], a[12:15], v[68:71], v[28:31]\n"
"v_mfma_f32_16x16x32_bf16 v[28:31], a[20:23], v[72:75], v[28:31]\n"
"v_mfma_f32_16x16x32_bf16 v[28:31], a[28:31], v[76:79], v[28:31]\n"
"s_setprio 0\n"
"ds_read_b128 v[64:67], v124 offset:28672\n"
"ds_read_b128 v[68:71], v124 offset:29696\n"
"ds_read_b128 v[72:75], v124 offset:30720\n"
"ds_read_b128 v[76:79], v124 offset:31744\n"
"ds_read_b128 v[114:117], v124 offset:38912\n"
"s_xor_b32 s43, s62, 0x3000\n"
"s_add_u32 m0, s55, 0xc00\n"
"s_add_u32 s43, s43, s39\n"
"buffer_load_dwordx4 v121, s[28:31], s43 offen offset:0 lds\n"
"s_waitcnt lgkmcnt(6)\n"
"v_mfma_f32_16x16x32_bf16 v[24:27], a[32:35], v[80:83], v[24:27]\n"
"s_setprio 2\n"
"v_mfma_f32_16x16x32_bf16 v[24:27], a[40:43], v[84:87], v[24:27]\n"
"s_waitcnt lgkmcnt(0)\n"
"v_add_f32 v114, v114, v115\n"
"v_add_f32 v116, v116, v117\n"
"v_add_f32 v114, v114, v116\n"
"v_mfma_f32_16x16x32_bf16 v[24:27], a[48:51], v[88:91], v[24:27]\n"
"v_rcp_f32 v120, v114\n"
"v_subrev_u32 v118, s46, v114\n"
"v_max_u32 v123, v123, v118\n"
"v_mfma_f32_16x16x32_bf16 v[24:27], a[56:59], v[92:95], v[24:27]\n"
"v_mul_f32 v16, v120, v16\n"
"v_mul_f32 v17, v120, v17\n"
"v_mfma_f32_16x16x32_bf16 v[28:31], a[36:39], v[80:83], v[28:31]\n"
"v_mul_f32 v18, v120, v18\n"
"v_mul_f32 v19, v120, v19\n"
"v_mfma_f32_16x16x32_bf16 v[28:31], a[44:47], v[84:87], v[28:31]\n"
"v_mul_f32 v20, v120, v20\n"
"v_mul_f32 v21, v120, v21\n"
"v_mfma_f32_16x16x32_bf16 v[28:31], a[52:55], v[88:91], v[28:31]\n"
"v_mul_f32 v22, v120, v22\n"
"v_mul_f32 v23, v120, v23\n"
"v_mfma_f32_16x16x32_bf16 v[28:31], a[60:63], v[92:95], v[28:31]\n"
"s_setprio 0\n"
"ds_read_b128 v[80:83], v124 offset:32768\n"
"ds_read_b128 v[84:87], v124 offset:33792\n"
"ds_read_b128 v[88:91], v124 offset:34816\n"
"ds_read_b128 v[92:95], v124 offset:35840\n"
"v_mfma_f32_16x16x32_bf16 v[24:27], a[64:67], v[64:67], v[24:27]\n"
"s_setprio 2\n"
"v_mfma_f32_16x16x32_bf16 v[24:27], a[72:75], v[68:71], v[24:27]\n"
"v_fmac_f32 v122, v16, v17\n"
"v_fmac_f32 v122, v18, v19\n"
"v_fmac_f32 v122, v20, v21\n"
"v_mfma_f32_16x16x32_bf16 v[24:27], a[80:83], v[72:75], v[24:27]\n"
"v_fmac_f32 v122, v22, v23\n"
"v_cvt_pk_bf16_f32 v16, v16, v17\n"
"v_cvt_pk_bf16_f32 v17, v18, v19\n"
"v_mfma_f32_16x16x32_bf16 v[24:27], a[88:91], v[76:79], v[24:27]\n"
"v_cvt_pk_bf16_f32 v18, v20, v21\n"
"v_cvt_pk_bf16_f32 v19, v22, v23\n"
"v_mfma_f32_16x16x32_bf16 v[28:31], a[68:71], v[64:67], v[28:31]\n"
"s_waitcnt vmcnt(7)\n"
"v_lshlrev_b32 v112, 16, v100\n"
"v_and_b32 v113, s45, v100\n"
"v_mfma_f32_16x16x32_bf16 v[28:31], a[76:79], v[68:71], v[28:31]\n"
"v_mul_f32 v112, s44, v112\n"
"v_mul_f32 v113, s44, v113\n"
"v_mfma_f32_16x16x32_bf16 v[28:31], a[84:87], v[72:75], v[28:31]\n"
"v_cvt_pk_bf16_f32 v100, v112, v113\n"
"v_lshlrev_b32 v112, 16, v101\n"
"s_waitcnt lgkmcnt(0)\n"
"s_setprio 0\n"
"v_mfma_f32_16x16x32_bf16 v[28:31], a[92:95], v[76:79], v[28:31]\n"
"s_barrier\n"
"ds_read_b128 v[64:67], v124 offset:0\n"
"ds_read_b128 v[68:71], v124 offset:1024\n"
"ds_read_b128 v[72:75], v124 offset:2048\n"
"ds_read_b128 v[76:79], v124 offset:3072\n"
"s_add_u32 s41, s52, 0x2000\n"
"buffer_store_dwordx4 v[16:19], v127, s[64:67], s41 offen sc1\n"
"s_add_u32 s40, s51, 0x10000\n"
"buffer_load_dwordx4 v[96:99], v124, s[20:23], s40 offen nt\n"
"s_xor_b32 s43, s62, 0x4000\n"
"s_add_u32 m0, s55, 0x1000\n"
"s_add_u32 s43, s43, s39\n"
"buffer_load_dwordx4 v121, s[28:31], s43 offen offset:0 lds\n"
"v_mfma_f32_16x16x32_bf16 v[24:27], a[96:99], v[80:83], v[24:27]\n"
"s_setprio 2\n"
"v_mfma_f32_16x16x32_bf16 v[24:27], a[104:107], v[84:87], v[24:27]\n"
"v_and_b32 v113, s45, v101\n"
"v_mul_f32 v112, s44, v112\n"
"v_mul_f32 v113, s44, v113\n"
"v_mfma_f32_16x16x32_bf16 v[24:27], a[112:115], v[88:91], v[24:27]\n"
"v_cvt_pk_bf16_f32 v101, v112, v113\n"
"v_lshlrev_b32 v112, 16, v102\n"
"v_and_b32 v113, s45, v102\n"
"v_mfma_f32_16x16x32_bf16 v[24:27], a[120:123], v[92:95], v[24:27]\n"
"v_mul_f32 v112, s44, v112\n"
"v_mul_f32 v113, s44, v113\n"
"v_mfma_f32_16x16x32_bf16 v[28:31], a[100:103], v[80:83], v[28:31]\n"
"v_cvt_pk_bf16_f32 v102, v112, v113\n"
"v_lshlrev_b32 v112, 16, v103\n"
"v_mfma_f32_16x16x32_bf16 v[28:31], a[108:111], v[84:87], v[28:31]\n"
"v_and_b32 v113, s45, v103\n"
"v_mul_f32 v112, s44, v112\n"
"v_mfma_f32_16x16x32_bf16 v[28:31], a[116:119], v[88:91], v[28:31]\n"
"v_mul_f32 v113, s44, v113\n"
"v_cvt_pk_bf16_f32 v103, v112, v113\n"
"v_mfma_f32_16x16x32_bf16 v[28:31], a[124:127], v[92:95], v[28:31]\n"
"s_setprio 0\n"
"ds_read_b128 v[80:83], v124 offset:4096\n"
"ds_read_b128 v[84:87], v124 offset:5120\n"
"ds_read_b128 v[88:91], v124 offset:6144\n"
"ds_read_b128 v[92:95], v124 offset:7168\n"
"ds_read_b128 v[114:117], v124 offset:47104\n"
"ds_write_b128 v126, v[100:103] offset:36864\n"
"s_waitcnt lgkmcnt(6)\n"
"v_mfma_f32_16x16x32_bf16 v[16:19], a[0:3], v[64:67], 0\n"
"s_setprio 2\n"
"v_mfma_f32_16x16x32_bf16 v[16:19], a[8:11], v[68:71], v[16:19]\n"
"s_waitcnt lgkmcnt(1)\n"
"v_add_f32 v114, v114, v115\n"
"v_add_f32 v116, v116, v117\n"
"v_add_f32 v114, v114, v116\n"
"v_mfma_f32_16x16x32_bf16 v[16:19], a[16:19], v[72:75], v[16:19]\n"
"v_rcp_f32 v119, v114\n"
"v_subrev_u32 v118, s46, v114\n"
"v_max_u32 v123, v123, v118\n"
"v_mfma_f32_16x16x32_bf16 v[16:19], a[24:27], v[76:79], v[16:19]\n"
"v_mul_f32 v24, v119, v24\n"
"v_mul_f32 v25, v119, v25\n"
"v_mfma_f32_16x16x32_bf16 v[20:23], a[4:7], v[64:67], 0\n"
"v_mul_f32 v26, v119, v26\n"
"v_mul_f32 v27, v119, v27\n"
"v_mfma_f32_16x16x32_bf16 v[20:23], a[12:15], v[68:71], v[20:23]\n"
"v_mul_f32 v28, v119, v28\n"
"v_mul_f32 v29, v119, v29\n"
"v_mfma_f32_16x16x32_bf16 v[20:23], a[20:23], v[72:75], v[20:23]\n"
"v_mul_f32 v30, v119, v30\n"
"v_mul_f32 v31, v119, v31\n"
"v_mfma_f32_16x16x32_bf16 v[20:23], a[28:31], v[76:79], v[20:23]\n"
"s_setprio 0\n"
"ds_read_b128 v[64:67], v124 offset:8192\n"
"ds_read_b128 v[68:71], v124 offset:9216\n"
"ds_read_b128 v[72:75], v124 offset:10240\n"
"ds_read_b128 v[76:79], v124 offset:11264\n"
"ds_read_b128 v[114:117], v124 offset:40960\n"
"s_xor_b32 s43, s62, 0x5000\n"
"s_add_u32 m0, s55, 0x1400\n"
"s_add_u32 s43, s43, s39\n"
"buffer_load_dwordx4 v121, s[28:31], s43 offen offset:0 lds\n"
"v_mfma_f32_16x16x32_bf16 v[16:19], a[32:35], v[80:83], v[16:19]\n"
"s_setprio 2\n"
"v_mfma_f32_16x16x32_bf16 v[16:19], a[40:43], v[84:87], v[16:19]\n"
"s_waitcnt lgkmcnt(0)\n"
"v_add_f32 v114, v114, v115\n"
"v_add_f32 v116, v116, v117\n"
"v_add_f32 v114, v114, v116\n"
"v_mfma_f32_16x16x32_bf16 v[16:19], a[48:51], v[88:91], v[16:19]\n"
"v_rcp_f32 v120, v114\n"
"v_subrev_u32 v118, s46, v114\n"
"v_max_u32 v123, v123, v118\n"
"v_mfma_f32_16x16x32_bf16 v[16:19], a[56:59], v[92:95], v[16:19]\n"
"v_fmac_f32 v122, v24, v25\n"
"v_fmac_f32 v122, v26, v27\n"
"v_mfma_f32_16x16x32_bf16 v[20:23], a[36:39], v[80:83], v[20:23]\n"
"v_fmac_f32 v122, v28, v29\n"
"v_fmac_f32 v122, v30, v31\n"
"v_mfma_f32_16x16x32_bf16 v[20:23], a[44:47], v[84:87], v[20:23]\n"
"v_cvt_pk_bf16_f32 v24, v24, v25\n"
"v_cvt_pk_bf16_f32 v25, v26, v27\n"
"v_mfma_f32_16x16x32_bf16 v[20:23], a[52:55], v[88:91], v[20:23]\n"
"v_cvt_pk_bf16_f32 v26, v28, v29\n"
"v_cvt_pk_bf16_f32 v27, v30, v31\n"
"v_mfma_f32_16x16x32_bf16 v[20:23], a[60:63], v[92:95], v[20:23]\n"
"s_setprio 0\n"
"ds_read_b128 v[80:83], v124 offset:12288\n"
"ds_read_b128 v[84:87], v124 offset:13312\n"
"ds_read_b128 v[88:91], v124 offset:14336\n"
"ds_read_b128 v[92:95], v124 offset:15360\n"
"s_add_u32 s41, s52, 0x8000\n"
"buffer_store_dwordx4 v[24:27], v127, s[64:67], s41 offen sc1\n"
"v_mfma_f32_16x16x32_bf16 v[16:19], a[64:67], v[64:67], v[16:19]\n"
"s_setprio 2\n"
"v_mfma_f32_16x16x32_bf16 v[16:19], a[72:75], v[68:71], v[16:19]\n"
"s_waitcnt vmcnt(10)\n"
"v_lshlrev_b32 v112, 16, v104\n"
"v_and_b32 v113, s45, v104\n"
"v_mul_f32 v112, s44, v112\n"
"v_mfma_f32_16x16x32_bf16 v[16:19], a[80:83], v[72:75], v[16:19]\n"
"v_mul_f32 v113, s44, v113\n"
"v_cvt_pk_bf16_f32 v104, v112, v113\n"
"v_lshlrev_b32 v112, 16, v105\n"
"v_mfma_f32_16x16x32_bf16 v[16:19], a[88:91], v[76:79], v[16:19]\n"
"v_and_b32 v113, s45, v105\n"
"v_mul_f32 v112, s44, v112\n"
"v_mfma_f32_16x16x32_bf16 v[20:23], a[68:71], v[64:67], v[20:23]\n"
"v_mul_f32 v113, s44, v113\n"
"v_cvt_pk_bf16_f32 v105, v112, v113\n"
"v_mfma_f32_16x16x32_bf16 v[20:23], a[76:79], v[68:71], v[20:23]\n"
"v_lshlrev_b32 v112, 16, v106\n"
"v_and_b32 v113, s45, v106\n"
"v_mfma_f32_16x16x32_bf16 v[20:23], a[84:87], v[72:75], v[20:23]\n"
"v_mul_f32 v112, s44, v112\n"
"v_mul_f32 v113, s44, v113\n"
"s_waitcnt lgkmcnt(0)\n"
"s_setprio 0\n"
"v_mfma_f32_16x16x32_bf16 v[20:23], a[92:95], v[76:79], v[20:23]\n"
"s_barrier\n"
"ds_read_b128 v[64:67], v124 offset:16384\n"
"ds_read_b128 v[68:71], v124 offset:17408\n"
"ds_read_b128 v[72:75], v124 offset:18432\n"
"ds_read_b128 v[76:79], v124 offset:19456\n"
"s_add_u32 s40, s51, 0x11000\n"
"buffer_load_dwordx4 v[100:103], v124, s[20:23], s40 offen nt\n"
"s_xor_b32 s43, s62, 0x6000\n"
"s_add_u32 m0, s55, 0x1800\n"
"s_add_u32 s43, s43, s39\n"
"buffer_load_dwordx4 v121, s[28:31], s43 offen offset:0 lds\n"
"v_mfma_f32_16x16x32_bf16 v[16:19], a[96:99], v[80:83], v[16:19]\n"
"s_setprio 2\n"
"v_mfma_f32_16x16x32_bf16 v[16:19], a[104:107], v[84:87], v[16:19]\n"
"v_cvt_pk_bf16_f32 v106, v112, v113\n"
"v_lshlrev_b32 v112, 16, v107\n"
"v_and_b32 v113, s45, v107\n"
"v_mfma_f32_16x16x32_bf16 v[16:19], a[112:115], v[88:91], v[16:19]\n"
"v_mul_f32 v112, s44, v112\n"
"v_mul_f32 v113, s44, v113\n"
"v_cvt_pk_bf16_f32 v107, v112, v113\n"
"v_mfma_f32_16x16x32_bf16 v[16:19], a[120:123], v[92:95], v[16:19]\n"
"v_mul_f32 v32, v120, v32\n"
"v_mul_f32 v33, v120, v33\n"
"v_mfma_f32_16x16x32_bf16 v[20:23], a[100:103], v[80:83], v[20:23]\n"
"v_mul_f32 v34, v120, v34\n"
"v_mul_f32 v35, v120, v35\n"
"v_mfma_f32_16x16x32_bf16 v[20:23], a[108:111], v[84:87], v[20:23]\n"
"v_mul_f32 v36, v120, v36\n"
"v_mul_f32 v37, v120, v37\n"
"v_mfma_f32_16x16x32_bf16 v[20:23], a[116:119], v[88:91], v[20:23]\n"
"v_mul_f32 v38, v120, v38\n"
"v_mul_f32 v39, v120, v39\n"
"v_mfma_f32_16x16x32_bf16 v[20:23], a[124:127], v[92:95], v[20:23]\n"
"s_setprio 0\n"
"ds_read_b128 v[80:83], v124 offset:20480\n"
"ds_read_b128 v[84:87], v124 offset:21504\n"
"ds_read_b128 v[88:91], v124 offset:22528\n"
"ds_read_b128 v[92:95], v124 offset:23552\n"
"ds_read_b128 v[114:117], v124 offset:45056\n"
"ds_write_b128 v126, v[104:107] offset:40960\n"
"s_waitcnt lgkmcnt(6)\n"
"v_mfma_f32_16x16x32_bf16 v[24:27], a[0:3], v[64:67], 0\n"
"s_setprio 2\n"
"v_mfma_f32_16x16x32_bf16 v[24:27], a[8:11], v[68:71], v[24:27]\n"
"s_waitcnt lgkmcnt(1)\n"
"v_add_f32 v114, v114, v115\n"
"v_add_f32 v116, v116, v117\n"
"v_add_f32 v114, v114, v116\n"
"v_mfma_f32_16x16x32_bf16 v[24:27], a[16:19], v[72:75], v[24:27]\n"
"v_rcp_f32 v119, v114\n"
"v_subrev_u32 v118, s46, v114\n"
"v_max_u32 v123, v123, v118\n"
"v_mfma_f32_16x16x32_bf16 v[24:27], a[24:27], v[76:79], v[24:27]\n"
"v_mul_f32 v16, v119, v16\n"
"v_mul_f32 v17, v119, v17\n"
"v_mfma_f32_16x16x32_bf16 v[28:31], a[4:7], v[64:67], 0\n"
"v_mul_f32 v18, v119, v18\n"
"v_mul_f32 v19, v119, v19\n"
"v_mfma_f32_16x16x32_bf16 v[28:31], a[12:15], v[68:71], v[28:31]\n"
"v_mul_f32 v20, v119, v20\n"
"v_mul_f32 v21, v119, v21\n"
"v_mfma_f32_16x16x32_bf16 v[28:31], a[20:23], v[72:75], v[28:31]\n"
"v_mul_f32 v22, v119, v22\n"
"v_mul_f32 v23, v119, v23\n"
"v_mfma_f32_16x16x32_bf16 v[28:31], a[28:31], v[76:79], v[28:31]\n"
"s_setprio 0\n"
"ds_read_b128 v[64:67], v124 offset:24576\n"
"ds_read_b128 v[68:71], v124 offset:25600\n"
"ds_read_b128 v[72:75], v124 offset:26624\n"
"ds_read_b128 v[76:79], v124 offset:27648\n"
"s_xor_b32 s43, s62, 0x7000\n"
"s_add_u32 m0, s55, 0x1c00\n"
"s_add_u32 s43, s43, s39\n"
"buffer_load_dwordx4 v121, s[28:31], s43 offen offset:0 lds\n"
"v_mfma_f32_16x16x32_bf16 v[24:27], a[32:35], v[80:83], v[24:27]\n"
"s_setprio 2\n"
"v_mfma_f32_16x16x32_bf16 v[24:27], a[40:43], v[84:87], v[24:27]\n"
"v_fmac_f32 v122, v16, v17\n"
"v_fmac_f32 v122, v18, v19\n"
"v_fmac_f32 v122, v20, v21\n"
"v_mfma_f32_16x16x32_bf16 v[24:27], a[48:51], v[88:91], v[24:27]\n"
"v_fmac_f32 v122, v22, v23\n"
"v_cvt_pk_bf16_f32 v16, v16, v17\n"
"v_cvt_pk_bf16_f32 v17, v18, v19\n"
"v_mfma_f32_16x16x32_bf16 v[24:27], a[56:59], v[92:95], v[24:27]\n"
"v_cvt_pk_bf16_f32 v18, v20, v21\n"
"v_cvt_pk_bf16_f32 v19, v22, v23\n"
"v_mfma_f32_16x16x32_bf16 v[28:31], a[36:39], v[80:83], v[28:31]\n"
"s_waitcnt vmcnt(10)\n"
"v_lshlrev_b32 v112, 16, v108\n"
"v_and_b32 v113, s45, v108\n"
"v_mfma_f32_16x16x32_bf16 v[28:31], a[44:47], v[84:87], v[28:31]\n"
"v_mul_f32 v112, s44, v112\n"
"v_mul_f32 v113, s44, v113\n"
"v_mfma_f32_16x16x32_bf16 v[28:31], a[52:55], v[88:91], v[28:31]\n"
"v_cvt_pk_bf16_f32 v108, v112, v113\n"
"v_lshlrev_b32 v112, 16, v109\n"
"v_mfma_f32_16x16x32_bf16 v[28:31], a[60:63], v[92:95], v[28:31]\n"
"s_setprio 0\n"
"ds_read_b128 v[80:83], v124 offset:28672\n"
"ds_read_b128 v[84:87], v124 offset:29696\n"
"ds_read_b128 v[88:91], v124 offset:30720\n"
"ds_read_b128 v[92:95], v124 offset:31744\n"
"s_add_u32 s41, s52, 0x9000\n"
"buffer_store_dwordx4 v[16:19], v127, s[64:67], s41 offen sc1\n"
"s_waitcnt lgkmcnt(4)\n"
"v_mfma_f32_16x16x32_bf16 v[24:27], a[64:67], v[64:67], v[24:27]\n"
"s_setprio 2\n"
"v_mfma_f32_16x16x32_bf16 v[24:27], a[72:75], v[68:71], v[24:27]\n"
"v_and_b32 v113, s45, v109\n"
"v_mul_f32 v112, s44, v112\n"
"v_mul_f32 v113, s44, v113\n"
"v_mfma_f32_16x16x32_bf16 v[24:27], a[80:83], v[72:75], v[24:27]\n"
"v_cvt_pk_bf16_f32 v109, v112, v113\n"
"v_lshlrev_b32 v112, 16, v110\n"
"v_and_b32 v113, s45, v110\n"
"v_mfma_f32_16x16x32_bf16 v[24:27], a[88:91], v[76:79], v[24:27]\n"
"v_mul_f32 v112, s44, v112\n"
"v_mul_f32 v113, s44, v113\n"
"v_mfma_f32_16x16x32_bf16 v[28:31], a[68:71], v[64:67], v[28:31]\n"
"v_cvt_pk_bf16_f32 v110, v112, v113\n"
"v_lshlrev_b32 v112, 16, v111\n"
"v_mfma_f32_16x16x32_bf16 v[28:31], a[76:79], v[68:71], v[28:31]\n"
"v_and_b32 v113, s45, v111\n"
"v_mul_f32 v112, s44, v112\n"
"v_mfma_f32_16x16x32_bf16 v[28:31], a[84:87], v[72:75], v[28:31]\n"
"v_mul_f32 v113, s44, v113\n"
"v_cvt_pk_bf16_f32 v111, v112, v113\n"
"s_waitcnt lgkmcnt(0)\n"
"s_setprio 0\n"
"v_mfma_f32_16x16x32_bf16 v[28:31], a[92:95], v[76:79], v[28:31]\n"
"s_barrier\n"
"ds_read_b128 v[64:67], v124 offset:32768\n"
"ds_read_b128 v[68:71], v124 offset:33792\n"
"ds_read_b128 v[72:75], v124 offset:34816\n"
"ds_read_b128 v[76:79], v124 offset:35840\n"
"ds_write_b128 v126, v[108:111] offset:45056\n"
"s_add_u32 s40, s51, 0x12000\n"
"buffer_load_dwordx4 v[104:107], v124, s[20:23], s40 offen nt\n"
"v_mfma_f32_16x16x32_bf16 v[24:27], a[96:99], v[80:83], v[24:27]\n"
"s_setprio 2\n"
"v_mfma_f32_16x16x32_bf16 v[24:27], a[104:107], v[84:87], v[24:27]\n"
"v_fmac_f32 v122, v32, v33\n"
"v_fmac_f32 v122, v34, v35\n"
"v_mfma_f32_16x16x32_bf16 v[24:27], a[112:115], v[88:91], v[24:27]\n"
"v_fmac_f32 v122, v36, v37\n"
"v_fmac_f32 v122, v38, v39\n"
"v_mfma_f32_16x16x32_bf16 v[24:27], a[120:123], v[92:95], v[24:27]\n"
"v_cvt_pk_bf16_f32 v32, v32, v33\n"
"v_cvt_pk_bf16_f32 v33, v34, v35\n"
"v_mfma_f32_16x16x32_bf16 v[28:31], a[100:103], v[80:83], v[28:31]\n"
"v_cvt_pk_bf16_f32 v34, v36, v37\n"
"v_mfma_f32_16x16x32_bf16 v[28:31], a[108:111], v[84:87], v[28:31]\n"
"v_cvt_pk_bf16_f32 v35, v38, v39\n"
"v_mfma_f32_16x16x32_bf16 v[28:31], a[116:119], v[88:91], v[28:31]\n"
"v_mfma_f32_16x16x32_bf16 v[28:31], a[124:127], v[92:95], v[28:31]\n"
"s_setprio 0\n"
"ds_read_b128 v[80:83], v124 offset:0\n"
"ds_read_b128 v[84:87], v124 offset:1024\n"
"ds_read_b128 v[88:91], v124 offset:2048\n"
"ds_read_b128 v[92:95], v124 offset:3072\n"
"ds_read_b128 v[114:117], v124 offset:46080\n"
"s_add_u32 s41, s52, 0x4000\n"
"buffer_store_dwordx4 v[32:35], v127, s[64:67], s41 offen sc1\n"
"s_waitcnt lgkmcnt(6)\n"
"v_mfma_f32_16x16x32_bf16 v[16:19], a[0:3], v[64:67], 0\n"
"s_setprio 2\n"
"v_mfma_f32_16x16x32_bf16 v[16:19], a[8:11], v[68:71], v[16:19]\n"
"s_waitcnt lgkmcnt(0)\n"
"v_add_f32 v114, v114, v115\n"
"v_add_f32 v116, v116, v117\n"
"v_add_f32 v114, v114, v116\n"
"v_mfma_f32_16x16x32_bf16 v[16:19], a[16:19], v[72:75], v[16:19]\n"
"v_rcp_f32 v119, v114\n"
"v_subrev_u32 v118, s46, v114\n"
"v_max_u32 v123, v123, v118\n"
"v_mfma_f32_16x16x32_bf16 v[16:19], a[24:27], v[76:79], v[16:19]\n"
"v_mul_f32 v24, v119, v24\n"
"v_mul_f32 v25, v119, v25\n"
"v_mfma_f32_16x16x32_bf16 v[20:23], a[4:7], v[64:67], 0\n"
"v_mul_f32 v26, v119, v26\n"
"v_mul_f32 v27, v119, v27\n"
"v_mfma_f32_16x16x32_bf16 v[20:23], a[12:15], v[68:71], v[20:23]\n"
"v_mul_f32 v28, v119, v28\n"
"v_mul_f32 v29, v119, v29\n"
"v_mfma_f32_16x16x32_bf16 v[20:23], a[20:23], v[72:75], v[20:23]\n"
"v_mul_f32 v30, v119, v30\n"
"v_mul_f32 v31, v119, v31\n"
"v_mfma_f32_16x16x32_bf16 v[20:23], a[28:31], v[76:79], v[20:23]\n"
"s_setprio 0\n"
"ds_read_b128 v[64:67], v124 offset:4096\n"
"ds_read_b128 v[68:71], v124 offset:5120\n"
"ds_read_b128 v[72:75], v124 offset:6144\n"
"ds_read_b128 v[76:79], v124 offset:7168\n"
"ds_read_b128 v[114:117], v124 offset:41984\n"
"v_mfma_f32_16x16x32_bf16 v[16:19], a[32:35], v[80:83], v[16:19]\n"
"s_setprio 2\n"
"v_mfma_f32_16x16x32_bf16 v[16:19], a[40:43], v[84:87], v[16:19]\n"
"s_waitcnt lgkmcnt(0)\n"
"v_add_f32 v114, v114, v115\n"
"v_add_f32 v116, v116, v117\n"
"v_add_f32 v114, v114, v116\n"
"v_mfma_f32_16x16x32_bf16 v[16:19], a[48:51], v[88:91], v[16:19]\n"
"v_rcp_f32 v120, v114\n"
"v_subrev_u32 v118, s46, v114\n"
"v_max_u32 v123, v123, v118\n"
"v_mfma_f32_16x16x32_bf16 v[16:19], a[56:59], v[92:95], v[16:19]\n"
"v_fmac_f32 v122, v24, v25\n"
"v_fmac_f32 v122, v26, v27\n"
"v_mfma_f32_16x16x32_bf16 v[20:23], a[36:39], v[80:83], v[20:23]\n"
"v_fmac_f32 v122, v28, v29\n"
"v_fmac_f32 v122, v30, v31\n"
"v_mfma_f32_16x16x32_bf16 v[20:23], a[44:47], v[84:87], v[20:23]\n"
"v_cvt_pk_bf16_f32 v24, v24, v25\n"
"v_cvt_pk_bf16_f32 v25, v26, v27\n"
"v_mfma_f32_16x16x32_bf16 v[20:23], a[52:55], v[88:91], v[20:23]\n"
"v_cvt_pk_bf16_f32 v26, v28, v29\n"
"v_cvt_pk_bf16_f32 v27, v30, v31\n"
"v_mfma_f32_16x16x32_bf16 v[20:23], a[60:63], v[92:95], v[20:23]\n"
"s_setprio 0\n"
"ds_read_b128 v[80:83], v124 offset:8192\n"
"ds_read_b128 v[84:87], v124 offset:9216\n"
"ds_read_b128 v[88:91], v124 offset:10240\n"
"ds_read_b128 v[92:95], v124 offset:11264\n"
"s_add_u32 s41, s52, 0xa000\n"
"buffer_store_dwordx4 v[24:27], v127, s[64:67], s41 offen sc1\n"
"v_mfma_f32_16x16x32_bf16 v[16:19], a[64:67], v[64:67], v[16:19]\n"
"s_setprio 2\n"
"v_mfma_f32_16x16x32_bf16 v[16:19], a[72:75], v[68:71], v[16:19]\n"
"s_waitcnt vmcnt(10)\n"
"v_lshlrev_b32 v112, 16, v96\n"
"v_and_b32 v113, s45, v96\n"
"v_mul_f32 v112, s44, v112\n"
"v_mfma_f32_16x16x32_bf16 v[16:19], a[80:83], v[72:75], v[16:19]\n"
"v_mul_f32 v113, s44, v113\n"
"v_cvt_pk_bf16_f32 v96, v112, v113\n"
"v_lshlrev_b32 v112, 16, v97\n"
"v_mfma_f32_16x16x32_bf16 v[16:19], a[88:91], v[76:79], v[16:19]\n"
"v_and_b32 v113, s45, v97\n"
"v_mul_f32 v112, s44, v112\n"
"v_mfma_f32_16x16x32_bf16 v[20:23], a[68:71], v[64:67], v[20:23]\n"
"v_mul_f32 v113, s44, v113\n"
"v_cvt_pk_bf16_f32 v97, v112, v113\n"
"v_mfma_f32_16x16x32_bf16 v[20:23], a[76:79], v[68:71], v[20:23]\n"
"v_lshlrev_b32 v112, 16, v98\n"
"v_and_b32 v113, s45, v98\n"
"v_mfma_f32_16x16x32_bf16 v[20:23], a[84:87], v[72:75], v[20:23]\n"
"v_mul_f32 v112, s44, v112\n"
"v_mul_f32 v113, s44, v113\n"
"s_waitcnt lgkmcnt(0)\n"
"s_setprio 0\n"
"v_mfma_f32_16x16x32_bf16 v[20:23], a[92:95], v[76:79], v[20:23]\n"
"s_barrier\n"
"ds_read_b128 v[64:67], v124 offset:12288\n"
"ds_read_b128 v[68:71], v124 offset:13312\n"
"ds_read_b128 v[72:75], v124 offset:14336\n"
"ds_read_b128 v[76:79], v124 offset:15360\n"
"s_add_u32 s40, s51, 0x13000\n"
"buffer_load_dwordx4 v[108:111], v124, s[20:23], s40 offen nt\n"
"v_mfma_f32_16x16x32_bf16 v[16:19], a[96:99], v[80:83], v[16:19]\n"
"s_setprio 2\n"
"v_mfma_f32_16x16x32_bf16 v[16:19], a[104:107], v[84:87], v[16:19]\n"
"v_cvt_pk_bf16_f32 v98, v112, v113\n"
"v_lshlrev_b32 v112, 16, v99\n"
"v_and_b32 v113, s45, v99\n"
"v_mfma_f32_16x16x32_bf16 v[16:19], a[112:115], v[88:91], v[16:19]\n"
"v_mul_f32 v112, s44, v112\n"
"v_mul_f32 v113, s44, v113\n"
"v_cvt_pk_bf16_f32 v99, v112, v113\n"
"v_mfma_f32_16x16x32_bf16 v[16:19], a[120:123], v[92:95], v[16:19]\n"
"v_mul_f32 v40, v120, v40\n"
"v_mul_f32 v41, v120, v41\n"
"v_mfma_f32_16x16x32_bf16 v[20:23], a[100:103], v[80:83], v[20:23]\n"
"v_mul_f32 v42, v120, v42\n"
"v_mul_f32 v43, v120, v43\n"
"v_mfma_f32_16x16x32_bf16 v[20:23], a[108:111], v[84:87], v[20:23]\n"
"v_mul_f32 v44, v120, v44\n"
"v_mul_f32 v45, v120, v45\n"
"v_mfma_f32_16x16x32_bf16 v[20:23], a[116:119], v[88:91], v[20:23]\n"
"v_mul_f32 v46, v120, v46\n"
"v_mul_f32 v47, v120, v47\n"
"v_mfma_f32_16x16x32_bf16 v[20:23], a[124:127], v[92:95], v[20:23]\n"
"s_setprio 0\n"
"ds_read_b128 v[80:83], v124 offset:16384\n"
"ds_read_b128 v[84:87], v124 offset:17408\n"
"ds_read_b128 v[88:91], v124 offset:18432\n"
"ds_read_b128 v[92:95], v124 offset:19456\n"
"ds_read_b128 v[114:117], v124 offset:47104\n"
"ds_write_b128 v126, v[96:99] offset:32768\n"
"s_waitcnt lgkmcnt(6)\n"
"v_mfma_f32_16x16x32_bf16 v[24:27], a[0:3], v[64:67], 0\n"
"s_setprio 2\n"
"v_mfma_f32_16x16x32_bf16 v[24:27], a[8:11], v[68:71], v[24:27]\n"
"s_waitcnt lgkmcnt(1)\n"
"v_add_f32 v114, v114, v115\n"
"v_add_f32 v116, v116, v117\n"
"v_add_f32 v114, v114, v116\n"
"v_mfma_f32_16x16x32_bf16 v[24:27], a[16:19], v[72:75], v[24:27]\n"
"v_rcp_f32 v119, v114\n"
"v_subrev_u32 v118, s46, v114\n"
"v_max_u32 v123, v123, v118\n"
"v_mfma_f32_16x16x32_bf16 v[24:27], a[24:27], v[76:79], v[24:27]\n"
"v_mul_f32 v16, v119, v16\n"
"v_mul_f32 v17, v119, v17\n"
"v_mfma_f32_16x16x32_bf16 v[28:31], a[4:7], v[64:67], 0\n"
"v_mul_f32 v18, v119, v18\n"
"v_mul_f32 v19, v119, v19\n"
"v_mfma_f32_16x16x32_bf16 v[28:31], a[12:15], v[68:71], v[28:31]\n"
"v_mul_f32 v20, v119, v20\n"
"v_mul_f32 v21, v119, v21\n"
"v_mfma_f32_16x16x32_bf16 v[28:31], a[20:23], v[72:75], v[28:31]\n"
"v_mul_f32 v22, v119, v22\n"
"v_mul_f32 v23, v119, v23\n"
"v_mfma_f32_16x16x32_bf16 v[28:31], a[28:31], v[76:79], v[28:31]\n"
"s_setprio 0\n"
"ds_read_b128 v[64:67], v124 offset:20480\n"
"ds_read_b128 v[68:71], v124 offset:21504\n"
"ds_read_b128 v[72:75], v124 offset:22528\n"
"ds_read_b128 v[76:79], v124 offset:23552\n"
"v_mfma_f32_16x16x32_bf16 v[24:27], a[32:35], v[80:83], v[24:27]\n"
"s_setprio 2\n"
"v_mfma_f32_16x16x32_bf16 v[24:27], a[40:43], v[84:87], v[24:27]\n"
"v_fmac_f32 v122, v16, v17\n"
"v_fmac_f32 v122, v18, v19\n"
"v_fmac_f32 v122, v20, v21\n"
"v_mfma_f32_16x16x32_bf16 v[24:27], a[48:51], v[88:91], v[24:27]\n"
"v_fmac_f32 v122, v22, v23\n"
"v_cvt_pk_bf16_f32 v16, v16, v17\n"
"v_cvt_pk_bf16_f32 v17, v18, v19\n"
"v_mfma_f32_16x16x32_bf16 v[24:27], a[56:59], v[92:95], v[24:27]\n"
"v_cvt_pk_bf16_f32 v18, v20, v21\n"
"v_cvt_pk_bf16_f32 v19, v22, v23\n"
"v_mfma_f32_16x16x32_bf16 v[28:31], a[36:39], v[80:83], v[28:31]\n"
"s_waitcnt vmcnt(7)\n"
"v_lshlrev_b32 v112, 16, v100\n"
"v_and_b32 v113, s45, v100\n"
"v_mfma_f32_16x16x32_bf16 v[28:31], a[44:47], v[84:87], v[28:31]\n"
"v_mul_f32 v112, s44, v112\n"
"v_mul_f32 v113, s44, v113\n"
"v_mfma_f32_16x16x32_bf16 v[28:31], a[52:55], v[88:91], v[28:31]\n"
"v_cvt_pk_bf16_f32 v100, v112, v113\n"
"v_lshlrev_b32 v112, 16, v101\n"
"v_mfma_f32_16x16x32_bf16 v[28:31], a[60:63], v[92:95], v[28:31]\n"
"s_setprio 0\n"
"ds_read_b128 v[80:83], v124 offset:24576\n"
"ds_read_b128 v[84:87], v124 offset:25600\n"
"ds_read_b128 v[88:91], v124 offset:26624\n"
"ds_read_b128 v[92:95], v124 offset:27648\n"
"s_add_u32 s41, s52, 0xb000\n"
"buffer_store_dwordx4 v[16:19], v127, s[64:67], s41 offen sc1\n"
"s_waitcnt lgkmcnt(4)\n"
"v_mfma_f32_16x16x32_bf16 v[24:27], a[64:67], v[64:67], v[24:27]\n"
"s_setprio 2\n"
"v_mfma_f32_16x16x32_bf16 v[24:27], a[72:75], v[68:71], v[24:27]\n"
"v_and_b32 v113, s45, v101\n"
"v_mul_f32 v112, s44, v112\n"
"v_mul_f32 v113, s44, v113\n"
"v_mfma_f32_16x16x32_bf16 v[24:27], a[80:83], v[72:75], v[24:27]\n"
"v_cvt_pk_bf16_f32 v101, v112, v113\n"
"v_lshlrev_b32 v112, 16, v102\n"
"v_and_b32 v113, s45, v102\n"
"v_mfma_f32_16x16x32_bf16 v[24:27], a[88:91], v[76:79], v[24:27]\n"
"v_mul_f32 v112, s44, v112\n"
"v_mul_f32 v113, s44, v113\n"
"v_mfma_f32_16x16x32_bf16 v[28:31], a[68:71], v[64:67], v[28:31]\n"
"v_cvt_pk_bf16_f32 v102, v112, v113\n"
"v_lshlrev_b32 v112, 16, v103\n"
"v_mfma_f32_16x16x32_bf16 v[28:31], a[76:79], v[68:71], v[28:31]\n"
"v_and_b32 v113, s45, v103\n"
"v_mul_f32 v112, s44, v112\n"
"v_mfma_f32_16x16x32_bf16 v[28:31], a[84:87], v[72:75], v[28:31]\n"
"v_mul_f32 v113, s44, v113\n"
"v_cvt_pk_bf16_f32 v103, v112, v113\n"
"s_waitcnt lgkmcnt(0)\n"
"s_setprio 0\n"
"v_mfma_f32_16x16x32_bf16 v[28:31], a[92:95], v[76:79], v[28:31]\n"
"s_barrier\n"
"ds_read_b128 v[64:67], v124 offset:28672\n"
"ds_read_b128 v[68:71], v124 offset:29696\n"
"ds_read_b128 v[72:75], v124 offset:30720\n"
"ds_read_b128 v[76:79], v124 offset:31744\n"
"ds_write_b128 v126, v[100:103] offset:36864\n"
"s_add_u32 s40, s51, 0x14000\n"
"buffer_load_dwordx4 v[96:99], v124, s[20:23], s40 offen nt\n"
"v_mfma_f32_16x16x32_bf16 v[24:27], a[96:99], v[80:83], v[24:27]\n"
"s_setprio 2\n"
"v_mfma_f32_16x16x32_bf16 v[24:27], a[104:107], v[84:87], v[24:27]\n"
"v_fmac_f32 v122, v40, v41\n"
"v_fmac_f32 v122, v42, v43\n"
"v_mfma_f32_16x16x32_bf16 v[24:27], a[112:115], v[88:91], v[24:27]\n"
"v_fmac_f32 v122, v44, v45\n"
"v_fmac_f32 v122, v46, v47\n"
"v_mfma_f32_16x16x32_bf16 v[24:27], a[120:123], v[92:95], v[24:27]\n"
"v_cvt_pk_bf16_f32 v40, v40, v41\n"
"v_cvt_pk_bf16_f32 v41, v42, v43\n"
"v_mfma_f32_16x16x32_bf16 v[28:31], a[100:103], v[80:83], v[28:31]\n"
"v_cvt_pk_bf16_f32 v42, v44, v45\n"
"v_mfma_f32_16x16x32_bf16 v[28:31], a[108:111], v[84:87], v[28:31]\n"
"v_cvt_pk_bf16_f32 v43, v46, v47\n"
"v_mfma_f32_16x16x32_bf16 v[28:31], a[116:119], v[88:91], v[28:31]\n"
"v_mfma_f32_16x16x32_bf16 v[28:31], a[124:127], v[92:95], v[28:31]\n"
"s_setprio 0\n"
"ds_read_b128 v[80:83], v124 offset:32768\n"
"ds_read_b128 v[84:87], v124 offset:33792\n"
"ds_read_b128 v[88:91], v124 offset:34816\n"
"ds_read_b128 v[92:95], v124 offset:35840\n"
"ds_read_b128 v[114:117], v124 offset:45056\n"
"s_add_u32 s41, s52, 0x5000\n"
"buffer_store_dwordx4 v[40:43], v127, s[64:67], s41 offen sc1\n"
"s_waitcnt lgkmcnt(6)\n"
"v_mfma_f32_16x16x32_bf16 v[16:19], a[0:3], v[64:67], 0\n"
"s_setprio 2\n"
"v_mfma_f32_16x16x32_bf16 v[16:19], a[8:11], v[68:71], v[16:19]\n"
"s_waitcnt lgkmcnt(0)\n"
"v_add_f32 v114, v114, v115\n"
"v_add_f32 v116, v116, v117\n"
"v_add_f32 v114, v114, v116\n"
"v_mfma_f32_16x16x32_bf16 v[16:19], a[16:19], v[72:75], v[16:19]\n"
"v_rcp_f32 v119, v114\n"
"v_subrev_u32 v118, s46, v114\n"
"v_max_u32 v123, v123, v118\n"
"v_mfma_f32_16x16x32_bf16 v[16:19], a[24:27], v[76:79], v[16:19]\n"
"v_mul_f32 v24, v119, v24\n"
"v_mul_f32 v25, v119, v25\n"
"v_mfma_f32_16x16x32_bf16 v[20:23], a[4:7], v[64:67], 0\n"
"v_mul_f32 v26, v119, v26\n"
"v_mul_f32 v27, v119, v27\n"
"v_mfma_f32_16x16x32_bf16 v[20:23], a[12:15], v[68:71], v[20:23]\n"
"v_mul_f32 v28, v119, v28\n"
"v_mul_f32 v29, v119, v29\n"
"v_mfma_f32_16x16x32_bf16 v[20:23], a[20:23], v[72:75], v[20:23]\n"
"v_mul_f32 v30, v119, v30\n"
"v_mul_f32 v31, v119, v31\n"
"v_mfma_f32_16x16x32_bf16 v[20:23], a[28:31], v[76:79], v[20:23]\n"
"s_setprio 0\n"
"ds_read_b128 v[64:67], v124 offset:0\n"
"ds_read_b128 v[68:71], v124 offset:1024\n"
"ds_read_b128 v[72:75], v124 offset:2048\n"
"ds_read_b128 v[76:79], v124 offset:3072\n"
"ds_read_b128 v[114:117], v124 offset:43008\n"
"v_mfma_f32_16x16x32_bf16 v[16:19], a[32:35], v[80:83], v[16:19]\n"
"s_setprio 2\n"
"v_mfma_f32_16x16x32_bf16 v[16:19], a[40:43], v[84:87], v[16:19]\n"
"s_waitcnt lgkmcnt(0)\n"
"v_add_f32 v114, v114, v115\n"
"v_add_f32 v116, v116, v117\n"
"v_add_f32 v114, v114, v116\n"
"v_mfma_f32_16x16x32_bf16 v[16:19], a[48:51], v[88:91], v[16:19]\n"
"v_rcp_f32 v120, v114\n"
"v_subrev_u32 v118, s46, v114\n"
"v_max_u32 v123, v123, v118\n"
"v_mfma_f32_16x16x32_bf16 v[16:19], a[56:59], v[92:95], v[16:19]\n"
"v_fmac_f32 v122, v24, v25\n"
"v_fmac_f32 v122, v26, v27\n"
"v_mfma_f32_16x16x32_bf16 v[20:23], a[36:39], v[80:83], v[20:23]\n"
"v_fmac_f32 v122, v28, v29\n"
"v_fmac_f32 v122, v30, v31\n"
"v_mfma_f32_16x16x32_bf16 v[20:23], a[44:47], v[84:87], v[20:23]\n"
"v_cvt_pk_bf16_f32 v24, v24, v25\n"
"v_cvt_pk_bf16_f32 v25, v26, v27\n"
"v_mfma_f32_16x16x32_bf16 v[20:23], a[52:55], v[88:91], v[20:23]\n"
"v_cvt_pk_bf16_f32 v26, v28, v29\n"
"v_cvt_pk_bf16_f32 v27, v30, v31\n"
"v_mfma_f32_16x16x32_bf16 v[20:23], a[60:63], v[92:95], v[20:23]\n"
"s_setprio 0\n"
"ds_read_b128 v[80:83], v124 offset:4096\n"
"ds_read_b128 v[84:87], v124 offset:5120\n"
"ds_read_b128 v[88:91], v124 offset:6144\n"
"ds_read_b128 v[92:95], v124 offset:7168\n"
"s_add_u32 s41, s52, 0xc000\n"
"buffer_store_dwordx4 v[24:27], v127, s[64:67], s41 offen sc1\n"
"v_mfma_f32_16x16x32_bf16 v[16:19], a[64:67], v[64:67], v[16:19]\n"
"s_setprio 2\n"
"v_mfma_f32_16x16x32_bf16 v[16:19], a[72:75], v[68:71], v[16:19]\n"
"s_waitcnt vmcnt(7)\n"
"v_lshlrev_b32 v112, 16, v104\n"
"v_and_b32 v113, s45, v104\n"
"v_mul_f32 v112, s44, v112\n"
"v_mfma_f32_16x16x32_bf16 v[16:19], a[80:83], v[72:75], v[16:19]\n"
"v_mul_f32 v113, s44, v113\n"
"v_cvt_pk_bf16_f32 v104, v112, v113\n"
"v_lshlrev_b32 v112, 16, v105\n"
"v_mfma_f32_16x16x32_bf16 v[16:19], a[88:91], v[76:79], v[16:19]\n"
"v_and_b32 v113, s45, v105\n"
"v_mul_f32 v112, s44, v112\n"
"v_mfma_f32_16x16x32_bf16 v[20:23], a[68:71], v[64:67], v[20:23]\n"
"v_mul_f32 v113, s44, v113\n"
"v_cvt_pk_bf16_f32 v105, v112, v113\n"
"v_mfma_f32_16x16x32_bf16 v[20:23], a[76:79], v[68:71], v[20:23]\n"
"v_lshlrev_b32 v112, 16, v106\n"
"v_and_b32 v113, s45, v106\n"
"v_mfma_f32_16x16x32_bf16 v[20:23], a[84:87], v[72:75], v[20:23]\n"
"v_mul_f32 v112, s44, v112\n"
"v_mul_f32 v113, s44, v113\n"
"s_waitcnt lgkmcnt(0)\n"
"s_setprio 0\n"
"v_mfma_f32_16x16x32_bf16 v[20:23], a[92:95], v[76:79], v[20:23]\n"
"s_barrier\n"
"ds_read_b128 v[64:67], v124 offset:8192\n"
"ds_read_b128 v[68:71], v124 offset:9216\n"
"ds_read_b128 v[72:75], v124 offset:10240\n"
"ds_read_b128 v[76:79], v124 offset:11264\n"
"s_add_u32 s40, s51, 0x15000\n"
"buffer_load_dwordx4 v[100:103], v124, s[20:23], s40 offen nt\n"
"v_mfma_f32_16x16x32_bf16 v[16:19], a[96:99], v[80:83], v[16:19]\n"
"s_setprio 2\n"
"v_mfma_f32_16x16x32_bf16 v[16:19], a[104:107], v[84:87], v[16:19]\n"
"v_cvt_pk_bf16_f32 v106, v112, v113\n"
"v_lshlrev_b32 v112, 16, v107\n"
"v_and_b32 v113, s45, v107\n"
"v_mfma_f32_16x16x32_bf16 v[16:19], a[112:115], v[88:91], v[16:19]\n"
"v_mul_f32 v112, s44, v112\n"
"v_mul_f32 v113, s44, v113\n"
"v_cvt_pk_bf16_f32 v107, v112, v113\n"
"v_mfma_f32_16x16x32_bf16 v[16:19], a[120:123], v[92:95], v[16:19]\n"
"v_mul_f32 v48, v120, v48\n"
"v_mul_f32 v49, v120, v49\n"
"v_mfma_f32_16x16x32_bf16 v[20:23], a[100:103], v[80:83], v[20:23]\n"
"v_mul_f32 v50, v120, v50\n"
"v_mul_f32 v51, v120, v51\n"
"v_mfma_f32_16x16x32_bf16 v[20:23], a[108:111], v[84:87], v[20:23]\n"
"v_mul_f32 v52, v120, v52\n"
"v_mul_f32 v53, v120, v53\n"
"v_mfma_f32_16x16x32_bf16 v[20:23], a[116:119], v[88:91], v[20:23]\n"
"v_mul_f32 v54, v120, v54\n"
"v_mul_f32 v55, v120, v55\n"
"v_mfma_f32_16x16x32_bf16 v[20:23], a[124:127], v[92:95], v[20:23]\n"
"s_setprio 0\n"
"ds_read_b128 v[80:83], v124 offset:12288\n"
"ds_read_b128 v[84:87], v124 offset:13312\n"
"ds_read_b128 v[88:91], v124 offset:14336\n"
"ds_read_b128 v[92:95], v124 offset:15360\n"
"ds_read_b128 v[114:117], v124 offset:46080\n"
"ds_write_b128 v126, v[104:107] offset:40960\n"
"s_waitcnt lgkmcnt(6)\n"
"v_mfma_f32_16x16x32_bf16 v[24:27], a[0:3], v[64:67], 0\n"
"s_setprio 2\n"
"v_mfma_f32_16x16x32_bf16 v[24:27], a[8:11], v[68:71], v[24:27]\n"
"s_waitcnt lgkmcnt(1)\n"
"v_add_f32 v114, v114, v115\n"
"v_add_f32 v116, v116, v117\n"
"v_add_f32 v114, v114, v116\n"
"v_mfma_f32_16x16x32_bf16 v[24:27], a[16:19], v[72:75], v[24:27]\n"
"v_rcp_f32 v119, v114\n"
"v_subrev_u32 v118, s46, v114\n"
"v_max_u32 v123, v123, v118\n"
"v_mfma_f32_16x16x32_bf16 v[24:27], a[24:27], v[76:79], v[24:27]\n"
"v_mul_f32 v16, v119, v16\n"
"v_mul_f32 v17, v119, v17\n"
"v_mfma_f32_16x16x32_bf16 v[28:31], a[4:7], v[64:67], 0\n"
"v_mul_f32 v18, v119, v18\n"
"v_mul_f32 v19, v119, v19\n"
"v_mfma_f32_16x16x32_bf16 v[28:31], a[12:15], v[68:71], v[28:31]\n"
"v_mul_f32 v20, v119, v20\n"
"v_mul_f32 v21, v119, v21\n"
"v_mfma_f32_16x16x32_bf16 v[28:31], a[20:23], v[72:75], v[28:31]\n"
"v_mul_f32 v22, v119, v22\n"
"v_mul_f32 v23, v119, v23\n"
"v_mfma_f32_16x16x32_bf16 v[28:31], a[28:31], v[76:79], v[28:31]\n"
"s_setprio 0\n"
"ds_read_b128 v[64:67], v124 offset:16384\n"
"ds_read_b128 v[68:71], v124 offset:17408\n"
"ds_read_b128 v[72:75], v124 offset:18432\n"
"ds_read_b128 v[76:79], v124 offset:19456\n"
"ds_read_b64_tr_b16 v[32:33], v125 offset:0\n"
"ds_read_b64_tr_b16 v[36:37], v125 offset:8\n"
"v_mfma_f32_16x16x32_bf16 v[24:27], a[32:35], v[80:83], v[24:27]\n"
"s_setprio 2\n"
"v_mfma_f32_16x16x32_bf16 v[24:27], a[40:43], v[84:87], v[24:27]\n"
"v_fmac_f32 v122, v16, v17\n"
"v_fmac_f32 v122, v18, v19\n"
"v_fmac_f32 v122, v20, v21\n"
"v_mfma_f32_16x16x32_bf16 v[24:27], a[48:51], v[88:91], v[24:27]\n"
"v_fmac_f32 v122, v22, v23\n"
"v_cvt_pk_bf16_f32 v16, v16, v17\n"
"v_cvt_pk_bf16_f32 v17, v18, v19\n"
"v_mfma_f32_16x16x32_bf16 v[24:27], a[56:59], v[92:95], v[24:27]\n"
"v_cvt_pk_bf16_f32 v18, v20, v21\n"
"v_cvt_pk_bf16_f32 v19, v22, v23\n"
"v_mfma_f32_16x16x32_bf16 v[28:31], a[36:39], v[80:83], v[28:31]\n"
"v_fmac_f32 v122, v48, v49\n"
"v_fmac_f32 v122, v50, v51\n"
"v_mfma_f32_16x16x32_bf16 v[28:31], a[44:47], v[84:87], v[28:31]\n"
"v_fmac_f32 v122, v52, v53\n"
"v_fmac_f32 v122, v54, v55\n"
"v_mfma_f32_16x16x32_bf16 v[28:31], a[52:55], v[88:91], v[28:31]\n"
"v_cvt_pk_bf16_f32 v48, v48, v49\n"
"v_cvt_pk_bf16_f32 v49, v50, v51\n"
"v_mfma_f32_16x16x32_bf16 v[28:31], a[60:63], v[92:95], v[28:31]\n"
"s_setprio 0\n"
"ds_read_b128 v[80:83], v124 offset:20480\n"
"ds_read_b128 v[84:87], v124 offset:21504\n"
"ds_read_b128 v[88:91], v124 offset:22528\n"
"ds_read_b128 v[92:95], v124 offset:23552\n"
"s_add_u32 s41, s52, 0xd000\n"
"buffer_store_dwordx4 v[16:19], v127, s[64:67], s41 offen sc1\n"
"s_waitcnt lgkmcnt(4)\n"
"s_xor_b32 s43, s62, 0x8000\n"
"s_add_u32 m0, s55, 0\n"
"s_add_u32 s43, s43, s39\n"
"buffer_load_dwordx4 v121, s[28:31], s43 offen offset:0 lds\n"
"v_mfma_f32_16x16x32_bf16 v[24:27], a[64:67], v[64:67], v[24:27]\n"
"s_setprio 2\n"
"v_mfma_f32_16x16x32_bf16 v[24:27], a[72:75], v[68:71], v[24:27]\n"
"v_cvt_pk_bf16_f32 v50, v52, v53\n"
"v_cvt_pk_bf16_f32 v51, v54, v55\n"
"s_waitcnt vmcnt(7)\n"
"v_lshlrev_b32 v112, 16, v108\n"
"v_mfma_f32_16x16x32_bf16 v[24:27], a[80:83], v[72:75], v[24:27]\n"
"v_and_b32 v113, s45, v108\n"
"v_mul_f32 v112, s44, v112\n"
"v_mul_f32 v113, s44, v113\n"
"v_mfma_f32_16x16x32_bf16 v[24:27], a[88:91], v[76:79], v[24:27]\n"
"v_cvt_pk_bf16_f32 v108, v112, v113\n"
"v_lshlrev_b32 v112, 16, v109\n"
"v_mfma_f32_16x16x32_bf16 v[28:31], a[68:71], v[64:67], v[28:31]\n"
"v_and_b32 v113, s45, v109\n"
"v_mul_f32 v112, s44, v112\n"
"v_mfma_f32_16x16x32_bf16 v[28:31], a[76:79], v[68:71], v[28:31]\n"
"v_mul_f32 v113, s44, v113\n"
"v_cvt_pk_bf16_f32 v109, v112, v113\n"
"v_mfma_f32_16x16x32_bf16 v[28:31], a[84:87], v[72:75], v[28:31]\n"
"v_lshlrev_b32 v112, 16, v110\n"
"v_and_b32 v113, s45, v110\n"
"s_waitcnt lgkmcnt(0)\n"
"s_setprio 0\n"
"v_mfma_f32_16x16x32_bf16 v[28:31], a[92:95], v[76:79], v[28:31]\n"
"s_barrier\n"
"ds_read_b128 v[64:67], v124 offset:24576\n"
"ds_read_b128 v[68:71], v124 offset:25600\n"
"ds_read_b128 v[72:75], v124 offset:26624\n"
"ds_read_b128 v[76:79], v124 offset:27648\n"
"ds_read_b64_tr_b16 v[34:35], v125 offset:1024\n"
"ds_read_b64_tr_b16 v[38:39], v125 offset:1032\n"
"s_add_u32 s41, s52, 0x6000\n"
"buffer_store_dwordx4 v[48:51], v127, s[64:67], s41 offen sc1\n"
"s_add_u32 s40, s51, 0x16000\n"
"buffer_load_dwordx4 v[104:107], v124, s[20:23], s40 offen nt\n"
"v_mfma_f32_16x16x32_bf16 v[24:27], a[96:99], v[80:83], v[24:27]\n"
"s_setprio 2\n"
"v_mfma_f32_16x16x32_bf16 v[24:27], a[104:107], v[84:87], v[24:27]\n"
"v_mul_f32 v112, s44, v112\n"
"v_mul_f32 v113, s44, v113\n"
"v_mfma_f32_16x16x32_bf16 v[24:27], a[112:115], v[88:91], v[24:27]\n"
"v_cvt_pk_bf16_f32 v110, v112, v113\n"
"v_lshlrev_b32 v112, 16, v111\n"
"v_mfma_f32_16x16x32_bf16 v[24:27], a[120:123], v[92:95], v[24:27]\n"
"v_and_b32 v113, s45, v111\n"
"v_mul_f32 v112, s44, v112\n"
"v_mfma_f32_16x16x32_bf16 v[28:31], a[100:103], v[80:83], v[28:31]\n"
"v_mul_f32 v113, s44, v113\n"
"v_mfma_f32_16x16x32_bf16 v[28:31], a[108:111], v[84:87], v[28:31]\n"
"v_cvt_pk_bf16_f32 v111, v112, v113\n"
"v_mfma_f32_16x16x32_bf16 v[28:31], a[116:119], v[88:91], v[28:31]\n"
"v_mfma_f32_16x16x32_bf16 v[28:31], a[124:127], v[92:95], v[28:31]\n"
"s_setprio 0\n"
"ds_read_b128 v[80:83], v124 offset:28672\n"
"ds_read_b128 v[84:87], v124 offset:29696\n"
"ds_read_b128 v[88:91], v124 offset:30720\n"
"ds_read_b128 v[92:95], v124 offset:31744\n"
"ds_read_b128 v[114:117], v124 offset:47104\n"
"ds_write_b128 v126, v[108:111] offset:45056\n"
"s_waitcnt lgkmcnt(8)\n"
"v_mfma_f32_16x16x32_bf16 v[16:19], a[0:3], v[64:67], 0\n"
"s_setprio 2\n"
"v_mfma_f32_16x16x32_bf16 v[16:19], a[8:11], v[68:71], v[16:19]\n"
"s_waitcnt lgkmcnt(1)\n"
"v_add_f32 v114, v114, v115\n"
"v_add_f32 v116, v116, v117\n"
"v_add_f32 v114, v114, v116\n"
"v_mfma_f32_16x16x32_bf16 v[16:19], a[16:19], v[72:75], v[16:19]\n"
"v_rcp_f32 v119, v114\n"
"v_subrev_u32 v118, s46, v114\n"
"v_max_u32 v123, v123, v118\n"
"v_mfma_f32_16x16x32_bf16 v[16:19], a[24:27], v[76:79], v[16:19]\n"
"v_mul_f32 v24, v119, v24\n"
"v_mul_f32 v25, v119, v25\n"
"v_mfma_f32_16x16x32_bf16 v[20:23], a[4:7], v[64:67], 0\n"
"v_mul_f32 v26, v119, v26\n"
"v_mul_f32 v27, v119, v27\n"
"v_mfma_f32_16x16x32_bf16 v[20:23], a[12:15], v[68:71], v[20:23]\n"
"v_mul_f32 v28, v119, v28\n"
"v_mul_f32 v29, v119, v29\n"
"v_mfma_f32_16x16x32_bf16 v[20:23], a[20:23], v[72:75], v[20:23]\n"
"v_mul_f32 v30, v119, v30\n"
"v_mul_f32 v31, v119, v31\n"
"v_mfma_f32_16x16x32_bf16 v[20:23], a[28:31], v[76:79], v[20:23]\n"
"s_setprio 0\n"
"ds_read_b128 v[64:67], v124 offset:32768\n"
"ds_read_b128 v[68:71], v124 offset:33792\n"
"ds_read_b128 v[72:75], v124 offset:34816\n"
"ds_read_b128 v[76:79], v124 offset:35840\n"
"ds_read_b64_tr_b16 v[40:41], v125 offset:2048\n"
"ds_read_b64_tr_b16 v[44:45], v125 offset:2056\n"
"ds_read_b128 v[114:117], v124 offset:44032\n"
"s_xor_b32 s43, s62, 0x9000\n"
"s_add_u32 m0, s55, 0x400\n"
"s_add_u32 s43, s43, s39\n"
"buffer_load_dwordx4 v121, s[28:31], s43 offen offset:0 lds\n"
"v_mfma_f32_16x16x32_bf16 v[16:19], a[32:35], v[80:83], v[16:19]\n"
"s_setprio 2\n"
"v_mfma_f32_16x16x32_bf16 v[16:19], a[40:43], v[84:87], v[16:19]\n"
"s_waitcnt lgkmcnt(0)\n"
"v_add_f32 v114, v114, v115\n"
"v_add_f32 v116, v116, v117\n"
"v_add_f32 v114, v114, v116\n"
"v_mfma_f32_16x16x32_bf16 v[16:19], a[48:51], v[88:91], v[16:19]\n"
"v_rcp_f32 v120, v114\n"
"v_subrev_u32 v118, s46, v114\n"
"v_max_u32 v123, v123, v118\n"
"v_mfma_f32_16x16x32_bf16 v[16:19], a[56:59], v[92:95], v[16:19]\n"
"v_fmac_f32 v122, v24, v25\n"
"v_fmac_f32 v122, v26, v27\n"
"v_mfma_f32_16x16x32_bf16 v[20:23], a[36:39], v[80:83], v[20:23]\n"
"v_fmac_f32 v122, v28, v29\n"
"v_fmac_f32 v122, v30, v31\n"
"v_mfma_f32_16x16x32_bf16 v[20:23], a[44:47], v[84:87], v[20:23]\n"
"v_cvt_pk_bf16_f32 v24, v24, v25\n"
"v_cvt_pk_bf16_f32 v25, v26, v27\n"
"v_mfma_f32_16x16x32_bf16 v[20:23], a[52:55], v[88:91], v[20:23]\n"
"v_cvt_pk_bf16_f32 v26, v28, v29\n"
"v_cvt_pk_bf16_f32 v27, v30, v31\n"
"v_mfma_f32_16x16x32_bf16 v[20:23], a[60:63], v[92:95], v[20:23]\n"
"s_setprio 0\n"
"ds_read_b128 v[80:83], v124 offset:0\n"
"ds_read_b128 v[84:87], v124 offset:1024\n"
"ds_read_b128 v[88:91], v124 offset:2048\n"
"ds_read_b128 v[92:95], v124 offset:3072\n"
"s_add_u32 s41, s52, 0xe000\n"
"buffer_store_dwordx4 v[24:27], v127, s[64:67], s41 offen sc1\n"
"v_mfma_f32_16x16x32_bf16 v[16:19], a[64:67], v[64:67], v[16:19]\n"
"s_setprio 2\n"
"v_mfma_f32_16x16x32_bf16 v[16:19], a[72:75], v[68:71], v[16:19]\n"
"v_mul_f32 v56, v120, v56\n"
"v_mul_f32 v57, v120, v57\n"
"v_mul_f32 v58, v120, v58\n"
"v_mfma_f32_16x16x32_bf16 v[16:19], a[80:83], v[72:75], v[16:19]\n"
"v_mul_f32 v59, v120, v59\n"
"v_mul_f32 v60, v120, v60\n"
"v_mul_f32 v61, v120, v61\n"
"v_mfma_f32_16x16x32_bf16 v[16:19], a[88:91], v[76:79], v[16:19]\n"
"v_mul_f32 v62, v120, v62\n"
"v_mul_f32 v63, v120, v63\n"
"v_mfma_f32_16x16x32_bf16 v[20:23], a[68:71], v[64:67], v[20:23]\n"
"v_fmac_f32 v122, v56, v57\n"
"v_fmac_f32 v122, v58, v59\n"
"v_mfma_f32_16x16x32_bf16 v[20:23], a[76:79], v[68:71], v[20:23]\n"
"v_fmac_f32 v122, v60, v61\n"
"v_fmac_f32 v122, v62, v63\n"
"v_mfma_f32_16x16x32_bf16 v[20:23], a[84:87], v[72:75], v[20:23]\n"
"v_cvt_pk_bf16_f32 v56, v56, v57\n"
"v_cvt_pk_bf16_f32 v57, v58, v59\n"
"s_waitcnt lgkmcnt(0)\n"
"s_setprio 0\n"
"v_mfma_f32_16x16x32_bf16 v[20:23], a[92:95], v[76:79], v[20:23]\n"
"s_barrier\n"
"ds_read_b128 v[64:67], v124 offset:4096\n"
"ds_read_b128 v[68:71], v124 offset:5120\n"
"ds_read_b128 v[72:75], v124 offset:6144\n"
"ds_read_b128 v[76:79], v124 offset:7168\n"
"ds_read_b64_tr_b16 v[42:43], v125 offset:3072\n"
"ds_read_b64_tr_b16 v[46:47], v125 offset:3080\n"
"s_add_u32 s40, s51, 0x2e000\n"
"buffer_load_dwordx4 v[108:111], v124, s[20:23], s40 offen nt\n"
"v_mfma_f32_16x16x32_bf16 v[16:19], a[96:99], v[80:83], v[16:19]\n"
"s_setprio 2\n"
"v_mfma_f32_16x16x32_bf16 v[16:19], a[104:107], v[84:87], v[16:19]\n"
"v_cvt_pk_bf16_f32 v58, v60, v61\n"
"v_cvt_pk_bf16_f32 v59, v62, v63\n"
"s_waitcnt vmcnt(10)\n"
"v_lshlrev_b32 v112, 16, v96\n"
"v_mfma_f32_16x16x32_bf16 v[16:19], a[112:115], v[88:91], v[16:19]\n"
"v_and_b32 v113, s45, v96\n"
"v_mul_f32 v112, s44, v112\n"
"v_mul_f32 v113, s44, v113\n"
"v_mfma_f32_16x16x32_bf16 v[16:19], a[120:123], v[92:95], v[16:19]\n"
"v_cvt_pk_bf16_f32 v96, v112, v113\n"
"v_lshlrev_b32 v112, 16, v97\n"
"v_mfma_f32_16x16x32_bf16 v[20:23], a[100:103], v[80:83], v[20:23]\n"
"v_and_b32 v113, s45, v97\n"
"v_mul_f32 v112, s44, v112\n"
"v_mfma_f32_16x16x32_bf16 v[20:23], a[108:111], v[84:87], v[20:23]\n"
"v_mul_f32 v113, s44, v113\n"
"v_cvt_pk_bf16_f32 v97, v112, v113\n"
"v_mfma_f32_16x16x32_bf16 v[20:23], a[116:119], v[88:91], v[20:23]\n"
"v_lshlrev_b32 v112, 16, v98\n"
"v_and_b32 v113, s45, v98\n"
"v_mfma_f32_16x16x32_bf16 v[20:23], a[124:127], v[92:95], v[20:23]\n"
"s_setprio 0\n"
"ds_read_b128 v[80:83], v124 offset:8192\n"
"ds_read_b128 v[84:87], v124 offset:9216\n"
"ds_read_b128 v[88:91], v124 offset:10240\n"
"ds_read_b128 v[92:95], v124 offset:11264\n"
"ds_read_b128 v[114:117], v124 offset:45056\n"
"s_add_u32 s41, s52, 0x7000\n"
"buffer_store_dwordx4 v[56:59], v127, s[64:67], s41 offen sc1\n"
"s_xor_b32 s43, s62, 0xa000\n"
"s_add_u32 m0, s55, 0x800\n"
"s_add_u32 s43, s43, s39\n"
"buffer_load_dwordx4 v121, s[28:31], s43 offen offset:0 lds\n"
"s_waitcnt lgkmcnt(7)\n"
"v_mfma_f32_16x16x32_bf16 v[24:27], a[0:3], v[64:67], 0\n"
"s_setprio 2\n"
"v_mfma_f32_16x16x32_bf16 v[24:27], a[8:11], v[68:71], v[24:27]\n"
"s_waitcnt lgkmcnt(0)\n"
"v_add_f32 v114, v114, v115\n"
"v_add_f32 v116, v116, v117\n"
"v_add_f32 v114, v114, v116\n"
"v_mfma_f32_16x16x32_bf16 v[24:27], a[16:19], v[72:75], v[24:27]\n"
"v_rcp_f32 v119, v114\n"
"v_subrev_u32 v118, s46, v114\n"
"v_max_u32 v123, v123, v118\n"
"v_mfma_f32_16x16x32_bf16 v[24:27], a[24:27], v[76:79], v[24:27]\n"
"v_mul_f32 v16, v119, v16\n"
"v_mul_f32 v17, v119, v17\n"
"v_mfma_f32_16x16x32_bf16 v[28:31], a[4:7], v[64:67], 0\n"
"v_mul_f32 v18, v119, v18\n"
"v_mul_f32 v19, v119, v19\n"
"v_mfma_f32_16x16x32_bf16 v[28:31], a[12:15], v[68:71], v[28:31]\n"
"v_mul_f32 v20, v119, v20\n"
"v_mul_f32 v21, v119, v21\n"
"v_mfma_f32_16x16x32_bf16 v[28:31], a[20:23], v[72:75], v[28:31]\n"
"v_mul_f32 v22, v119, v22\n"
"v_mul_f32 v23, v119, v23\n"
"v_mfma_f32_16x16x32_bf16 v[28:31], a[28:31], v[76:79], v[28:31]\n"
"s_setprio 0\n"
"ds_read_b128 v[64:67], v124 offset:12288\n"
"ds_read_b128 v[68:71], v124 offset:13312\n"
"ds_read_b128 v[72:75], v124 offset:14336\n"
"ds_read_b128 v[76:79], v124 offset:15360\n"
"ds_read_b64_tr_b16 v[48:49], v125 offset:4096\n"
"ds_read_b64_tr_b16 v[52:53], v125 offset:4104\n"
"ds_read_b128 v[114:117], v124 offset:36864\n"
"v_mfma_f32_16x16x32_bf16 v[24:27], a[32:35], v[80:83], v[24:27]\n"
"s_setprio 2\n"
"v_mfma_f32_16x16x32_bf16 v[24:27], a[40:43], v[84:87], v[24:27]\n"
"s_waitcnt lgkmcnt(0)\n"
"v_add_f32 v114, v114, v115\n"
"v_add_f32 v116, v116, v117\n"
"v_add_f32 v114, v114, v116\n"
"v_mfma_f32_16x16x32_bf16 v[24:27], a[48:51], v[88:91], v[24:27]\n"
"v_rcp_f32 v120, v114\n"
"v_subrev_u32 v118, s46, v114\n"
"v_max_u32 v123, v123, v118\n"
"v_mfma_f32_16x16x32_bf16 v[24:27], a[56:59], v[92:95], v[24:27]\n"
"v_fmac_f32 v122, v16, v17\n"
"v_fmac_f32 v122, v18, v19\n"
"v_mfma_f32_16x16x32_bf16 v[28:31], a[36:39], v[80:83], v[28:31]\n"
"v_fmac_f32 v122, v20, v21\n"
"v_fmac_f32 v122, v22, v23\n"
"v_mfma_f32_16x16x32_bf16 v[28:31], a[44:47], v[84:87], v[28:31]\n"
"v_cvt_pk_bf16_f32 v16, v16, v17\n"
"v_cvt_pk_bf16_f32 v17, v18, v19\n"
"v_mfma_f32_16x16x32_bf16 v[28:31], a[52:55], v[88:91], v[28:31]\n"
"v_cvt_pk_bf16_f32 v18, v20, v21\n"
"v_cvt_pk_bf16_f32 v19, v22, v23\n"
"v_mfma_f32_16x16x32_bf16 v[28:31], a[60:63], v[92:95], v[28:31]\n"
"s_setprio 0\n"
"ds_read_b128 v[80:83], v124 offset:16384\n"
"ds_read_b128 v[84:87], v124 offset:17408\n"
"ds_read_b128 v[88:91], v124 offset:18432\n"
"ds_read_b128 v[92:95], v124 offset:19456\n"
"s_add_u32 s41, s52, 0xf000\n"
"buffer_store_dwordx4 v[16:19], v127, s[64:67], s41 offen sc1\n"
"v_mfma_f32_16x16x32_bf16 v[24:27], a[64:67], v[64:67], v[24:27]\n"
"s_setprio 2\n"
"v_mfma_f32_16x16x32_bf16 v[24:27], a[72:75], v[68:71], v[24:27]\n"
"v_mul_f32 v112, s44, v112\n"
"v_mul_f32 v113, s44, v113\n"
"v_cvt_pk_bf16_f32 v98, v112, v113\n"
"v_mfma_f32_16x16x32_bf16 v[24:27], a[80:83], v[72:75], v[24:27]\n"
"v_lshlrev_b32 v112, 16, v99\n"
"v_and_b32 v113, s45, v99\n"
"v_mul_f32 v112, s44, v112\n"
"v_mfma_f32_16x16x32_bf16 v[24:27], a[88:91], v[76:79], v[24:27]\n"
"v_mul_f32 v113, s44, v113\n"
"v_cvt_pk_bf16_f32 v99, v112, v113\n"
"v_mfma_f32_16x16x32_bf16 v[28:31], a[68:71], v[64:67], v[28:31]\n"
"s_waitcnt vmcnt(10)\n"
"v_lshlrev_b32 v112, 16, v100\n"
"v_and_b32 v113, s45, v100\n"
"v_mfma_f32_16x16x32_bf16 v[28:31], a[76:79], v[68:71], v[28:31]\n"
"v_mul_f32 v112, s44, v112\n"
"v_mul_f32 v113, s44, v113\n"
"v_mfma_f32_16x16x32_bf16 v[28:31], a[84:87], v[72:75], v[28:31]\n"
"v_cvt_pk_bf16_f32 v100, v112, v113\n"
"v_lshlrev_b32 v112, 16, v101\n"
"s_waitcnt lgkmcnt(0)\n"
"s_setprio 0\n"
"v_mfma_f32_16x16x32_bf16 v[28:31], a[92:95], v[76:79], v[28:31]\n"
"s_barrier\n"
"ds_read_b128 v[64:67], v124 offset:20480\n"
"ds_read_b128 v[68:71], v124 offset:21504\n"
"ds_read_b128 v[72:75], v124 offset:22528\n"
"ds_read_b128 v[76:79], v124 offset:23552\n"
"ds_read_b64_tr_b16 v[50:51], v125 offset:5120\n"
"ds_read_b64_tr_b16 v[54:55], v125 offset:5128\n"
"ds_write_b128 v126, v[96:99] offset:32768\n"
"s_xor_b32 s43, s62, 0xb000\n"
"s_add_u32 m0, s55, 0xc00\n"
"s_add_u32 s43, s43, s39\n"
"buffer_load_dwordx4 v121, s[28:31], s43 offen offset:0 lds\n"
"v_mfma_f32_16x16x32_bf16 v[24:27], a[96:99], v[80:83], v[24:27]\n"
"s_setprio 2\n"
"v_mfma_f32_16x16x32_bf16 v[24:27], a[104:107], v[84:87], v[24:27]\n"
"v_and_b32 v113, s45, v101\n"
"v_mul_f32 v112, s44, v112\n"
"v_mul_f32 v113, s44, v113\n"
"v_mfma_f32_16x16x32_bf16 v[24:27], a[112:115], v[88:91], v[24:27]\n"
"v_cvt_pk_bf16_f32 v101, v112, v113\n"
"v_lshlrev_b32 v112, 16, v102\n"
"v_and_b32 v113, s45, v102\n"
"v_mfma_f32_16x16x32_bf16 v[24:27], a[120:123], v[92:95], v[24:27]\n"
"v_mul_f32 v112, s44, v112\n"
"v_mul_f32 v113, s44, v113\n"
"v_mfma_f32_16x16x32_bf16 v[28:31], a[100:103], v[80:83], v[28:31]\n"
"v_cvt_pk_bf16_f32 v102, v112, v113\n"
"v_lshlrev_b32 v112, 16, v103\n"
"v_mfma_f32_16x16x32_bf16 v[28:31], a[108:111], v[84:87], v[28:31]\n"
"v_and_b32 v113, s45, v103\n"
"v_mul_f32 v112, s44, v112\n"
"v_mfma_f32_16x16x32_bf16 v[28:31], a[116:119], v[88:91], v[28:31]\n"
"v_mul_f32 v113, s44, v113\n"
"v_cvt_pk_bf16_f32 v103, v112, v113\n"
"v_mfma_f32_16x16x32_bf16 v[28:31], a[124:127], v[92:95], v[28:31]\n"
"s_setprio 0\n"
"ds_read_b128 v[80:83], v124 offset:24576\n"
"ds_read_b128 v[84:87], v124 offset:25600\n"
"ds_read_b128 v[88:91], v124 offset:26624\n"
"ds_read_b128 v[92:95], v124 offset:27648\n"
"ds_read_b128 v[114:117], v124 offset:46080\n"
"ds_write_b128 v126, v[100:103] offset:36864\n"
"s_add_u32 s40, s51, 0x2f000\n"
"buffer_load_dwordx4 v[96:99], v124, s[20:23], s40 offen nt\n"
"s_waitcnt lgkmcnt(9)\n"
"v_mfma_f32_16x16x32_bf16 v[16:19], a[0:3], v[64:67], 0\n"
"s_setprio 2\n"
"v_mfma_f32_16x16x32_bf16 v[16:19], a[8:11], v[68:71], v[16:19]\n"
"s_waitcnt lgkmcnt(1)\n"
"v_add_f32 v114, v114, v115\n"
"v_add_f32 v116, v116, v117\n"
"v_add_f32 v114, v114, v116\n"
"v_mfma_f32_16x16x32_bf16 v[16:19], a[16:19], v[72:75], v[16:19]\n"
"v_rcp_f32 v119, v114\n"
"v_subrev_u32 v118, s46, v114\n"
"v_max_u32 v123, v123, v118\n"
"v_mfma_f32_16x16x32_bf16 v[16:19], a[24:27], v[76:79], v[16:19]\n"
"v_mul_f32 v24, v119, v24\n"
"v_mul_f32 v25, v119, v25\n"
"v_mfma_f32_16x16x32_bf16 v[20:23], a[4:7], v[64:67], 0\n"
"v_mul_f32 v26, v119, v26\n"
"v_mul_f32 v27, v119, v27\n"
"v_mfma_f32_16x16x32_bf16 v[20:23], a[12:15], v[68:71], v[20:23]\n"
"v_mul_f32 v28, v119, v28\n"
"v_mul_f32 v29, v119, v29\n"
"v_mfma_f32_16x16x32_bf16 v[20:23], a[20:23], v[72:75], v[20:23]\n"
"v_mul_f32 v30, v119, v30\n"
"v_mul_f32 v31, v119, v31\n"
"v_mfma_f32_16x16x32_bf16 v[20:23], a[28:31], v[76:79], v[20:23]\n"
"s_setprio 0\n"
"ds_read_b128 v[64:67], v124 offset:28672\n"
"ds_read_b128 v[68:71], v124 offset:29696\n"
"ds_read_b128 v[72:75], v124 offset:30720\n"
"ds_read_b128 v[76:79], v124 offset:31744\n"
"ds_read_b64_tr_b16 v[56:57], v125 offset:6144\n"
"ds_read_b64_tr_b16 v[60:61], v125 offset:6152\n"
"s_add_u32 s40, s51, 0x30000\n"
"buffer_load_dwordx4 v[100:103], v124, s[20:23], s40 offen nt\n"
"v_mfma_f32_16x16x32_bf16 v[16:19], a[32:35], v[80:83], v[16:19]\n"
"s_setprio 2\n"
"v_mfma_f32_16x16x32_bf16 v[16:19], a[40:43], v[84:87], v[16:19]\n"
"v_fmac_f32 v122, v24, v25\n"
"v_fmac_f32 v122, v26, v27\n"
"v_fmac_f32 v122, v28, v29\n"
"v_mfma_f32_16x16x32_bf16 v[16:19], a[48:51], v[88:91], v[16:19]\n"
"v_fmac_f32 v122, v30, v31\n"
"v_cvt_pk_bf16_f32 v24, v24, v25\n"
"v_cvt_pk_bf16_f32 v25, v26, v27\n"
"v_mfma_f32_16x16x32_bf16 v[16:19], a[56:59], v[92:95], v[16:19]\n"
"v_cvt_pk_bf16_f32 v26, v28, v29\n"
"v_cvt_pk_bf16_f32 v27, v30, v31\n"
"v_mfma_f32_16x16x32_bf16 v[20:23], a[36:39], v[80:83], v[20:23]\n"
"s_waitcnt vmcnt(9)\n"
"v_lshlrev_b32 v112, 16, v104\n"
"v_and_b32 v113, s45, v104\n"
"v_mfma_f32_16x16x32_bf16 v[20:23], a[44:47], v[84:87], v[20:23]\n"
"v_mul_f32 v112, s44, v112\n"
"v_mul_f32 v113, s44, v113\n"
"v_mfma_f32_16x16x32_bf16 v[20:23], a[52:55], v[88:91], v[20:23]\n"
"v_cvt_pk_bf16_f32 v104, v112, v113\n"
"v_lshlrev_b32 v112, 16, v105\n"
"v_mfma_f32_16x16x32_bf16 v[20:23], a[60:63], v[92:95], v[20:23]\n"
"s_setprio 0\n"
"ds_read_b128 v[80:83], v124 offset:32768\n"
"ds_read_b128 v[84:87], v124 offset:33792\n"
"ds_read_b128 v[88:91], v124 offset:34816\n"
"ds_read_b128 v[92:95], v124 offset:35840\n"
"s_add_u32 s41, s52, 0x10000\n"
"buffer_store_dwordx4 v[24:27], v127, s[64:67], s41 offen sc1\n"
"s_xor_b32 s43, s62, 0xc000\n"
"s_add_u32 m0, s55, 0x1000\n"
"s_add_u32 s43, s43, s39\n"
"buffer_load_dwordx4 v121, s[28:31], s43 offen offset:0 lds\n"
"s_waitcnt lgkmcnt(6)\n"
"v_mfma_f32_16x16x32_bf16 v[16:19], a[64:67], v[64:67], v[16:19]\n"
"s_setprio 2\n"
"v_mfma_f32_16x16x32_bf16 v[16:19], a[72:75], v[68:71], v[16:19]\n"
"v_and_b32 v113, s45, v105\n"
"v_mul_f32 v112, s44, v112\n"
"v_mul_f32 v113, s44, v113\n"
"v_mfma_f32_16x16x32_bf16 v[16:19], a[80:83], v[72:75], v[16:19]\n"
"v_cvt_pk_bf16_f32 v105, v112, v113\n"
"v_lshlrev_b32 v112, 16, v106\n"
"v_and_b32 v113, s45, v106\n"
"v_mfma_f32_16x16x32_bf16 v[16:19], a[88:91], v[76:79], v[16:19]\n"
"v_mul_f32 v112, s44, v112\n"
"v_mul_f32 v113, s44, v113\n"
"v_mfma_f32_16x16x32_bf16 v[20:23], a[68:71], v[64:67], v[20:23]\n"
"v_cvt_pk_bf16_f32 v106, v112, v113\n"
"v_lshlrev_b32 v112, 16, v107\n"
"v_mfma_f32_16x16x32_bf16 v[20:23], a[76:79], v[68:71], v[20:23]\n"
"v_and_b32 v113, s45, v107\n"
"v_mul_f32 v112, s44, v112\n"
"v_mfma_f32_16x16x32_bf16 v[20:23], a[84:87], v[72:75], v[20:23]\n"
"v_mul_f32 v113, s44, v113\n"
"v_cvt_pk_bf16_f32 v107, v112, v113\n"
"s_waitcnt lgkmcnt(0)\n"
"s_setprio 0\n"
"v_mfma_f32_16x16x32_bf16 v[20:23], a[92:95], v[76:79], v[20:23]\n"
"s_barrier\n"
"ds_read_b128 v[64:67], v124 offset:0\n"
"ds_read_b128 v[68:71], v124 offset:1024\n"
"ds_read_b128 v[72:75], v124 offset:2048\n"
"ds_read_b128 v[76:79], v124 offset:3072\n"
"ds_read_b64_tr_b16 v[58:59], v125 offset:7168\n"
"ds_read_b64_tr_b16 v[62:63], v125 offset:7176\n"
"ds_write_b128 v126, v[104:107] offset:40960\n"
"v_mfma_f32_16x16x32_bf16 v[16:19], a[96:99], v[80:83], v[16:19]\n"
"s_setprio 2\n"
"v_mfma_f32_16x16x32_bf16 v[16:19], a[104:107], v[84:87], v[16:19]\n"
"v_mul_f32 v0, v120, v0\n"
"v_mul_f32 v1, v120, v1\n"
"v_mul_f32 v2, v120, v2\n"
"v_mfma_f32_16x16x32_bf16 v[16:19], a[112:115], v[88:91], v[16:19]\n"
"v_mul_f32 v3, v120, v3\n"
"v_mul_f32 v4, v120, v4\n"
"v_mul_f32 v5, v120, v5\n"
"v_mfma_f32_16x16x32_bf16 v[16:19], a[120:123], v[92:95], v[16:19]\n"
"v_mul_f32 v6, v120, v6\n"
"v_mul_f32 v7, v120, v7\n"
"v_mfma_f32_16x16x32_bf16 v[20:23], a[100:103], v[80:83], v[20:23]\n"
"v_fmac_f32 v122, v0, v1\n"
"v_fmac_f32 v122, v2, v3\n"
"v_mfma_f32_16x16x32_bf16 v[20:23], a[108:111], v[84:87], v[20:23]\n"
"v_fmac_f32 v122, v4, v5\n"
"v_fmac_f32 v122, v6, v7\n"
"v_mfma_f32_16x16x32_bf16 v[20:23], a[116:119], v[88:91], v[20:23]\n"
"v_cvt_pk_bf16_f32 v0, v0, v1\n"
"v_cvt_pk_bf16_f32 v1, v2, v3\n"
"v_mfma_f32_16x16x32_bf16 v[20:23], a[124:127], v[92:95], v[20:23]\n"
"s_setprio 0\n"
"ds_read_b128 v[80:83], v124 offset:4096\n"
"ds_read_b128 v[84:87], v124 offset:5120\n"
"ds_read_b128 v[88:91], v124 offset:6144\n"
"ds_read_b128 v[92:95], v124 offset:7168\n"
"ds_read_b128 v[114:117], v124 offset:47104\n"
"s_add_u32 s40, s51, 0x31000\n"
"buffer_load_dwordx4 v[104:107], v124, s[20:23], s40 offen nt\n"
"s_waitcnt lgkmcnt(8)\n"
"v_mfma_f32_16x16x32_bf16 v[24:27], a[0:3], v[64:67], 0\n"
"s_setprio 2\n"
"v_mfma_f32_16x16x32_bf16 v[24:27], a[8:11], v[68:71], v[24:27]\n"
"s_waitcnt lgkmcnt(0)\n"
"v_add_f32 v114, v114, v115\n"
"v_add_f32 v116, v116, v117\n"
"v_add_f32 v114, v114, v116\n"
"v_mfma_f32_16x16x32_bf16 v[24:27], a[16:19], v[72:75], v[24:27]\n"
"v_rcp_f32 v119, v114\n"
"v_subrev_u32 v118, s46, v114\n"
"v_max_u32 v123, v123, v118\n"
"v_mfma_f32_16x16x32_bf16 v[24:27], a[24:27], v[76:79], v[24:27]\n"
"v_mul_f32 v16, v119, v16\n"
"v_mul_f32 v17, v119, v17\n"
"v_mfma_f32_16x16x32_bf16 v[28:31], a[4:7], v[64:67], 0\n"
"v_mul_f32 v18, v119, v18\n"
"v_mul_f32 v19, v119, v19\n"
"v_mfma_f32_16x16x32_bf16 v[28:31], a[12:15], v[68:71], v[28:31]\n"
"v_mul_f32 v20, v119, v20\n"
"v_mul_f32 v21, v119, v21\n"
"v_mfma_f32_16x16x32_bf16 v[28:31], a[20:23], v[72:75], v[28:31]\n"
"v_mul_f32 v22, v119, v22\n"
"v_mul_f32 v23, v119, v23\n"
"v_mfma_f32_16x16x32_bf16 v[28:31], a[28:31], v[76:79], v[28:31]\n"
"s_setprio 0\n"
"ds_read_b128 v[64:67], v124 offset:8192\n"
"ds_read_b128 v[68:71], v124 offset:9216\n"
"ds_read_b128 v[72:75], v124 offset:10240\n"
"ds_read_b128 v[76:79], v124 offset:11264\n"
"s_xor_b32 s43, s62, 0xd000\n"
"s_add_u32 m0, s55, 0x1400\n"
"s_add_u32 s43, s43, s39\n"
"buffer_load_dwordx4 v121, s[28:31], s43 offen offset:0 lds\n"
"v_mfma_f32_16x16x32_bf16 v[24:27], a[32:35], v[80:83], v[24:27]\n"
"s_setprio 2\n"
"v_mfma_f32_16x16x32_bf16 v[24:27], a[40:43], v[84:87], v[24:27]\n"
"v_fmac_f32 v122, v16, v17\n"
"v_fmac_f32 v122, v18, v19\n"
"v_fmac_f32 v122, v20, v21\n"
"v_mfma_f32_16x16x32_bf16 v[24:27], a[48:51], v[88:91], v[24:27]\n"
"v_fmac_f32 v122, v22, v23\n"
"v_cvt_pk_bf16_f32 v16, v16, v17\n"
"v_cvt_pk_bf16_f32 v17, v18, v19\n"
"v_mfma_f32_16x16x32_bf16 v[24:27], a[56:59], v[92:95], v[24:27]\n"
"v_cvt_pk_bf16_f32 v18, v20, v21\n"
"v_cvt_pk_bf16_f32 v19, v22, v23\n"
"v_mfma_f32_16x16x32_bf16 v[28:31], a[36:39], v[80:83], v[28:31]\n"
"s_waitcnt vmcnt(10)\n"
"v_lshlrev_b32 v112, 16, v108\n"
"v_and_b32 v113, s45, v108\n"
"v_mfma_f32_16x16x32_bf16 v[28:31], a[44:47], v[84:87], v[28:31]\n"
"v_mul_f32 v112, s44, v112\n"
"v_mul_f32 v113, s44, v113\n"
"v_mfma_f32_16x16x32_bf16 v[28:31], a[52:55], v[88:91], v[28:31]\n"
"v_cvt_pk_bf16_f32 v108, v112, v113\n"
"v_lshlrev_b32 v112, 16, v109\n"
"v_mfma_f32_16x16x32_bf16 v[28:31], a[60:63], v[92:95], v[28:31]\n"
"s_setprio 0\n"
"ds_read_b128 v[80:83], v124 offset:12288\n"
"ds_read_b128 v[84:87], v124 offset:13312\n"
"ds_read_b128 v[88:91], v124 offset:14336\n"
"ds_read_b128 v[92:95], v124 offset:15360\n"
"s_add_u32 s41, s52, 0x11000\n"
"buffer_store_dwordx4 v[16:19], v127, s[64:67], s41 offen sc1\n"
"s_waitcnt lgkmcnt(4)\n"
"v_mfma_f32_16x16x32_bf16 v[24:27], a[64:67], v[64:67], v[24:27]\n"
"s_setprio 2\n"
"v_mfma_f32_16x16x32_bf16 v[24:27], a[72:75], v[68:71], v[24:27]\n"
"v_and_b32 v113, s45, v109\n"
"v_mul_f32 v112, s44, v112\n"
"v_mul_f32 v113, s44, v113\n"
"v_mfma_f32_16x16x32_bf16 v[24:27], a[80:83], v[72:75], v[24:27]\n"
"v_cvt_pk_bf16_f32 v109, v112, v113\n"
"v_lshlrev_b32 v112, 16, v110\n"
"v_and_b32 v113, s45, v110\n"
"v_mfma_f32_16x16x32_bf16 v[24:27], a[88:91], v[76:79], v[24:27]\n"
"v_mul_f32 v112, s44, v112\n"
"v_mul_f32 v113, s44, v113\n"
"v_mfma_f32_16x16x32_bf16 v[28:31], a[68:71], v[64:67], v[28:31]\n"
"v_cvt_pk_bf16_f32 v110, v112, v113\n"
"v_lshlrev_b32 v112, 16, v111\n"
"v_mfma_f32_16x16x32_bf16 v[28:31], a[76:79], v[68:71], v[28:31]\n"
"v_and_b32 v113, s45, v111\n"
"v_mul_f32 v112, s44, v112\n"
"v_mfma_f32_16x16x32_bf16 v[28:31], a[84:87], v[72:75], v[28:31]\n"
"v_mul_f32 v113, s44, v113\n"
"v_cvt_pk_bf16_f32 v111, v112, v113\n"
"s_waitcnt lgkmcnt(0)\n"
"s_setprio 0\n"
"v_mfma_f32_16x16x32_bf16 v[28:31], a[92:95], v[76:79], v[28:31]\n"
"s_barrier\n"
"ds_read_b128 v[64:67], v124 offset:16384\n"
"ds_read_b128 v[68:71], v124 offset:17408\n"
"ds_read_b128 v[72:75], v124 offset:18432\n"
"ds_read_b128 v[76:79], v124 offset:19456\n"
"ds_write_b128 v126, v[108:111] offset:0\n"
"v_mfma_f32_16x16x32_bf16 v[24:27], a[96:99], v[80:83], v[24:27]\n"
"s_setprio 2\n"
"v_mfma_f32_16x16x32_bf16 v[24:27], a[104:107], v[84:87], v[24:27]\n"
"v_cvt_pk_bf16_f32 v2, v4, v5\n"
"v_mfma_f32_16x16x32_bf16 v[24:27], a[112:115], v[88:91], v[24:27]\n"
"v_cvt_pk_bf16_f32 v3, v6, v7\n"
"v_mfma_f32_16x16x32_bf16 v[24:27], a[120:123], v[92:95], v[24:27]\n"
"v_mfma_f32_16x16x32_bf16 v[28:31], a[100:103], v[80:83], v[28:31]\n"
"v_mfma_f32_16x16x32_bf16 v[28:31], a[108:111], v[84:87], v[28:31]\n"
"v_mfma_f32_16x16x32_bf16 v[28:31], a[116:119], v[88:91], v[28:31]\n"
"v_mfma_f32_16x16x32_bf16 v[28:31], a[124:127], v[92:95], v[28:31]\n"
"s_setprio 0\n"
"ds_read_b128 v[80:83], v124 offset:20480\n"
"ds_read_b128 v[84:87], v124 offset:21504\n"
"ds_read_b128 v[88:91], v124 offset:22528\n"
"ds_read_b128 v[92:95], v124 offset:23552\n"
"ds_read_b128 v[114:117], v124 offset:45056\n"
"s_add_u32 s41, s52, 0\n"
"buffer_store_dwordx4 v[0:3], v127, s[64:67], s41 offen sc1\n"
"s_add_u32 s40, s51, 0x32000\n"
"buffer_load_dwordx4 v[108:111], v124, s[20:23], s40 offen nt\n"
"s_xor_b32 s43, s62, 0xe000\n"
"s_add_u32 m0, s55, 0x1800\n"
"s_add_u32 s43, s43, s39\n"
"buffer_load_dwordx4 v121, s[28:31], s43 offen offset:0 lds\n"
"s_waitcnt lgkmcnt(6)\n"
"v_mfma_f32_16x16x32_bf16 v[16:19], a[0:3], v[64:67], 0\n"
"s_setprio 2\n"
"v_mfma_f32_16x16x32_bf16 v[16:19], a[8:11], v[68:71], v[16:19]\n"
"s_waitcnt lgkmcnt(0)\n"
"v_add_f32 v114, v114, v115\n"
"v_add_f32 v116, v116, v117\n"
"v_add_f32 v114, v114, v116\n"
"v_mfma_f32_16x16x32_bf16 v[16:19], a[16:19], v[72:75], v[16:19]\n"
"v_rcp_f32 v119, v114\n"
"v_subrev_u32 v118, s46, v114\n"
"v_max_u32 v123, v123, v118\n"
"v_mfma_f32_16x16x32_bf16 v[16:19], a[24:27], v[76:79], v[16:19]\n"
"v_mul_f32 v24, v119, v24\n"
"v_mul_f32 v25, v119, v25\n"
"v_mfma_f32_16x16x32_bf16 v[20:23], a[4:7], v[64:67], 0\n"
"v_mul_f32 v26, v119, v26\n"
"v_mul_f32 v27, v119, v27\n"
"v_mfma_f32_16x16x32_bf16 v[20:23], a[12:15], v[68:71], v[20:23]\n"
"v_mul_f32 v28, v119, v28\n"
"v_mul_f32 v29, v119, v29\n"
"v_mfma_f32_16x16x32_bf16 v[20:23], a[20:23], v[72:75], v[20:23]\n"
"v_mul_f32 v30, v119, v30\n"
"v_mul_f32 v31, v119, v31\n"
"v_mfma_f32_16x16x32_bf16 v[20:23], a[28:31], v[76:79], v[20:23]\n"
"s_setprio 0\n"
"ds_read_b128 v[64:67], v124 offset:24576\n"
"ds_read_b128 v[68:71], v124 offset:25600\n"
"ds_read_b128 v[72:75], v124 offset:26624\n"
"ds_read_b128 v[76:79], v124 offset:27648\n"
"ds_read_b128 v[114:117], v124 offset:37888\n"
"v_mfma_f32_16x16x32_bf16 v[16:19], a[32:35], v[80:83], v[16:19]\n"
"s_setprio 2\n"
"v_mfma_f32_16x16x32_bf16 v[16:19], a[40:43], v[84:87], v[16:19]\n"
"s_waitcnt lgkmcnt(0)\n"
"v_add_f32 v114, v114, v115\n"
"v_add_f32 v116, v116, v117\n"
"v_add_f32 v114, v114, v116\n"
"v_mfma_f32_16x16x32_bf16 v[16:19], a[48:51], v[88:91], v[16:19]\n"
"v_rcp_f32 v120, v114\n"
"v_subrev_u32 v118, s46, v114\n"
"v_max_u32 v123, v123, v118\n"
"v_mfma_f32_16x16x32_bf16 v[16:19], a[56:59], v[92:95], v[16:19]\n"
"v_fmac_f32 v122, v24, v25\n"
"v_fmac_f32 v122, v26, v27\n"
"v_mfma_f32_16x16x32_bf16 v[20:23], a[36:39], v[80:83], v[20:23]\n"
"v_fmac_f32 v122, v28, v29\n"
"v_fmac_f32 v122, v30, v31\n"
"v_mfma_f32_16x16x32_bf16 v[20:23], a[44:47], v[84:87], v[20:23]\n"
"v_cvt_pk_bf16_f32 v24, v24, v25\n"
"v_cvt_pk_bf16_f32 v25, v26, v27\n"
"v_mfma_f32_16x16x32_bf16 v[20:23], a[52:55], v[88:91], v[20:23]\n"
"v_cvt_pk_bf16_f32 v26, v28, v29\n"
"v_cvt_pk_bf16_f32 v27, v30, v31\n"
"v_mfma_f32_16x16x32_bf16 v[20:23], a[60:63], v[92:95], v[20:23]\n"
"s_setprio 0\n"
"ds_read_b128 v[80:83], v124 offset:28672\n"
"ds_read_b128 v[84:87], v124 offset:29696\n"
"ds_read_b128 v[88:91], v124 offset:30720\n"
"ds_read_b128 v[92:95], v124 offset:31744\n"
"s_add_u32 s41, s52, 0x12000\n"
"buffer_store_dwordx4 v[24:27], v127, s[64:67], s41 offen sc1\n"
"v_mfma_f32_16x16x32_bf16 v[16:19], a[64:67], v[64:67], v[16:19]\n"
"s_setprio 2\n"
"v_mfma_f32_16x16x32_bf16 v[16:19], a[72:75], v[68:71], v[16:19]\n"
"s_waitcnt vmcnt(10)\n"
"v_lshlrev_b32 v112, 16, v96\n"
"v_and_b32 v113, s45, v96\n"
"v_mul_f32 v112, s44, v112\n"
"v_mfma_f32_16x16x32_bf16 v[16:19], a[80:83], v[72:75], v[16:19]\n"
"v_mul_f32 v113, s44, v113\n"
"v_cvt_pk_bf16_f32 v96, v112, v113\n"
"v_lshlrev_b32 v112, 16, v97\n"
"v_mfma_f32_16x16x32_bf16 v[16:19], a[88:91], v[76:79], v[16:19]\n"
"v_and_b32 v113, s45, v97\n"
"v_mul_f32 v112, s44, v112\n"
"v_mfma_f32_16x16x32_bf16 v[20:23], a[68:71], v[64:67], v[20:23]\n"
"v_mul_f32 v113, s44, v113\n"
"v_cvt_pk_bf16_f32 v97, v112, v113\n"
"v_mfma_f32_16x16x32_bf16 v[20:23], a[76:79], v[68:71], v[20:23]\n"
"v_lshlrev_b32 v112, 16, v98\n"
"v_and_b32 v113, s45, v98\n"
"v_mfma_f32_16x16x32_bf16 v[20:23], a[84:87], v[72:75], v[20:23]\n"
"v_mul_f32 v112, s44, v112\n"
"v_mul_f32 v113, s44, v113\n"
"s_waitcnt lgkmcnt(0)\n"
"s_setprio 0\n"
"v_mfma_f32_16x16x32_bf16 v[20:23], a[92:95], v[76:79], v[20:23]\n"
"s_barrier\n"
"ds_read_b128 v[64:67], v124 offset:32768\n"
"ds_read_b128 v[68:71], v124 offset:33792\n"
"ds_read_b128 v[72:75], v124 offset:34816\n"
"ds_read_b128 v[76:79], v124 offset:35840\n"
"s_xor_b32 s43, s62, 0xf000\n"
"s_add_u32 m0, s55, 0x1c00\n"
"s_add_u32 s43, s43, s39\n"
"buffer_load_dwordx4 v121, s[28:31], s43 offen offset:0 lds\n"
"v_mfma_f32_16x16x32_bf16 v[16:19], a[96:99], v[80:83], v[16:19]\n"
"s_setprio 2\n"
"v_mfma_f32_16x16x32_bf16 v[16:19], a[104:107], v[84:87], v[16:19]\n"
"v_cvt_pk_bf16_f32 v98, v112, v113\n"
"v_lshlrev_b32 v112, 16, v99\n"
"v_and_b32 v113, s45, v99\n"
"v_mfma_f32_16x16x32_bf16 v[16:19], a[112:115], v[88:91], v[16:19]\n"
"v_mul_f32 v112, s44, v112\n"
"v_mul_f32 v113, s44, v113\n"
"v_cvt_pk_bf16_f32 v99, v112, v113\n"
"v_mfma_f32_16x16x32_bf16 v[16:19], a[120:123], v[92:95], v[16:19]\n"
"s_waitcnt vmcnt(10)\n"
"v_lshlrev_b32 v112, 16, v100\n"
"v_and_b32 v113, s45, v100\n"
"v_mfma_f32_16x16x32_bf16 v[20:23], a[100:103], v[80:83], v[20:23]\n"
"v_mul_f32 v112, s44, v112\n"
"v_mul_f32 v113, s44, v113\n"
"v_mfma_f32_16x16x32_bf16 v[20:23], a[108:111], v[84:87], v[20:23]\n"
"v_cvt_pk_bf16_f32 v100, v112, v113\n"
"v_lshlrev_b32 v112, 16, v101\n"
"v_mfma_f32_16x16x32_bf16 v[20:23], a[116:119], v[88:91], v[20:23]\n"
"v_and_b32 v113, s45, v101\n"
"v_mul_f32 v112, s44, v112\n"
"v_mfma_f32_16x16x32_bf16 v[20:23], a[124:127], v[92:95], v[20:23]\n"
"s_setprio 0\n"
"ds_read_b128 v[80:83], v124 offset:0\n"
"ds_read_b128 v[84:87], v124 offset:1024\n"
"ds_read_b128 v[88:91], v124 offset:2048\n"
"ds_read_b128 v[92:95], v124 offset:3072\n"
"ds_read_b128 v[114:117], v124 offset:46080\n"
"ds_write_b128 v126, v[96:99] offset:4096\n"
"s_waitcnt lgkmcnt(6)\n"
"v_mfma_f32_16x16x32_bf16 v[24:27], a[0:3], v[64:67], 0\n"
"s_setprio 2\n"
"v_mfma_f32_16x16x32_bf16 v[24:27], a[8:11], v[68:71], v[24:27]\n"
"s_waitcnt lgkmcnt(1)\n"
"v_add_f32 v114, v114, v115\n"
"v_add_f32 v116, v116, v117\n"
"v_add_f32 v114, v114, v116\n"
"v_mfma_f32_16x16x32_bf16 v[24:27], a[16:19], v[72:75], v[24:27]\n"
"v_rcp_f32 v119, v114\n"
"v_subrev_u32 v118, s46, v114\n"
"v_max_u32 v123, v123, v118\n"
"v_mfma_f32_16x16x32_bf16 v[24:27], a[24:27], v[76:79], v[24:27]\n"
"v_mul_f32 v16, v119, v16\n"
"v_mul_f32 v17, v119, v17\n"
"v_mfma_f32_16x16x32_bf16 v[28:31], a[4:7], v[64:67], 0\n"
"v_mul_f32 v18, v119, v18\n"
"v_mul_f32 v19, v119, v19\n"
"v_mfma_f32_16x16x32_bf16 v[28:31], a[12:15], v[68:71], v[28:31]\n"
"v_mul_f32 v20, v119, v20\n"
"v_mul_f32 v21, v119, v21\n"
"v_mfma_f32_16x16x32_bf16 v[28:31], a[20:23], v[72:75], v[28:31]\n"
"v_mul_f32 v22, v119, v22\n"
"v_mul_f32 v23, v119, v23\n"
"v_mfma_f32_16x16x32_bf16 v[28:31], a[28:31], v[76:79], v[28:31]\n"
"s_setprio 0\n"
"ds_read_b128 v[64:67], v124 offset:4096\n"
"ds_read_b128 v[68:71], v124 offset:5120\n"
"ds_read_b128 v[72:75], v124 offset:6144\n"
"ds_read_b128 v[76:79], v124 offset:7168\n"
"s_add_u32 s40, s51, 0x33000\n"
"buffer_load_dwordx4 v[96:99], v124, s[20:23], s40 offen nt\n"
"v_mfma_f32_16x16x32_bf16 v[24:27], a[32:35], v[80:83], v[24:27]\n"
"s_setprio 2\n"
"v_mfma_f32_16x16x32_bf16 v[24:27], a[40:43], v[84:87], v[24:27]\n"
"v_fmac_f32 v122, v16, v17\n"
"v_fmac_f32 v122, v18, v19\n"
"v_fmac_f32 v122, v20, v21\n"
"v_mfma_f32_16x16x32_bf16 v[24:27], a[48:51], v[88:91], v[24:27]\n"
"v_fmac_f32 v122, v22, v23\n"
"v_cvt_pk_bf16_f32 v16, v16, v17\n"
"v_cvt_pk_bf16_f32 v17, v18, v19\n"
"v_mfma_f32_16x16x32_bf16 v[24:27], a[56:59], v[92:95], v[24:27]\n"
"v_cvt_pk_bf16_f32 v18, v20, v21\n"
"v_cvt_pk_bf16_f32 v19, v22, v23\n"
"v_mfma_f32_16x16x32_bf16 v[28:31], a[36:39], v[80:83], v[28:31]\n"
"v_mul_f32 v113, s44, v113\n"
"v_cvt_pk_bf16_f32 v101, v112, v113\n"
"v_mfma_f32_16x16x32_bf16 v[28:31], a[44:47], v[84:87], v[28:31]\n"
"v_lshlrev_b32 v112, 16, v102\n"
"v_and_b32 v113, s45, v102\n"
"v_mfma_f32_16x16x32_bf16 v[28:31], a[52:55], v[88:91], v[28:31]\n"
"v_mul_f32 v112, s44, v112\n"
"v_mul_f32 v113, s44, v113\n"
"v_mfma_f32_16x16x32_bf16 v[28:31], a[60:63], v[92:95], v[28:31]\n"
"s_setprio 0\n"
"ds_read_b128 v[80:83], v124 offset:8192\n"
"ds_read_b128 v[84:87], v124 offset:9216\n"
"ds_read_b128 v[88:91], v124 offset:10240\n"
"ds_read_b128 v[92:95], v124 offset:11264\n"
"s_add_u32 s41, s52, 0x13000\n"
"buffer_store_dwordx4 v[16:19], v127, s[64:67], s41 offen sc1\n"
"s_waitcnt lgkmcnt(4)\n"
"v_mfma_f32_16x16x32_bf16 v[24:27], a[64:67], v[64:67], v[24:27]\n"
"s_setprio 2\n"
"v_mfma_f32_16x16x32_bf16 v[24:27], a[72:75], v[68:71], v[24:27]\n"
"v_cvt_pk_bf16_f32 v102, v112, v113\n"
"v_lshlrev_b32 v112, 16, v103\n"
"v_and_b32 v113, s45, v103\n"
"v_mfma_f32_16x16x32_bf16 v[24:27], a[80:83], v[72:75], v[24:27]\n"
"v_mul_f32 v112, s44, v112\n"
"v_mul_f32 v113, s44, v113\n"
"v_cvt_pk_bf16_f32 v103, v112, v113\n"
"v_mfma_f32_16x16x32_bf16 v[24:27], a[88:91], v[76:79], v[24:27]\n"
"s_waitcnt vmcnt(9)\n"
"v_lshlrev_b32 v112, 16, v104\n"
"v_and_b32 v113, s45, v104\n"
"v_mfma_f32_16x16x32_bf16 v[28:31], a[68:71], v[64:67], v[28:31]\n"
"v_mul_f32 v112, s44, v112\n"
"v_mul_f32 v113, s44, v113\n"
"v_mfma_f32_16x16x32_bf16 v[28:31], a[76:79], v[68:71], v[28:31]\n"
"v_cvt_pk_bf16_f32 v104, v112, v113\n"
"v_lshlrev_b32 v112, 16, v105\n"
"v_mfma_f32_16x16x32_bf16 v[28:31], a[84:87], v[72:75], v[28:31]\n"
"v_and_b32 v113, s45, v105\n"
"v_mul_f32 v112, s44, v112\n"
"s_waitcnt lgkmcnt(0)\n"
"s_setprio 0\n"
"v_mfma_f32_16x16x32_bf16 v[28:31], a[92:95], v[76:79], v[28:31]\n"
"s_barrier\n"
"ds_read_b128 v[64:67], v124 offset:12288\n"
"ds_read_b128 v[68:71], v124 offset:13312\n"
"ds_read_b128 v[72:75], v124 offset:14336\n"
"ds_read_b128 v[76:79], v124 offset:15360\n"
"ds_write_b128 v126, v[100:103] offset:8192\n"
"v_mfma_f32_16x16x32_bf16 v[24:27], a[96:99], v[80:83], v[24:27]\n"
"s_setprio 2\n"
"v_mfma_f32_16x16x32_bf16 v[24:27], a[104:107], v[84:87], v[24:27]\n"
"v_mul_f32 v113, s44, v113\n"
"v_cvt_pk_bf16_f32 v105, v112, v113\n"
"v_lshlrev_b32 v112, 16, v106\n"
"v_mfma_f32_16x16x32_bf16 v[24:27], a[112:115], v[88:91], v[24:27]\n"
"v_and_b32 v113, s45, v106\n"
"v_mul_f32 v112, s44, v112\n"
"v_mul_f32 v113, s44, v113\n"
"v_mfma_f32_16x16x32_bf16 v[24:27], a[120:123], v[92:95], v[24:27]\n"
"v_cvt_pk_bf16_f32 v106, v112, v113\n"
"v_lshlrev_b32 v112, 16, v107\n"
"v_mfma_f32_16x16x32_bf16 v[28:31], a[100:103], v[80:83], v[28:31]\n"
"v_and_b32 v113, s45, v107\n"
"v_mul_f32 v112, s44, v112\n"
"v_mfma_f32_16x16x32_bf16 v[28:31], a[108:111], v[84:87], v[28:31]\n"
"v_mul_f32 v113, s44, v113\n"
"v_cvt_pk_bf16_f32 v107, v112, v113\n"
"v_mfma_f32_16x16x32_bf16 v[28:31], a[116:119], v[88:91], v[28:31]\n"
"v_mul_f32 v8, v120, v8\n"
"v_mul_f32 v9, v120, v9\n"
"v_mfma_f32_16x16x32_bf16 v[28:31], a[124:127], v[92:95], v[28:31]\n"
"s_setprio 0\n"
"ds_read_b128 v[80:83], v124 offset:16384\n"
"ds_read_b128 v[84:87], v124 offset:17408\n"
"ds_read_b128 v[88:91], v124 offset:18432\n"
"ds_read_b128 v[92:95], v124 offset:19456\n"
"ds_read_b128 v[114:117], v124 offset:47104\n"
"ds_write_b128 v126, v[104:107] offset:12288\n"
"s_add_u32 s40, s51, 0x34000\n"
"buffer_load_dwordx4 v[100:103], v124, s[20:23], s40 offen nt\n"
"s_waitcnt lgkmcnt(7)\n"
"v_mfma_f32_16x16x32_bf16 v[16:19], a[0:3], v[64:67], 0\n"
"s_setprio 2\n"
"v_mfma_f32_16x16x32_bf16 v[16:19], a[8:11], v[68:71], v[16:19]\n"
"s_waitcnt lgkmcnt(1)\n"
"v_add_f32 v114, v114, v115\n"
"v_add_f32 v116, v116, v117\n"
"v_add_f32 v114, v114, v116\n"
"v_mfma_f32_16x16x32_bf16 v[16:19], a[16:19], v[72:75], v[16:19]\n"
"v_rcp_f32 v119, v114\n"
"v_subrev_u32 v118, s46, v114\n"
"v_max_u32 v123, v123, v118\n"
"v_mfma_f32_16x16x32_bf16 v[16:19], a[24:27], v[76:79], v[16:19]\n"
"v_mul_f32 v24, v119, v24\n"
"v_mul_f32 v25, v119, v25\n"
"v_mfma_f32_16x16x32_bf16 v[20:23], a[4:7], v[64:67], 0\n"
"v_mul_f32 v26, v119, v26\n"
"v_mul_f32 v27, v119, v27\n"
"v_mfma_f32_16x16x32_bf16 v[20:23], a[12:15], v[68:71], v[20:23]\n"
"v_mul_f32 v28, v119, v28\n"
"v_mul_f32 v29, v119, v29\n"
"v_mfma_f32_16x16x32_bf16 v[20:23], a[20:23], v[72:75], v[20:23]\n"
"v_mul_f32 v30, v119, v30\n"
"v_mul_f32 v31, v119, v31\n"
"v_mfma_f32_16x16x32_bf16 v[20:23], a[28:31], v[76:79], v[20:23]\n"
"s_setprio 0\n"
"ds_read_b128 v[64:67], v124 offset:20480\n"
"ds_read_b128 v[68:71], v124 offset:21504\n"
"ds_read_b128 v[72:75], v124 offset:22528\n"
"ds_read_b128 v[76:79], v124 offset:23552\n"
"s_add_u32 s40, s51, 0x35000\n"
"buffer_load_dwordx4 v[104:107], v124, s[20:23], s40 offen nt\n"
"v_mfma_f32_16x16x32_bf16 v[16:19], a[32:35], v[80:83], v[16:19]\n"
"s_setprio 2\n"
"v_mfma_f32_16x16x32_bf16 v[16:19], a[40:43], v[84:87], v[16:19]\n"
"v_fmac_f32 v122, v24, v25\n"
"v_fmac_f32 v122, v26, v27\n"
"v_fmac_f32 v122, v28, v29\n"
"v_mfma_f32_16x16x32_bf16 v[16:19], a[48:51], v[88:91], v[16:19]\n"
"v_fmac_f32 v122, v30, v31\n"
"v_cvt_pk_bf16_f32 v24, v24, v25\n"
"v_cvt_pk_bf16_f32 v25, v26, v27\n"
"v_mfma_f32_16x16x32_bf16 v[16:19], a[56:59], v[92:95], v[16:19]\n"
"v_cvt_pk_bf16_f32 v26, v28, v29\n"
"v_cvt_pk_bf16_f32 v27, v30, v31\n"
"v_mfma_f32_16x16x32_bf16 v[20:23], a[36:39], v[80:83], v[20:23]\n"
"s_waitcnt vmcnt(7)\n"
"v_lshlrev_b32 v112, 16, v108\n"
"v_and_b32 v113, s45, v108\n"
"v_mfma_f32_16x16x32_bf16 v[20:23], a[44:47], v[84:87], v[20:23]\n"
"v_mul_f32 v112, s44, v112\n"
"v_mul_f32 v113, s44, v113\n"
"v_mfma_f32_16x16x32_bf16 v[20:23], a[52:55], v[88:91], v[20:23]\n"
"v_cvt_pk_bf16_f32 v108, v112, v113\n"
"v_lshlrev_b32 v112, 16, v109\n"
"v_mfma_f32_16x16x32_bf16 v[20:23], a[60:63], v[92:95], v[20:23]\n"
"s_setprio 0\n"
"ds_read_b128 v[80:83], v124 offset:24576\n"
"ds_read_b128 v[84:87], v124 offset:25600\n"
"ds_read_b128 v[88:91], v124 offset:26624\n"
"ds_read_b128 v[92:95], v124 offset:27648\n"
"s_add_u32 s41, s52, 0x14000\n"
"buffer_store_dwordx4 v[24:27], v127, s[64:67], s41 offen sc1\n"
"s_waitcnt lgkmcnt(4)\n"
"v_mfma_f32_16x16x32_bf16 v[16:19], a[64:67], v[64:67], v[16:19]\n"
"s_setprio 2\n"
"v_mfma_f32_16x16x32_bf16 v[16:19], a[72:75], v[68:71], v[16:19]\n"
"v_and_b32 v113, s45, v109\n"
"v_mul_f32 v112, s44, v112\n"
"v_mul_f32 v113, s44, v113\n"
"v_mfma_f32_16x16x32_bf16 v[16:19], a[80:83], v[72:75], v[16:19]\n"
"v_cvt_pk_bf16_f32 v109, v112, v113\n"
"v_lshlrev_b32 v112, 16, v110\n"
"v_and_b32 v113, s45, v110\n"
"v_mfma_f32_16x16x32_bf16 v[16:19], a[88:91], v[76:79], v[16:19]\n"
"v_mul_f32 v112, s44, v112\n"
"v_mul_f32 v113, s44, v113\n"
"v_mfma_f32_16x16x32_bf16 v[20:23], a[68:71], v[64:67], v[20:23]\n"
"v_cvt_pk_bf16_f32 v110, v112, v113\n"
"v_lshlrev_b32 v112, 16, v111\n"
"v_mfma_f32_16x16x32_bf16 v[20:23], a[76:79], v[68:71], v[20:23]\n"
"v_and_b32 v113, s45, v111\n"
"v_mul_f32 v112, s44, v112\n"
"v_mfma_f32_16x16x32_bf16 v[20:23], a[84:87], v[72:75], v[20:23]\n"
"v_mul_f32 v113, s44, v113\n"
"v_cvt_pk_bf16_f32 v111, v112, v113\n"
"s_waitcnt lgkmcnt(0)\n"
"s_setprio 0\n"
"v_mfma_f32_16x16x32_bf16 v[20:23], a[92:95], v[76:79], v[20:23]\n"
"s_barrier\n"
"ds_read_b128 v[64:67], v124 offset:28672\n"
"ds_read_b128 v[68:71], v124 offset:29696\n"
"ds_read_b128 v[72:75], v124 offset:30720\n"
"ds_read_b128 v[76:79], v124 offset:31744\n"
"ds_write_b128 v126, v[108:111] offset:16384\n"
"v_mfma_f32_16x16x32_bf16 v[16:19], a[96:99], v[80:83], v[16:19]\n"
"s_setprio 2\n"
"v_mfma_f32_16x16x32_bf16 v[16:19], a[104:107], v[84:87], v[16:19]\n"
"s_waitcnt vmcnt(4)\n"
"v_lshlrev_b32 v112, 16, v96\n"
"v_and_b32 v113, s45, v96\n"
"v_mul_f32 v112, s44, v112\n"
"v_mfma_f32_16x16x32_bf16 v[16:19], a[112:115], v[88:91], v[16:19]\n"
"v_mul_f32 v113, s44, v113\n"
"v_cvt_pk_bf16_f32 v96, v112, v113\n"
"v_lshlrev_b32 v112, 16, v97\n"
"v_mfma_f32_16x16x32_bf16 v[16:19], a[120:123], v[92:95], v[16:19]\n"
"v_and_b32 v113, s45, v97\n"
"v_mul_f32 v112, s44, v112\n"
"v_mfma_f32_16x16x32_bf16 v[20:23], a[100:103], v[80:83], v[20:23]\n"
"v_mul_f32 v113, s44, v113\n"
"v_cvt_pk_bf16_f32 v97, v112, v113\n"
"v_mfma_f32_16x16x32_bf16 v[20:23], a[108:111], v[84:87], v[20:23]\n"
"v_lshlrev_b32 v112, 16, v98\n"
"v_and_b32 v113, s45, v98\n"
"v_mfma_f32_16x16x32_bf16 v[20:23], a[116:119], v[88:91], v[20:23]\n"
"v_mul_f32 v112, s44, v112\n"
"v_mul_f32 v113, s44, v113\n"
"v_mfma_f32_16x16x32_bf16 v[20:23], a[124:127], v[92:95], v[20:23]\n"
"s_setprio 0\n"
"ds_read_b128 v[80:83], v124 offset:32768\n"
"ds_read_b128 v[84:87], v124 offset:33792\n"
"ds_read_b128 v[88:91], v124 offset:34816\n"
"ds_read_b128 v[92:95], v124 offset:35840\n"
"ds_read_b128 v[114:117], v124 offset:45056\n"
"s_waitcnt lgkmcnt(6)\n"
"v_mfma_f32_16x16x32_bf16 v[24:27], a[0:3], v[64:67], 0\n"
"s_setprio 2\n"
"v_mfma_f32_16x16x32_bf16 v[24:27], a[8:11], v[68:71], v[24:27]\n"
"s_waitcnt lgkmcnt(0)\n"
"v_add_f32 v114, v114, v115\n"
"v_add_f32 v116, v116, v117\n"
"v_add_f32 v114, v114, v116\n"
"v_mfma_f32_16x16x32_bf16 v[24:27], a[16:19], v[72:75], v[24:27]\n"
"v_rcp_f32 v119, v114\n"
"v_subrev_u32 v118, s46, v114\n"
"v_max_u32 v123, v123, v118\n"
"v_mfma_f32_16x16x32_bf16 v[24:27], a[24:27], v[76:79], v[24:27]\n"
"v_cvt_pk_bf16_f32 v98, v112, v113\n"
"v_lshlrev_b32 v112, 16, v99\n"
"v_mfma_f32_16x16x32_bf16 v[28:31], a[4:7], v[64:67], 0\n"
"v_and_b32 v113, s45, v99\n"
"v_mul_f32 v112, s44, v112\n"
"v_mfma_f32_16x16x32_bf16 v[28:31], a[12:15], v[68:71], v[28:31]\n"
"v_mul_f32 v113, s44, v113\n"
"v_cvt_pk_bf16_f32 v99, v112, v113\n"
"v_mfma_f32_16x16x32_bf16 v[28:31], a[20:23], v[72:75], v[28:31]\n"
"v_mul_f32 v16, v119, v16\n"
"v_mul_f32 v17, v119, v17\n"
"v_mfma_f32_16x16x32_bf16 v[28:31], a[28:31], v[76:79], v[28:31]\n"
"s_setprio 0\n"
"ds_read_b128 v[64:67], v124 offset:0\n"
"ds_read_b128 v[68:71], v124 offset:1024\n"
"ds_read_b128 v[72:75], v124 offset:2048\n"
"ds_read_b128 v[76:79], v124 offset:3072\n"
"ds_write_b128 v126, v[96:99] offset:20480\n"
"v_mfma_f32_16x16x32_bf16 v[24:27], a[32:35], v[80:83], v[24:27]\n"
"s_setprio 2\n"
"v_mfma_f32_16x16x32_bf16 v[24:27], a[40:43], v[84:87], v[24:27]\n"
"v_mul_f32 v18, v119, v18\n"
"v_mul_f32 v19, v119, v19\n"
"v_mul_f32 v20, v119, v20\n"
"v_mfma_f32_16x16x32_bf16 v[24:27], a[48:51], v[88:91], v[24:27]\n"
"v_mul_f32 v21, v119, v21\n"
"v_mul_f32 v22, v119, v22\n"
"v_mul_f32 v23, v119, v23\n"
"v_mfma_f32_16x16x32_bf16 v[24:27], a[56:59], v[92:95], v[24:27]\n"
"v_fmac_f32 v122, v16, v17\n"
"v_fmac_f32 v122, v18, v19\n"
"v_mfma_f32_16x16x32_bf16 v[28:31], a[36:39], v[80:83], v[28:31]\n"
"v_fmac_f32 v122, v20, v21\n"
"v_fmac_f32 v122, v22, v23\n"
"v_mfma_f32_16x16x32_bf16 v[28:31], a[44:47], v[84:87], v[28:31]\n"
"v_cvt_pk_bf16_f32 v16, v16, v17\n"
"v_cvt_pk_bf16_f32 v17, v18, v19\n"
"v_mfma_f32_16x16x32_bf16 v[28:31], a[52:55], v[88:91], v[28:31]\n"
"v_cvt_pk_bf16_f32 v18, v20, v21\n"
"v_cvt_pk_bf16_f32 v19, v22, v23\n"
"v_mfma_f32_16x16x32_bf16 v[28:31], a[60:63], v[92:95], v[28:31]\n"
"s_setprio 0\n"
"ds_read_b128 v[80:83], v124 offset:4096\n"
"ds_read_b128 v[84:87], v124 offset:5120\n"
"ds_read_b128 v[88:91], v124 offset:6144\n"
"ds_read_b128 v[92:95], v124 offset:7168\n"
"ds_read_b64_tr_b16 a[32:33], v125 offset:0\n"
"ds_read_b64_tr_b16 a[36:37], v125 offset:8\n"
"ds_read_b64_tr_b16 a[34:35], v125 offset:1024\n"
"ds_read_b64_tr_b16 a[38:39], v125 offset:1032\n"
"s_add_u32 s41, s52, 0x15000\n"
"buffer_store_dwordx4 v[16:19], v127, s[64:67], s41 offen sc1\n"
"s_waitcnt lgkmcnt(9)\n"
"v_mfma_f32_16x16x32_bf16 v[24:27], a[64:67], v[64:67], v[24:27]\n"
"s_setprio 2\n"
"v_mfma_f32_16x16x32_bf16 v[24:27], a[72:75], v[68:71], v[24:27]\n"
"v_mul_f32 v10, v120, v10\n"
"v_mul_f32 v11, v120, v11\n"
"v_mul_f32 v12, v120, v12\n"
"v_mfma_f32_16x16x32_bf16 v[24:27], a[80:83], v[72:75], v[24:27]\n"
"v_mul_f32 v13, v120, v13\n"
"v_mul_f32 v14, v120, v14\n"
"v_mul_f32 v15, v120, v15\n"
"v_mfma_f32_16x16x32_bf16 v[24:27], a[88:91], v[76:79], v[24:27]\n"
"v_fmac_f32 v122, v8, v9\n"
"v_fmac_f32 v122, v10, v11\n"
"v_mfma_f32_16x16x32_bf16 v[28:31], a[68:71], v[64:67], v[28:31]\n"
"v_fmac_f32 v122, v12, v13\n"
"v_fmac_f32 v122, v14, v15\n"
"v_mfma_f32_16x16x32_bf16 v[28:31], a[76:79], v[68:71], v[28:31]\n"
"v_cvt_pk_bf16_f32 v8, v8, v9\n"
"v_cvt_pk_bf16_f32 v9, v10, v11\n"
"v_mfma_f32_16x16x32_bf16 v[28:31], a[84:87], v[72:75], v[28:31]\n"
"v_cvt_pk_bf16_f32 v10, v12, v13\n"
"v_cvt_pk_bf16_f32 v11, v14, v15\n"
"s_waitcnt lgkmcnt(0)\n"
"s_setprio 0\n"
"v_mfma_f32_16x16x32_bf16 v[28:31], a[92:95], v[76:79], v[28:31]\n"
"s_barrier\n"
"ds_read_b128 v[64:67], v124 offset:8192\n"
"ds_read_b128 v[68:71], v124 offset:9216\n"
"ds_read_b128 v[72:75], v124 offset:10240\n"
"ds_read_b128 v[76:79], v124 offset:11264\n"
"ds_read_b64_tr_b16 a[40:41], v125 offset:2048\n"
"ds_read_b64_tr_b16 a[44:45], v125 offset:2056\n"
"ds_read_b64_tr_b16 a[42:43], v125 offset:3072\n"
"ds_read_b64_tr_b16 a[46:47], v125 offset:3080\n"
"s_add_u32 s41, s52, 0x1000\n"
"buffer_store_dwordx4 v[8:11], v127, s[64:67], s41 offen sc1\n"
"s_xor_b32 s43, s62, 0x10000\n"
"s_add_u32 m0, s55, 0\n"
"s_add_u32 s43, s43, s39\n"
"buffer_load_dwordx4 v121, s[28:31], s43 offen offset:0 lds\n"
"s_xor_b32 s43, s62, 0x11000\n"
"s_add_u32 m0, s55, 0x400\n"
"s_add_u32 s43, s43, s39\n"
"buffer_load_dwordx4 v121, s[28:31], s43 offen offset:0 lds\n"
"v_mfma_f32_16x16x32_bf16 v[24:27], a[96:99], v[80:83], v[24:27]\n"
"s_setprio 2\n"
"v_mfma_f32_16x16x32_bf16 v[24:27], a[104:107], v[84:87], v[24:27]\n"
"s_waitcnt vmcnt(6)\n"
"v_lshlrev_b32 v112, 16, v100\n"
"v_and_b32 v113, s45, v100\n"
"v_mul_f32 v112, s44, v112\n"
"v_mfma_f32_16x16x32_bf16 v[24:27], a[112:115], v[88:91], v[24:27]\n"
"v_mul_f32 v113, s44, v113\n"
"v_cvt_pk_bf16_f32 v100, v112, v113\n"
"v_lshlrev_b32 v112, 16, v101\n"
"v_mfma_f32_16x16x32_bf16 v[24:27], a[120:123], v[92:95], v[24:27]\n"
"v_and_b32 v113, s45, v101\n"
"v_mul_f32 v112, s44, v112\n"
"v_mfma_f32_16x16x32_bf16 v[28:31], a[100:103], v[80:83], v[28:31]\n"
"v_mul_f32 v113, s44, v113\n"
"v_cvt_pk_bf16_f32 v101, v112, v113\n"
"v_mfma_f32_16x16x32_bf16 v[28:31], a[108:111], v[84:87], v[28:31]\n"
"v_lshlrev_b32 v112, 16, v102\n"
"v_and_b32 v113, s45, v102\n"
"v_mfma_f32_16x16x32_bf16 v[28:31], a[116:119], v[88:91], v[28:31]\n"
"v_mul_f32 v112, s44, v112\n"
"v_mul_f32 v113, s44, v113\n"
"v_mfma_f32_16x16x32_bf16 v[28:31], a[124:127], v[92:95], v[28:31]\n"
"s_setprio 0\n"
"ds_read_b128 v[80:83], v124 offset:12288\n"
"ds_read_b128 v[84:87], v124 offset:13312\n"
"ds_read_b128 v[88:91], v124 offset:14336\n"
"ds_read_b128 v[92:95], v124 offset:15360\n"
"ds_read_b128 v[114:117], v124 offset:46080\n"
"ds_read_b64_tr_b16 a[48:49], v125 offset:4096\n"
"ds_read_b64_tr_b16 a[52:53], v125 offset:4104\n"
"ds_read_b64_tr_b16 a[50:51], v125 offset:5120\n"
"ds_read_b64_tr_b16 a[54:55], v125 offset:5128\n"
"s_waitcnt lgkmcnt(11)\n"
"s_xor_b32 s43, s62, 0x12000\n"
"s_add_u32 m0, s55, 0x800\n"
"s_add_u32 s43, s43, s39\n"
"buffer_load_dwordx4 v121, s[28:31], s43 offen offset:0 lds\n"
"s_waitcnt lgkmcnt(9)\n"
"s_xor_b32 s43, s62, 0x13000\n"
"s_add_u32 m0, s55, 0xc00\n"
"s_add_u32 s43, s43, s39\n"
"buffer_load_dwordx4 v121, s[28:31], s43 offen offset:0 lds\n"
"v_mfma_f32_16x16x32_bf16 v[0:3], v[32:35], v[64:67], 0\n"
"s_setprio 2\n"
"v_mfma_f32_16x16x32_bf16 v[0:3], v[40:43], v[68:71], v[0:3]\n"
"s_waitcnt lgkmcnt(4)\n"
"v_add_f32 v114, v114, v115\n"
"v_add_f32 v116, v116, v117\n"
"v_add_f32 v114, v114, v116\n"
"v_mfma_f32_16x16x32_bf16 v[0:3], v[48:51], v[72:75], v[0:3]\n"
"v_rcp_f32 v119, v114\n"
"v_subrev_u32 v118, s46, v114\n"
"v_mfma_f32_16x16x32_bf16 v[0:3], v[56:59], v[76:79], v[0:3]\n"
"v_max_u32 v123, v123, v118\n"
"v_cvt_pk_bf16_f32 v102, v112, v113\n"
"v_mfma_f32_16x16x32_bf16 v[4:7], v[36:39], v[64:67], 0\n"
"v_lshlrev_b32 v112, 16, v103\n"
"v_and_b32 v113, s45, v103\n"
"v_mfma_f32_16x16x32_bf16 v[4:7], v[44:47], v[68:71], v[4:7]\n"
"v_mul_f32 v112, s44, v112\n"
"v_mul_f32 v113, s44, v113\n"
"v_mfma_f32_16x16x32_bf16 v[4:7], v[52:55], v[72:75], v[4:7]\n"
"v_cvt_pk_bf16_f32 v103, v112, v113\n"
"s_waitcnt vmcnt(7)\n"
"v_lshlrev_b32 v112, 16, v104\n"
"v_mfma_f32_16x16x32_bf16 v[4:7], v[60:63], v[76:79], v[4:7]\n"
"s_setprio 0\n"
"ds_read_b128 v[64:67], v124 offset:16384\n"
"ds_read_b128 v[68:71], v124 offset:17408\n"
"ds_read_b128 v[72:75], v124 offset:18432\n"
"ds_read_b128 v[76:79], v124 offset:19456\n"
"ds_read_b64_tr_b16 a[56:57], v125 offset:6144\n"
"ds_read_b64_tr_b16 a[60:61], v125 offset:6152\n"
"ds_read_b64_tr_b16 a[58:59], v125 offset:7168\n"
"ds_read_b64_tr_b16 a[62:63], v125 offset:7176\n"
"ds_write_b128 v126, v[100:103] offset:24576\n"
"s_waitcnt lgkmcnt(11)\n"
"s_xor_b32 s43, s62, 0x14000\n"
"s_add_u32 m0, s55, 0x1000\n"
"s_add_u32 s43, s43, s39\n"
"buffer_load_dwordx4 v121, s[28:31], s43 offen offset:0 lds\n"
"s_waitcnt lgkmcnt(9)\n"
"s_xor_b32 s43, s62, 0x15000\n"
"s_add_u32 m0, s55, 0x1400\n"
"s_add_u32 s43, s43, s39\n"
"buffer_load_dwordx4 v121, s[28:31], s43 offen offset:0 lds\n"
"v_mfma_f32_16x16x32_bf16 v[0:3], a[32:35], v[80:83], v[0:3]\n"
"s_setprio 2\n"
"v_mfma_f32_16x16x32_bf16 v[0:3], a[40:43], v[84:87], v[0:3]\n"
"v_and_b32 v113, s45, v104\n"
"v_mul_f32 v112, s44, v112\n"
"v_mul_f32 v113, s44, v113\n"
"v_cvt_pk_bf16_f32 v104, v112, v113\n"
"v_mfma_f32_16x16x32_bf16 v[0:3], a[48:51], v[88:91], v[0:3]\n"
"v_lshlrev_b32 v112, 16, v105\n"
"v_and_b32 v113, s45, v105\n"
"v_mul_f32 v112, s44, v112\n"
"s_waitcnt lgkmcnt(2)\n"
"v_mfma_f32_16x16x32_bf16 v[0:3], a[56:59], v[92:95], v[0:3]\n"
"v_mul_f32 v113, s44, v113\n"
"v_cvt_pk_bf16_f32 v105, v112, v113\n"
"v_lshlrev_b32 v112, 16, v106\n"
"v_mfma_f32_16x16x32_bf16 v[4:7], a[36:39], v[80:83], v[4:7]\n"
"v_and_b32 v113, s45, v106\n"
"v_mul_f32 v112, s44, v112\n"
"v_mul_f32 v113, s44, v113\n"
"v_mfma_f32_16x16x32_bf16 v[4:7], a[44:47], v[84:87], v[4:7]\n"
"v_cvt_pk_bf16_f32 v106, v112, v113\n"
"v_lshlrev_b32 v112, 16, v107\n"
"v_and_b32 v113, s45, v107\n"
"v_mfma_f32_16x16x32_bf16 v[4:7], a[52:55], v[88:91], v[4:7]\n"
"v_mul_f32 v112, s44, v112\n"
"v_mul_f32 v113, s44, v113\n"
"v_cvt_pk_bf16_f32 v107, v112, v113\n"
"s_waitcnt lgkmcnt(1)\n"
"v_mfma_f32_16x16x32_bf16 v[4:7], a[60:63], v[92:95], v[4:7]\n"
"s_setprio 0\n"
"ds_read_b128 v[80:83], v124 offset:20480\n"
"ds_read_b128 v[84:87], v124 offset:21504\n"
"ds_read_b128 v[88:91], v124 offset:22528\n"
"ds_read_b128 v[92:95], v124 offset:23552\n"
"ds_write_b128 v126, v[104:107] offset:28672\n"
"s_xor_b32 s43, s62, 0x16000\n"
"s_add_u32 m0, s55, 0x1800\n"
"s_add_u32 s43, s43, s39\n"
"buffer_load_dwordx4 v121, s[28:31], s43 offen offset:0 lds\n"
"s_xor_b32 s43, s62, 0x17000\n"
"s_add_u32 m0, s55, 0x1c00\n"
"s_add_u32 s43, s43, s39\n"
"buffer_load_dwordx4 v121, s[28:31], s43 offen offset:0 lds\n"
"v_mfma_f32_16x16x32_bf16 v[8:11], v[32:35], v[64:67], 0\n"
"s_setprio 2\n"
"v_mfma_f32_16x16x32_bf16 v[8:11], v[40:43], v[68:71], v[8:11]\n"
"v_mul_f32 v24, v119, v24\n"
"v_mul_f32 v25, v119, v25\n"
"v_mfma_f32_16x16x32_bf16 v[8:11], v[48:51], v[72:75], v[8:11]\n"
"v_mul_f32 v26, v119, v26\n"
"v_mul_f32 v27, v119, v27\n"
"v_mfma_f32_16x16x32_bf16 v[8:11], v[56:59], v[76:79], v[8:11]\n"
"v_mul_f32 v28, v119, v28\n"
"v_mul_f32 v29, v119, v29\n"
"v_mfma_f32_16x16x32_bf16 v[12:15], v[36:39], v[64:67], 0\n"
"v_mul_f32 v30, v119, v30\n"
"v_mul_f32 v31, v119, v31\n"
"v_mfma_f32_16x16x32_bf16 v[12:15], v[44:47], v[68:71], v[12:15]\n"
"v_fmac_f32 v122, v24, v25\n"
"v_fmac_f32 v122, v26, v27\n"
"v_mfma_f32_16x16x32_bf16 v[12:15], v[52:55], v[72:75], v[12:15]\n"
"v_fmac_f32 v122, v28, v29\n"
"v_fmac_f32 v122, v30, v31\n"
"s_waitcnt lgkmcnt(0)\n"
"s_setprio 0\n"
"v_mfma_f32_16x16x32_bf16 v[12:15], v[60:63], v[76:79], v[12:15]\n"
"s_barrier\n"
"ds_read_b128 v[64:67], v124 offset:24576\n"
"ds_read_b128 v[68:71], v124 offset:25600\n"
"ds_read_b128 v[72:75], v124 offset:26624\n"
"ds_read_b128 v[76:79], v124 offset:27648\n"
"v_mfma_f32_16x16x32_bf16 v[8:11], a[32:35], v[80:83], v[8:11]\n"
"s_setprio 2\n"
"v_mfma_f32_16x16x32_bf16 v[8:11], a[40:43], v[84:87], v[8:11]\n"
"v_cvt_pk_bf16_f32 v24, v24, v25\n"
"v_mfma_f32_16x16x32_bf16 v[8:11], a[48:51], v[88:91], v[8:11]\n"
"v_cvt_pk_bf16_f32 v25, v26, v27\n"
"v_mfma_f32_16x16x32_bf16 v[8:11], a[56:59], v[92:95], v[8:11]\n"
"v_cvt_pk_bf16_f32 v26, v28, v29\n"
"v_mfma_f32_16x16x32_bf16 v[12:15], a[36:39], v[80:83], v[12:15]\n"
"v_cvt_pk_bf16_f32 v27, v30, v31\n"
"v_mfma_f32_16x16x32_bf16 v[12:15], a[44:47], v[84:87], v[12:15]\n"
"v_mfma_f32_16x16x32_bf16 v[12:15], a[52:55], v[88:91], v[12:15]\n"
"v_mfma_f32_16x16x32_bf16 v[12:15], a[60:63], v[92:95], v[12:15]\n"
"s_setprio 0\n"
"ds_read_b128 v[80:83], v124 offset:28672\n"
"ds_read_b128 v[84:87], v124 offset:29696\n"
"ds_read_b128 v[88:91], v124 offset:30720\n"
"ds_read_b128 v[92:95], v124 offset:31744\n"
"s_add_u32 s41, s52, 0x16000\n"
"buffer_store_dwordx4 v[24:27], v127, s[64:67], s41 offen sc1\n"
"s_waitcnt lgkmcnt(4)\n"
"v_mfma_f32_16x16x32_bf16 v[16:19], v[32:35], v[64:67], 0\n"
"s_setprio 2\n"
"v_mfma_f32_16x16x32_bf16 v[16:19], v[40:43], v[68:71], v[16:19]\n"
"v_mfma_f32_16x16x32_bf16 v[16:19], v[48:51], v[72:75], v[16:19]\n"
"v_mfma_f32_16x16x32_bf16 v[16:19], v[56:59], v[76:79], v[16:19]\n"
"v_mfma_f32_16x16x32_bf16 v[20:23], v[36:39], v[64:67], 0\n"
"v_mfma_f32_16x16x32_bf16 v[20:23], v[44:47], v[68:71], v[20:23]\n"
"v_mfma_f32_16x16x32_bf16 v[20:23], v[52:55], v[72:75], v[20:23]\n"
"v_mfma_f32_16x16x32_bf16 v[20:23], v[60:63], v[76:79], v[20:23]\n"
"s_setprio 0\n"
"ds_read_b128 v[64:67], v124 offset:32768\n"
"ds_read_b128 v[68:71], v124 offset:33792\n"
"ds_read_b128 v[72:75], v124 offset:34816\n"
"ds_read_b128 v[76:79], v124 offset:35840\n"
"s_waitcnt lgkmcnt(4)\n"
"v_mfma_f32_16x16x32_bf16 v[16:19], a[32:35], v[80:83], v[16:19]\n"
"s_setprio 2\n"
"v_mfma_f32_16x16x32_bf16 v[16:19], a[40:43], v[84:87], v[16:19]\n"
"v_mfma_f32_16x16x32_bf16 v[16:19], a[48:51], v[88:91], v[16:19]\n"
"v_mfma_f32_16x16x32_bf16 v[16:19], a[56:59], v[92:95], v[16:19]\n"
"v_mfma_f32_16x16x32_bf16 v[20:23], a[36:39], v[80:83], v[20:23]\n"
"v_mfma_f32_16x16x32_bf16 v[20:23], a[44:47], v[84:87], v[20:23]\n"
"v_mfma_f32_16x16x32_bf16 v[20:23], a[52:55], v[88:91], v[20:23]\n"
"v_mfma_f32_16x16x32_bf16 v[20:23], a[60:63], v[92:95], v[20:23]\n"
"s_setprio 0\n"
"ds_read_b128 v[80:83], v124 offset:0\n"
"ds_read_b128 v[84:87], v124 offset:1024\n"
"ds_read_b128 v[88:91], v124 offset:2048\n"
"ds_read_b128 v[92:95], v124 offset:3072\n"
"s_waitcnt lgkmcnt(4)\n"
"v_mfma_f32_16x16x32_bf16 v[24:27], v[32:35], v[64:67], 0\n"
"s_setprio 2\n"
"v_mfma_f32_16x16x32_bf16 v[24:27], v[40:43], v[68:71], v[24:27]\n"
"v_mfma_f32_16x16x32_bf16 v[24:27], v[48:51], v[72:75], v[24:27]\n"
"v_mfma_f32_16x16x32_bf16 v[24:27], v[56:59], v[76:79], v[24:27]\n"
"v_mfma_f32_16x16x32_bf16 v[28:31], v[36:39], v[64:67], 0\n"
"v_mfma_f32_16x16x32_bf16 v[28:31], v[44:47], v[68:71], v[28:31]\n"
"v_mfma_f32_16x16x32_bf16 v[28:31], v[52:55], v[72:75], v[28:31]\n"
"s_waitcnt lgkmcnt(0)\n"
"s_setprio 0\n"
"v_mfma_f32_16x16x32_bf16 v[28:31], v[60:63], v[76:79], v[28:31]\n"
"s_barrier\n"
"ds_read_b128 v[64:67], v124 offset:4096\n"
"ds_read_b128 v[68:71], v124 offset:5120\n"
"ds_read_b128 v[72:75], v124 offset:6144\n"
"ds_read_b128 v[76:79], v124 offset:7168\n"
"v_mfma_f32_16x16x32_bf16 v[24:27], a[32:35], v[80:83], v[24:27]\n"
"s_setprio 2\n"
"v_mfma_f32_16x16x32_bf16 v[24:27], a[40:43], v[84:87], v[24:27]\n"
"v_mfma_f32_16x16x32_bf16 v[24:27], a[48:51], v[88:91], v[24:27]\n"
"v_mfma_f32_16x16x32_bf16 v[24:27], a[56:59], v[92:95], v[24:27]\n"
"v_mfma_f32_16x16x32_bf16 v[28:31], a[36:39], v[80:83], v[28:31]\n"
"v_mfma_f32_16x16x32_bf16 v[28:31], a[44:47], v[84:87], v[28:31]\n"
"v_mfma_f32_16x16x32_bf16 v[28:31], a[52:55], v[88:91], v[28:31]\n"
"v_mfma_f32_16x16x32_bf16 v[28:31], a[60:63], v[92:95], v[28:31]\n"
"s_setprio 0\n"
"ds_read_b128 v[80:83], v124 offset:8192\n"
"ds_read_b128 v[84:87], v124 offset:9216\n"
"ds_read_b128 v[88:91], v124 offset:10240\n"
"ds_read_b128 v[92:95], v124 offset:11264\n"
"s_waitcnt lgkmcnt(4)\n"
"v_mfma_f32_16x16x32_bf16 a[0:3], v[32:35], v[64:67], 0\n"
"s_setprio 2\n"
"v_mfma_f32_16x16x32_bf16 a[0:3], v[40:43], v[68:71], a[0:3]\n"
"v_mfma_f32_16x16x32_bf16 a[0:3], v[48:51], v[72:75], a[0:3]\n"
"v_mfma_f32_16x16x32_bf16 a[0:3], v[56:59], v[76:79], a[0:3]\n"
"v_mfma_f32_16x16x32_bf16 a[4:7], v[36:39], v[64:67], 0\n"
"v_mfma_f32_16x16x32_bf16 a[4:7], v[44:47], v[68:71], a[4:7]\n"
"v_mfma_f32_16x16x32_bf16 a[4:7], v[52:55], v[72:75], a[4:7]\n"
"v_mfma_f32_16x16x32_bf16 a[4:7], v[60:63], v[76:79], a[4:7]\n"
"s_setprio 0\n"
"ds_read_b128 v[64:67], v124 offset:12288\n"
"ds_read_b128 v[68:71], v124 offset:13312\n"
"ds_read_b128 v[72:75], v124 offset:14336\n"
"ds_read_b128 v[76:79], v124 offset:15360\n"
"s_add_u32 s40, s51, 0x36000\n"
"buffer_load_dwordx4 v[108:111], v124, s[20:23], s40 offen nt\n"
"s_waitcnt lgkmcnt(4)\n"
"v_mfma_f32_16x16x32_bf16 a[0:3], a[32:35], v[80:83], a[0:3]\n"
"s_setprio 2\n"
"v_mfma_f32_16x16x32_bf16 a[0:3], a[40:43], v[84:87], a[0:3]\n"
"v_mfma_f32_16x16x32_bf16 a[0:3], a[48:51], v[88:91], a[0:3]\n"
"v_mfma_f32_16x16x32_bf16 a[0:3], a[56:59], v[92:95], a[0:3]\n"
"v_mfma_f32_16x16x32_bf16 a[4:7], a[36:39], v[80:83], a[4:7]\n"
"v_mfma_f32_16x16x32_bf16 a[4:7], a[44:47], v[84:87], a[4:7]\n"
"v_mfma_f32_16x16x32_bf16 a[4:7], a[52:55], v[88:91], a[4:7]\n"
"v_mfma_f32_16x16x32_bf16 a[4:7], a[60:63], v[92:95], a[4:7]\n"
"s_setprio 0\n"
"ds_read_b128 v[80:83], v124 offset:16384\n"
"ds_read_b128 v[84:87], v124 offset:17408\n"
"ds_read_b128 v[88:91], v124 offset:18432\n"
"ds_read_b128 v[92:95], v124 offset:19456\n"
"s_waitcnt vmcnt(9)\n"
"ds_read_b64_tr_b16 a[64:65], v125 offset:0\n"
"ds_read_b64_tr_b16 a[68:69], v125 offset:8\n"
"s_waitcnt vmcnt(8)\n"
"ds_read_b64_tr_b16 a[66:67], v125 offset:1024\n"
"ds_read_b64_tr_b16 a[70:71], v125 offset:1032\n"
"s_add_u32 s40, s51, 0x37000\n"
"buffer_load_dwordx4 v[96:99], v124, s[20:23], s40 offen nt\n"
"s_waitcnt lgkmcnt(8)\n"
"v_mfma_f32_16x16x32_bf16 a[8:11], v[32:35], v[64:67], 0\n"
"s_setprio 2\n"
"v_mfma_f32_16x16x32_bf16 a[8:11], v[40:43], v[68:71], a[8:11]\n"
"v_mfma_f32_16x16x32_bf16 a[8:11], v[48:51], v[72:75], a[8:11]\n"
"v_mfma_f32_16x16x32_bf16 a[8:11], v[56:59], v[76:79], a[8:11]\n"
"v_mfma_f32_16x16x32_bf16 a[12:15], v[36:39], v[64:67], 0\n"
"v_mfma_f32_16x16x32_bf16 a[12:15], v[44:47], v[68:71], a[12:15]\n"
"v_mfma_f32_16x16x32_bf16 a[12:15], v[52:55], v[72:75], a[12:15]\n"
"s_waitcnt lgkmcnt(0)\n"
"s_setprio 0\n"
"v_mfma_f32_16x16x32_bf16 a[12:15], v[60:63], v[76:79], a[12:15]\n"
"s_barrier\n"
"ds_read_b128 v[64:67], v124 offset:20480\n"
"ds_read_b128 v[68:71], v124 offset:21504\n"
"ds_read_b128 v[72:75], v124 offset:22528\n"
"ds_read_b128 v[76:79], v124 offset:23552\n"
"s_waitcnt vmcnt(8)\n"
"ds_read_b64_tr_b16 a[72:73], v125 offset:2048\n"
"ds_read_b64_tr_b16 a[76:77], v125 offset:2056\n"
"s_waitcnt vmcnt(7)\n"
"ds_read_b64_tr_b16 a[74:75], v125 offset:3072\n"
"ds_read_b64_tr_b16 a[78:79], v125 offset:3080\n"
"s_add_u32 s40, s51, 0x38000\n"
"buffer_load_dwordx4 v[100:103], v124, s[20:23], s40 offen nt\n"
"s_xor_b32 s43, s62, 0x18000\n"
"s_add_u32 m0, s55, 0\n"
"s_add_u32 s43, s43, s39\n"
"buffer_load_dwordx4 v121, s[28:31], s43 offen offset:0 lds\n"
"s_xor_b32 s43, s62, 0x19000\n"
"s_add_u32 m0, s55, 0x400\n"
"s_add_u32 s43, s43, s39\n"
"buffer_load_dwordx4 v121, s[28:31], s43 offen offset:0 lds\n"
"v_mfma_f32_16x16x32_bf16 a[8:11], a[32:35], v[80:83], a[8:11]\n"
"s_setprio 2\n"
"v_mfma_f32_16x16x32_bf16 a[8:11], a[40:43], v[84:87], a[8:11]\n"
"v_mfma_f32_16x16x32_bf16 a[8:11], a[48:51], v[88:91], a[8:11]\n"
"v_mfma_f32_16x16x32_bf16 a[8:11], a[56:59], v[92:95], a[8:11]\n"
"v_mfma_f32_16x16x32_bf16 a[12:15], a[36:39], v[80:83], a[12:15]\n"
"v_mfma_f32_16x16x32_bf16 a[12:15], a[44:47], v[84:87], a[12:15]\n"
"v_mfma_f32_16x16x32_bf16 a[12:15], a[52:55], v[88:91], a[12:15]\n"
"v_mfma_f32_16x16x32_bf16 a[12:15], a[60:63], v[92:95], a[12:15]\n"
"s_setprio 0\n"
"ds_read_b128 v[80:83], v124 offset:24576\n"
"ds_read_b128 v[84:87], v124 offset:25600\n"
"ds_read_b128 v[88:91], v124 offset:26624\n"
"ds_read_b128 v[92:95], v124 offset:27648\n"
"s_waitcnt vmcnt(9)\n"
"ds_read_b64_tr_b16 a[80:81], v125 offset:4096\n"
"ds_read_b64_tr_b16 a[84:85], v125 offset:4104\n"
"s_waitcnt vmcnt(8)\n"
"ds_read_b64_tr_b16 a[82:83], v125 offset:5120\n"
"ds_read_b64_tr_b16 a[86:87], v125 offset:5128\n"
"s_waitcnt lgkmcnt(10)\n"
"s_xor_b32 s43, s62, 0x1a000\n"
"s_add_u32 m0, s55, 0x800\n"
"s_add_u32 s43, s43, s39\n"
"buffer_load_dwordx4 v121, s[28:31], s43 offen offset:0 lds\n"
"s_waitcnt lgkmcnt(8)\n"
"s_xor_b32 s43, s62, 0x1b000\n"
"s_add_u32 m0, s55, 0xc00\n"
"s_add_u32 s43, s43, s39\n"
"buffer_load_dwordx4 v121, s[28:31], s43 offen offset:0 lds\n"
"v_mfma_f32_16x16x32_bf16 a[16:19], v[32:35], v[64:67], 0\n"
"s_setprio 2\n"
"v_mfma_f32_16x16x32_bf16 a[16:19], v[40:43], v[68:71], a[16:19]\n"
"v_mfma_f32_16x16x32_bf16 a[16:19], v[48:51], v[72:75], a[16:19]\n"
"v_mfma_f32_16x16x32_bf16 a[16:19], v[56:59], v[76:79], a[16:19]\n"
"v_mfma_f32_16x16x32_bf16 a[20:23], v[36:39], v[64:67], 0\n"
"v_mfma_f32_16x16x32_bf16 a[20:23], v[44:47], v[68:71], a[20:23]\n"
"v_mfma_f32_16x16x32_bf16 a[20:23], v[52:55], v[72:75], a[20:23]\n"
"v_mfma_f32_16x16x32_bf16 a[20:23], v[60:63], v[76:79], a[20:23]\n"
"s_setprio 0\n"
"ds_read_b128 v[64:67], v124 offset:28672\n"
"ds_read_b128 v[68:71], v124 offset:29696\n"
"ds_read_b128 v[72:75], v124 offset:30720\n"
"ds_read_b128 v[76:79], v124 offset:31744\n"
"s_waitcnt vmcnt(9)\n"
"ds_read_b64_tr_b16 a[88:89], v125 offset:6144\n"
"ds_read_b64_tr_b16 a[92:93], v125 offset:6152\n"
"s_waitcnt vmcnt(8)\n"
"ds_read_b64_tr_b16 a[90:91], v125 offset:7168\n"
"ds_read_b64_tr_b16 a[94:95], v125 offset:7176\n"
"s_waitcnt lgkmcnt(10)\n"
"s_xor_b32 s43, s62, 0x1c000\n"
"s_add_u32 m0, s55, 0x1000\n"
"s_add_u32 s43, s43, s39\n"
"buffer_load_dwordx4 v121, s[28:31], s43 offen offset:0 lds\n"
"s_waitcnt lgkmcnt(8)\n"
"s_xor_b32 s43, s62, 0x1d000\n"
"s_add_u32 m0, s55, 0x1400\n"
"s_add_u32 s43, s43, s39\n"
"buffer_load_dwordx4 v121, s[28:31], s43 offen offset:0 lds\n"
"v_mfma_f32_16x16x32_bf16 a[16:19], a[32:35], v[80:83], a[16:19]\n"
"s_setprio 2\n"
"v_mfma_f32_16x16x32_bf16 a[16:19], a[40:43], v[84:87], a[16:19]\n"
"v_mfma_f32_16x16x32_bf16 a[16:19], a[48:51], v[88:91], a[16:19]\n"
"v_mfma_f32_16x16x32_bf16 a[16:19], a[56:59], v[92:95], a[16:19]\n"
"v_mfma_f32_16x16x32_bf16 a[20:23], a[36:39], v[80:83], a[20:23]\n"
"v_mfma_f32_16x16x32_bf16 a[20:23], a[44:47], v[84:87], a[20:23]\n"
"v_mfma_f32_16x16x32_bf16 a[20:23], a[52:55], v[88:91], a[20:23]\n"
"v_mfma_f32_16x16x32_bf16 a[20:23], a[60:63], v[92:95], a[20:23]\n"
"s_setprio 0\n"
"ds_read_b128 v[80:83], v124 offset:32768\n"
"ds_read_b128 v[84:87], v124 offset:33792\n"
"ds_read_b128 v[88:91], v124 offset:34816\n"
"ds_read_b128 v[92:95], v124 offset:35840\n"
"s_waitcnt lgkmcnt(6)\n"
"s_xor_b32 s43, s62, 0x1e000\n"
"s_add_u32 m0, s55, 0x1800\n"
"s_add_u32 s43, s43, s39\n"
"buffer_load_dwordx4 v121, s[28:31], s43 offen offset:0 lds\n"
"s_waitcnt lgkmcnt(4)\n"
"s_xor_b32 s43, s62, 0x1f000\n"
"s_add_u32 m0, s55, 0x1c00\n"
"s_add_u32 s43, s43, s39\n"
"buffer_load_dwordx4 v121, s[28:31], s43 offen offset:0 lds\n"
"v_mfma_f32_16x16x32_bf16 a[24:27], v[32:35], v[64:67], 0\n"
"s_setprio 2\n"
"v_mfma_f32_16x16x32_bf16 a[24:27], v[40:43], v[68:71], a[24:27]\n"
"v_mfma_f32_16x16x32_bf16 a[24:27], v[48:51], v[72:75], a[24:27]\n"
"v_mfma_f32_16x16x32_bf16 a[24:27], v[56:59], v[76:79], a[24:27]\n"
"v_mfma_f32_16x16x32_bf16 a[28:31], v[36:39], v[64:67], 0\n"
"v_mfma_f32_16x16x32_bf16 a[28:31], v[44:47], v[68:71], a[28:31]\n"
"v_mfma_f32_16x16x32_bf16 a[28:31], v[52:55], v[72:75], a[28:31]\n"
"s_waitcnt lgkmcnt(0)\n"
"s_setprio 0\n"
"v_mfma_f32_16x16x32_bf16 a[28:31], v[60:63], v[76:79], a[28:31]\n"
"s_barrier\n"
"ds_read_b128 v[64:67], v124 offset:0\n"
"ds_read_b128 v[68:71], v124 offset:1024\n"
"ds_read_b128 v[72:75], v124 offset:2048\n"
"ds_read_b128 v[76:79], v124 offset:3072\n"
"s_add_u32 s40, s51, 0x39000\n"
"buffer_load_dwordx4 v[104:107], v124, s[20:23], s40 offen nt\n"
"v_mfma_f32_16x16x32_bf16 a[24:27], a[32:35], v[80:83], a[24:27]\n"
"s_setprio 2\n"
"v_mfma_f32_16x16x32_bf16 a[24:27], a[40:43], v[84:87], a[24:27]\n"
"v_mfma_f32_16x16x32_bf16 a[24:27], a[48:51], v[88:91], a[24:27]\n"
"v_mfma_f32_16x16x32_bf16 a[24:27], a[56:59], v[92:95], a[24:27]\n"
"v_mfma_f32_16x16x32_bf16 a[28:31], a[36:39], v[80:83], a[28:31]\n"
"v_mfma_f32_16x16x32_bf16 a[28:31], a[44:47], v[84:87], a[28:31]\n"
"v_mfma_f32_16x16x32_bf16 a[28:31], a[52:55], v[88:91], a[28:31]\n"
"v_mfma_f32_16x16x32_bf16 a[28:31], a[60:63], v[92:95], a[28:31]\n"
"s_setprio 0\n"
"ds_read_b128 v[80:83], v124 offset:4096\n"
"ds_read_b128 v[84:87], v124 offset:5120\n"
"ds_read_b128 v[88:91], v124 offset:6144\n"
"ds_read_b128 v[92:95], v124 offset:7168\n"
"s_waitcnt lgkmcnt(4)\n"
"v_mfma_f32_16x16x32_bf16 v[0:3], a[64:67], v[64:67], v[0:3]\n"
"s_setprio 2\n"
"v_mfma_f32_16x16x32_bf16 v[0:3], a[72:75], v[68:71], v[0:3]\n"
"v_mfma_f32_16x16x32_bf16 v[0:3], a[80:83], v[72:75], v[0:3]\n"
"v_mfma_f32_16x16x32_bf16 v[0:3], a[88:91], v[76:79], v[0:3]\n"
"v_mfma_f32_16x16x32_bf16 v[4:7], a[68:71], v[64:67], v[4:7]\n"
"v_mfma_f32_16x16x32_bf16 v[4:7], a[76:79], v[68:71], v[4:7]\n"
"v_mfma_f32_16x16x32_bf16 v[4:7], a[84:87], v[72:75], v[4:7]\n"
"v_mfma_f32_16x16x32_bf16 v[4:7], a[92:95], v[76:79], v[4:7]\n"
"s_setprio 0\n"
"ds_read_b128 v[64:67], v124 offset:8192\n"
"ds_read_b128 v[68:71], v124 offset:9216\n"
"ds_read_b128 v[72:75], v124 offset:10240\n"
"ds_read_b128 v[76:79], v124 offset:11264\n"
"s_waitcnt lgkmcnt(4)\n"
"v_mfma_f32_16x16x32_bf16 v[8:11], a[64:67], v[80:83], v[8:11]\n"
"s_setprio 2\n"
"v_mfma_f32_16x16x32_bf16 v[8:11], a[72:75], v[84:87], v[8:11]\n"
"v_mfma_f32_16x16x32_bf16 v[8:11], a[80:83], v[88:91], v[8:11]\n"
"v_mfma_f32_16x16x32_bf16 v[8:11], a[88:91], v[92:95], v[8:11]\n"
"v_mfma_f32_16x16x32_bf16 v[12:15], a[68:71], v[80:83], v[12:15]\n"
"v_mfma_f32_16x16x32_bf16 v[12:15], a[76:79], v[84:87], v[12:15]\n"
"v_mfma_f32_16x16x32_bf16 v[12:15], a[84:87], v[88:91], v[12:15]\n"
"v_mfma_f32_16x16x32_bf16 v[12:15], a[92:95], v[92:95], v[12:15]\n"
"s_setprio 0\n"
"ds_read_b128 v[80:83], v124 offset:12288\n"
"ds_read_b128 v[84:87], v124 offset:13312\n"
"ds_read_b128 v[88:91], v124 offset:14336\n"
"ds_read_b128 v[92:95], v124 offset:15360\n"
"s_waitcnt lgkmcnt(4)\n"
"v_mfma_f32_16x16x32_bf16 v[16:19], a[64:67], v[64:67], v[16:19]\n"
"s_setprio 2\n"
"v_mfma_f32_16x16x32_bf16 v[16:19], a[72:75], v[68:71], v[16:19]\n"
"s_waitcnt vmcnt(11)\n"
"v_lshlrev_b32 v112, 16, v108\n"
"v_and_b32 v113, s45, v108\n"
"v_mul_f32 v112, s44, v112\n"
"v_mfma_f32_16x16x32_bf16 v[16:19], a[80:83], v[72:75], v[16:19]\n"
"v_mul_f32 v113, s44, v113\n"
"v_cvt_pk_bf16_f32 v108, v112, v113\n"
"v_mfma_f32_16x16x32_bf16 v[16:19], a[88:91], v[76:79], v[16:19]\n"
"v_lshlrev_b32 v112, 16, v109\n"
"v_and_b32 v113, s45, v109\n"
"v_mfma_f32_16x16x32_bf16 v[20:23], a[68:71], v[64:67], v[20:23]\n"
"v_mul_f32 v112, s44, v112\n"
"v_mul_f32 v113, s44, v113\n"
"v_mfma_f32_16x16x32_bf16 v[20:23], a[76:79], v[68:71], v[20:23]\n"
"v_cvt_pk_bf16_f32 v109, v112, v113\n"
"v_lshlrev_b32 v112, 16, v110\n"
"v_mfma_f32_16x16x32_bf16 v[20:23], a[84:87], v[72:75], v[20:23]\n"
"v_and_b32 v113, s45, v110\n"
"v_mul_f32 v112, s44, v112\n"
"s_waitcnt lgkmcnt(0)\n"
"s_setprio 0\n"
"v_mfma_f32_16x16x32_bf16 v[20:23], a[92:95], v[76:79], v[20:23]\n"
"s_barrier\n"
"ds_read_b128 v[64:67], v124 offset:16384\n"
"ds_read_b128 v[68:71], v124 offset:17408\n"
"ds_read_b128 v[72:75], v124 offset:18432\n"
"ds_read_b128 v[76:79], v124 offset:19456\n"
"v_mfma_f32_16x16x32_bf16 v[24:27], a[64:67], v[80:83], v[24:27]\n"
"s_setprio 2\n"
"v_mfma_f32_16x16x32_bf16 v[24:27], a[72:75], v[84:87], v[24:27]\n"
"v_mul_f32 v113, s44, v113\n"
"v_cvt_pk_bf16_f32 v110, v112, v113\n"
"v_lshlrev_b32 v112, 16, v111\n"
"v_mfma_f32_16x16x32_bf16 v[24:27], a[80:83], v[88:91], v[24:27]\n"
"v_and_b32 v113, s45, v111\n"
"v_mul_f32 v112, s44, v112\n"
"v_mfma_f32_16x16x32_bf16 v[24:27], a[88:91], v[92:95], v[24:27]\n"
"v_mul_f32 v113, s44, v113\n"
"v_cvt_pk_bf16_f32 v111, v112, v113\n"
"v_mfma_f32_16x16x32_bf16 v[28:31], a[68:71], v[80:83], v[28:31]\n"
"s_waitcnt vmcnt(10)\n"
"v_lshlrev_b32 v112, 16, v96\n"
"v_and_b32 v113, s45, v96\n"
"v_mfma_f32_16x16x32_bf16 v[28:31], a[76:79], v[84:87], v[28:31]\n"
"v_mul_f32 v112, s44, v112\n"
"v_mul_f32 v113, s44, v113\n"
"v_mfma_f32_16x16x32_bf16 v[28:31], a[84:87], v[88:91], v[28:31]\n"
"v_cvt_pk_bf16_f32 v96, v112, v113\n"
"v_lshlrev_b32 v112, 16, v97\n"
"v_mfma_f32_16x16x32_bf16 v[28:31], a[92:95], v[92:95], v[28:31]\n"
"s_setprio 0\n"
"ds_read_b128 v[80:83], v124 offset:20480\n"
"ds_read_b128 v[84:87], v124 offset:21504\n"
"ds_read_b128 v[88:91], v124 offset:22528\n"
"ds_read_b128 v[92:95], v124 offset:23552\n"
"ds_write_b128 v126, v[108:111] offset:45056\n"
"s_waitcnt lgkmcnt(5)\n"
"v_mfma_f32_16x16x32_bf16 a[0:3], a[64:67], v[64:67], a[0:3]\n"
"s_setprio 2\n"
"v_mfma_f32_16x16x32_bf16 a[0:3], a[72:75], v[68:71], a[0:3]\n"
"v_and_b32 v113, s45, v97\n"
"v_mul_f32 v112, s44, v112\n"
"v_mul_f32 v113, s44, v113\n"
"v_mfma_f32_16x16x32_bf16 a[0:3], a[80:83], v[72:75], a[0:3]\n"
"v_cvt_pk_bf16_f32 v97, v112, v113\n"
"v_lshlrev_b32 v112, 16, v98\n"
"v_mfma_f32_16x16x32_bf16 a[0:3], a[88:91], v[76:79], a[0:3]\n"
"v_and_b32 v113, s45, v98\n"
"v_mul_f32 v112, s44, v112\n"
"v_mfma_f32_16x16x32_bf16 a[4:7], a[68:71], v[64:67], a[4:7]\n"
"v_mul_f32 v113, s44, v113\n"
"v_cvt_pk_bf16_f32 v98, v112, v113\n"
"v_mfma_f32_16x16x32_bf16 a[4:7], a[76:79], v[68:71], a[4:7]\n"
"v_lshlrev_b32 v112, 16, v99\n"
"v_and_b32 v113, s45, v99\n"
"v_mfma_f32_16x16x32_bf16 a[4:7], a[84:87], v[72:75], a[4:7]\n"
"v_mul_f32 v112, s44, v112\n"
"v_mul_f32 v113, s44, v113\n"
"v_mfma_f32_16x16x32_bf16 a[4:7], a[92:95], v[76:79], a[4:7]\n"
"s_setprio 0\n"
"ds_read_b128 v[64:67], v124 offset:24576\n"
"ds_read_b128 v[68:71], v124 offset:25600\n"
"ds_read_b128 v[72:75], v124 offset:26624\n"
"ds_read_b128 v[76:79], v124 offset:27648\n"
"s_waitcnt vmcnt(8)\n"
"ds_read_b64_tr_b16 a[96:97], v125 offset:0\n"
"ds_read_b64_tr_b16 a[100:101], v125 offset:8\n"
"s_waitcnt vmcnt(7)\n"
"ds_read_b64_tr_b16 a[98:99], v125 offset:1024\n"
"ds_read_b64_tr_b16 a[102:103], v125 offset:1032\n"
"s_add_u32 s40, s51, 0x3a000\n"
"buffer_load_dwordx4 v[108:111], v124, s[20:23], s40 offen\n"
"s_waitcnt lgkmcnt(9)\n"
"v_mfma_f32_16x16x32_bf16 a[8:11], a[64:67], v[80:83], a[8:11]\n"
"s_setprio 2\n"
"v_mfma_f32_16x16x32_bf16 a[8:11], a[72:75], v[84:87], a[8:11]\n"
"v_cvt_pk_bf16_f32 v99, v112, v113\n"
"v_lshlrev_b32 v112, 16, v100\n"
"v_and_b32 v113, s45, v100\n"
"v_mfma_f32_16x16x32_bf16 a[8:11], a[80:83], v[88:91], a[8:11]\n"
"v_mul_f32 v112, s44, v112\n"
"v_mul_f32 v113, s44, v113\n"
"v_mfma_f32_16x16x32_bf16 a[8:11], a[88:91], v[92:95], a[8:11]\n"
"v_cvt_pk_bf16_f32 v100, v112, v113\n"
"v_lshlrev_b32 v112, 16, v101\n"
"v_mfma_f32_16x16x32_bf16 a[12:15], a[68:71], v[80:83], a[12:15]\n"
"v_and_b32 v113, s45, v101\n"
"v_mul_f32 v112, s44, v112\n"
"v_mfma_f32_16x16x32_bf16 a[12:15], a[76:79], v[84:87], a[12:15]\n"
"v_mul_f32 v113, s44, v113\n"
"v_cvt_pk_bf16_f32 v101, v112, v113\n"
"v_mfma_f32_16x16x32_bf16 a[12:15], a[84:87], v[88:91], a[12:15]\n"
"v_lshlrev_b32 v112, 16, v102\n"
"v_and_b32 v113, s45, v102\n"
"v_mfma_f32_16x16x32_bf16 a[12:15], a[92:95], v[92:95], a[12:15]\n"
"s_setprio 0\n"
"ds_read_b128 v[80:83], v124 offset:28672\n"
"ds_read_b128 v[84:87], v124 offset:29696\n"
"ds_read_b128 v[88:91], v124 offset:30720\n"
"ds_read_b128 v[92:95], v124 offset:31744\n"
"s_waitcnt vmcnt(7)\n"
"ds_read_b64_tr_b16 a[104:105], v125 offset:2048\n"
"ds_read_b64_tr_b16 a[108:109], v125 offset:2056\n"
"s_waitcnt vmcnt(6)\n"
"ds_read_b64_tr_b16 a[106:107], v125 offset:3072\n"
"ds_read_b64_tr_b16 a[110:111], v125 offset:3080\n"
"ds_write_b128 v126, v[96:99] offset:32768\n"
"s_waitcnt lgkmcnt(13)\n"
"v_mfma_f32_16x16x32_bf16 a[16:19], a[64:67], v[64:67], a[16:19]\n"
"s_setprio 2\n"
"v_mfma_f32_16x16x32_bf16 a[16:19], a[72:75], v[68:71], a[16:19]\n"
"v_mul_f32 v112, s44, v112\n"
"v_mul_f32 v113, s44, v113\n"
"v_mfma_f32_16x16x32_bf16 a[16:19], a[80:83], v[72:75], a[16:19]\n"
"v_cvt_pk_bf16_f32 v102, v112, v113\n"
"v_lshlrev_b32 v112, 16, v103\n"
"v_mfma_f32_16x16x32_bf16 a[16:19], a[88:91], v[76:79], a[16:19]\n"
"v_and_b32 v113, s45, v103\n"
"v_mul_f32 v112, s44, v112\n"
"v_mfma_f32_16x16x32_bf16 a[20:23], a[68:71], v[64:67], a[20:23]\n"
"v_mul_f32 v113, s44, v113\n"
"v_mfma_f32_16x16x32_bf16 a[20:23], a[76:79], v[68:71], a[20:23]\n"
"v_cvt_pk_bf16_f32 v103, v112, v113\n"
"v_mfma_f32_16x16x32_bf16 a[20:23], a[84:87], v[72:75], a[20:23]\n"
"s_waitcnt lgkmcnt(0)\n"
"s_setprio 0\n"
"v_mfma_f32_16x16x32_bf16 a[20:23], a[92:95], v[76:79], a[20:23]\n"
"s_barrier\n"
"ds_read_b128 v[64:67], v124 offset:32768\n"
"ds_read_b128 v[68:71], v124 offset:33792\n"
"ds_read_b128 v[72:75], v124 offset:34816\n"
"ds_read_b128 v[76:79], v124 offset:35840\n"
"s_waitcnt vmcnt(5)\n"
"ds_read_b64_tr_b16 a[112:113], v125 offset:4096\n"
"ds_read_b64_tr_b16 a[116:117], v125 offset:4104\n"
"s_waitcnt vmcnt(4)\n"
"ds_read_b64_tr_b16 a[114:115], v125 offset:5120\n"
"ds_read_b64_tr_b16 a[118:119], v125 offset:5128\n"
"ds_write_b128 v126, v[100:103] offset:36864\n"
"s_add_u32 s40, s51, 0x3b000\n"
"buffer_load_dwordx4 v[96:99], v124, s[20:23], s40 offen\n"
"v_mfma_f32_16x16x32_bf16 a[24:27], a[64:67], v[80:83], a[24:27]\n"
"s_setprio 2\n"
"v_mfma_f32_16x16x32_bf16 a[24:27], a[72:75], v[84:87], a[24:27]\n"
"v_mfma_f32_16x16x32_bf16 a[24:27], a[80:83], v[88:91], a[24:27]\n"
"v_mfma_f32_16x16x32_bf16 a[24:27], a[88:91], v[92:95], a[24:27]\n"
"v_mfma_f32_16x16x32_bf16 a[28:31], a[68:71], v[80:83], a[28:31]\n"
"v_mfma_f32_16x16x32_bf16 a[28:31], a[76:79], v[84:87], a[28:31]\n"
"v_mfma_f32_16x16x32_bf16 a[28:31], a[84:87], v[88:91], a[28:31]\n"
"v_mfma_f32_16x16x32_bf16 a[28:31], a[92:95], v[92:95], a[28:31]\n"
"s_setprio 0\n"
"ds_read_b128 v[80:83], v124 offset:0\n"
"ds_read_b128 v[84:87], v124 offset:1024\n"
"ds_read_b128 v[88:91], v124 offset:2048\n"
"ds_read_b128 v[92:95], v124 offset:3072\n"
"s_waitcnt vmcnt(4)\n"
"ds_read_b64_tr_b16 a[120:121], v125 offset:6144\n"
"ds_read_b64_tr_b16 a[124:125], v125 offset:6152\n"
"s_waitcnt vmcnt(3)\n"
"ds_read_b64_tr_b16 a[122:123], v125 offset:7168\n"
"ds_read_b64_tr_b16 a[126:127], v125 offset:7176\n"
"s_waitcnt lgkmcnt(13)\n"
"v_mfma_f32_16x16x32_bf16 v[0:3], a[96:99], v[64:67], v[0:3]\n"
"s_setprio 2\n"
"v_mfma_f32_16x16x32_bf16 v[0:3], a[104:107], v[68:71], v[0:3]\n"
"s_waitcnt vmcnt(2)\n"
"v_lshlrev_b32 v112, 16, v104\n"
"v_and_b32 v113, s45, v104\n"
"v_mul_f32 v112, s44, v112\n"
"s_waitcnt lgkmcnt(10)\n"
"v_mfma_f32_16x16x32_bf16 v[0:3], a[112:115], v[72:75], v[0:3]\n"
"v_mul_f32 v113, s44, v113\n"
"v_cvt_pk_bf16_f32 v104, v112, v113\n"
"v_lshlrev_b32 v112, 16, v105\n"
"s_waitcnt lgkmcnt(1)\n"
"v_mfma_f32_16x16x32_bf16 v[0:3], a[120:123], v[76:79], v[0:3]\n"
"v_and_b32 v113, s45, v105\n"
"v_mul_f32 v112, s44, v112\n"
"v_mfma_f32_16x16x32_bf16 v[4:7], a[100:103], v[64:67], v[4:7]\n"
"v_mul_f32 v113, s44, v113\n"
"v_cvt_pk_bf16_f32 v105, v112, v113\n"
"v_mfma_f32_16x16x32_bf16 v[4:7], a[108:111], v[68:71], v[4:7]\n"
"v_lshlrev_b32 v112, 16, v106\n"
"v_and_b32 v113, s45, v106\n"
"v_mfma_f32_16x16x32_bf16 v[4:7], a[116:119], v[72:75], v[4:7]\n"
"v_mul_f32 v112, s44, v112\n"
"v_mul_f32 v113, s44, v113\n"
"s_waitcnt lgkmcnt(0)\n"
"v_mfma_f32_16x16x32_bf16 v[4:7], a[124:127], v[76:79], v[4:7]\n"
"s_setprio 0\n"
"ds_read_b128 v[64:67], v124 offset:4096\n"
"ds_read_b128 v[68:71], v124 offset:5120\n"
"ds_read_b128 v[72:75], v124 offset:6144\n"
"ds_read_b128 v[76:79], v124 offset:7168\n"
"v_mfma_f32_16x16x32_bf16 v[8:11], a[96:99], v[80:83], v[8:11]\n"
"s_setprio 2\n"
"v_mfma_f32_16x16x32_bf16 v[8:11], a[104:107], v[84:87], v[8:11]\n"
"v_cvt_pk_bf16_f32 v106, v112, v113\n"
"v_lshlrev_b32 v112, 16, v107\n"
"v_mfma_f32_16x16x32_bf16 v[8:11], a[112:115], v[88:91], v[8:11]\n"
"v_and_b32 v113, s45, v107\n"
"v_mfma_f32_16x16x32_bf16 v[8:11], a[120:123], v[92:95], v[8:11]\n"
"v_mul_f32 v112, s44, v112\n"
"v_mfma_f32_16x16x32_bf16 v[12:15], a[100:103], v[80:83], v[12:15]\n"
"v_mul_f32 v113, s44, v113\n"
"v_mfma_f32_16x16x32_bf16 v[12:15], a[108:111], v[84:87], v[12:15]\n"
"v_cvt_pk_bf16_f32 v107, v112, v113\n"
"v_mfma_f32_16x16x32_bf16 v[12:15], a[116:119], v[88:91], v[12:15]\n"
"v_mfma_f32_16x16x32_bf16 v[12:15], a[124:127], v[92:95], v[12:15]\n"
"s_setprio 0\n"
"ds_read_b128 v[80:83], v124 offset:8192\n"
"ds_read_b128 v[84:87], v124 offset:9216\n"
"ds_read_b128 v[88:91], v124 offset:10240\n"
"ds_read_b128 v[92:95], v124 offset:11264\n"
"ds_write_b128 v126, v[104:107] offset:40960\n"
"s_waitcnt lgkmcnt(5)\n"
"v_mfma_f32_16x16x32_bf16 v[16:19], a[96:99], v[64:67], v[16:19]\n"
"s_setprio 2\n"
"v_mfma_f32_16x16x32_bf16 v[16:19], a[104:107], v[68:71], v[16:19]\n"
"v_mfma_f32_16x16x32_bf16 v[16:19], a[112:115], v[72:75], v[16:19]\n"
"v_mfma_f32_16x16x32_bf16 v[16:19], a[120:123], v[76:79], v[16:19]\n"
"v_mfma_f32_16x16x32_bf16 v[20:23], a[100:103], v[64:67], v[20:23]\n"
"v_mfma_f32_16x16x32_bf16 v[20:23], a[108:111], v[68:71], v[20:23]\n"
"v_mfma_f32_16x16x32_bf16 v[20:23], a[116:119], v[72:75], v[20:23]\n"
"s_waitcnt lgkmcnt(0)\n"
"s_setprio 0\n"
"v_mfma_f32_16x16x32_bf16 v[20:23], a[124:127], v[76:79], v[20:23]\n"
"s_barrier\n"
"ds_read_b128 v[64:67], v124 offset:12288\n"
"ds_read_b128 v[68:71], v124 offset:13312\n"
"ds_read_b128 v[72:75], v124 offset:14336\n"
"ds_read_b128 v[76:79], v124 offset:15360\n"
"s_add_u32 s40, s51, 0x3c000\n"
"buffer_load_dwordx4 v[100:103], v124, s[20:23], s40 offen\n"
"v_mfma_f32_16x16x32_bf16 v[24:27], a[96:99], v[80:83], v[24:27]\n"
"s_setprio 2\n"
"v_mfma_f32_16x16x32_bf16 v[24:27], a[104:107], v[84:87], v[24:27]\n"
"v_mfma_f32_16x16x32_bf16 v[24:27], a[112:115], v[88:91], v[24:27]\n"
"v_mfma_f32_16x16x32_bf16 v[24:27], a[120:123], v[92:95], v[24:27]\n"
"v_mfma_f32_16x16x32_bf16 v[28:31], a[100:103], v[80:83], v[28:31]\n"
"v_mfma_f32_16x16x32_bf16 v[28:31], a[108:111], v[84:87], v[28:31]\n"
"v_mfma_f32_16x16x32_bf16 v[28:31], a[116:119], v[88:91], v[28:31]\n"
"v_mfma_f32_16x16x32_bf16 v[28:31], a[124:127], v[92:95], v[28:31]\n"
"s_setprio 0\n"
"ds_read_b128 v[80:83], v124 offset:16384\n"
"ds_read_b128 v[84:87], v124 offset:17408\n"
"ds_read_b128 v[88:91], v124 offset:18432\n"
"ds_read_b128 v[92:95], v124 offset:19456\n"
"s_waitcnt lgkmcnt(4)\n"
"v_mfma_f32_16x16x32_bf16 a[0:3], a[96:99], v[64:67], a[0:3]\n"
"s_setprio 2\n"
"v_mfma_f32_16x16x32_bf16 a[0:3], a[104:107], v[68:71], a[0:3]\n"
"v_mfma_f32_16x16x32_bf16 a[0:3], a[112:115], v[72:75], a[0:3]\n"
"v_mfma_f32_16x16x32_bf16 a[0:3], a[120:123], v[76:79], a[0:3]\n"
"v_mfma_f32_16x16x32_bf16 a[4:7], a[100:103], v[64:67], a[4:7]\n"
"v_mfma_f32_16x16x32_bf16 a[4:7], a[108:111], v[68:71], a[4:7]\n"
"v_mfma_f32_16x16x32_bf16 a[4:7], a[116:119], v[72:75], a[4:7]\n"
"v_mfma_f32_16x16x32_bf16 a[4:7], a[124:127], v[76:79], a[4:7]\n"
"s_setprio 0\n"
"ds_read_b128 v[64:67], v124 offset:20480\n"
"ds_read_b128 v[68:71], v124 offset:21504\n"
"ds_read_b128 v[72:75], v124 offset:22528\n"
"ds_read_b128 v[76:79], v124 offset:23552\n"
"ds_read_b128 v[114:117], v124 offset:38912\n"
"s_waitcnt lgkmcnt(5)\n"
"v_mfma_f32_16x16x32_bf16 a[8:11], a[96:99], v[80:83], a[8:11]\n"
"s_setprio 2\n"
"v_mfma_f32_16x16x32_bf16 a[8:11], a[104:107], v[84:87], a[8:11]\n"
"s_waitcnt lgkmcnt(0)\n"
"v_add_f32 v114, v114, v115\n"
"v_add_f32 v116, v116, v117\n"
"v_add_f32 v114, v114, v116\n"
"v_mfma_f32_16x16x32_bf16 a[8:11], a[112:115], v[88:91], a[8:11]\n"
"v_rcp_f32 v120, v114\n"
"v_subrev_u32 v118, s46, v114\n"
"v_max_u32 v123, v123, v118\n"
"v_mfma_f32_16x16x32_bf16 a[8:11], a[120:123], v[92:95], a[8:11]\n"
"v_mul_f32 v16, v120, v16\n"
"v_mul_f32 v17, v120, v17\n"
"v_mul_f32 v18, v120, v18\n"
"v_mfma_f32_16x16x32_bf16 a[12:15], a[100:103], v[80:83], a[12:15]\n"
"v_mul_f32 v19, v120, v19\n"
"v_mul_f32 v20, v120, v20\n"
"v_mfma_f32_16x16x32_bf16 a[12:15], a[108:111], v[84:87], a[12:15]\n"
"v_mul_f32 v21, v120, v21\n"
"v_mul_f32 v22, v120, v22\n"
"v_mfma_f32_16x16x32_bf16 a[12:15], a[116:119], v[88:91], a[12:15]\n"
"v_mul_f32 v23, v120, v23\n"
"v_fmac_f32 v122, v16, v17\n"
"v_mfma_f32_16x16x32_bf16 a[12:15], a[124:127], v[92:95], a[12:15]\n"
"s_setprio 0\n"
"ds_read_b128 v[80:83], v124 offset:24576\n"
"ds_read_b128 v[84:87], v124 offset:25600\n"
"ds_read_b128 v[88:91], v124 offset:26624\n"
"ds_read_b128 v[92:95], v124 offset:27648\n"
"v_mfma_f32_16x16x32_bf16 a[16:19], a[96:99], v[64:67], a[16:19]\n"
"s_setprio 2\n"
"v_mfma_f32_16x16x32_bf16 a[16:19], a[104:107], v[68:71], a[16:19]\n"
"v_fmac_f32 v122, v18, v19\n"
"v_fmac_f32 v122, v20, v21\n"
"v_fmac_f32 v122, v22, v23\n"
"v_mfma_f32_16x16x32_bf16 a[16:19], a[112:115], v[72:75], a[16:19]\n"
"v_cvt_pk_bf16_f32 v16, v16, v17\n"
"v_cvt_pk_bf16_f32 v17, v18, v19\n"
"v_cvt_pk_bf16_f32 v18, v20, v21\n"
"v_mfma_f32_16x16x32_bf16 a[16:19], a[120:123], v[76:79], a[16:19]\n"
"v_cvt_pk_bf16_f32 v19, v22, v23\n"
"s_waitcnt vmcnt(2)\n"
"v_lshlrev_b32 v112, 16, v108\n"
"v_and_b32 v113, s45, v108\n"
"v_mfma_f32_16x16x32_bf16 a[20:23], a[100:103], v[64:67], a[20:23]\n"
"v_mul_f32 v112, s44, v112\n"
"v_mul_f32 v113, s44, v113\n"
"v_mfma_f32_16x16x32_bf16 a[20:23], a[108:111], v[68:71], a[20:23]\n"
"v_cvt_pk_bf16_f32 v108, v112, v113\n"
"v_lshlrev_b32 v112, 16, v109\n"
"v_mfma_f32_16x16x32_bf16 a[20:23], a[116:119], v[72:75], a[20:23]\n"
"v_and_b32 v113, s45, v109\n"
"v_mul_f32 v112, s44, v112\n"
"s_waitcnt lgkmcnt(0)\n"
"s_setprio 0\n"
"v_mfma_f32_16x16x32_bf16 a[20:23], a[124:127], v[76:79], a[20:23]\n"
"s_barrier\n"
"ds_read_b128 v[64:67], v124 offset:28672\n"
"ds_read_b128 v[68:71], v124 offset:29696\n"
"ds_read_b128 v[72:75], v124 offset:30720\n"
"ds_read_b128 v[76:79], v124 offset:31744\n"
"s_add_u32 s41, s52, 0x30000\n"
"buffer_store_dwordx4 v[16:19], v127, s[64:67], s41 offen sc1\n"
"s_add_u32 s40, s51, 0x3d000\n"
"buffer_load_dwordx4 v[104:107], v124, s[20:23], s40 offen\n"
"v_mfma_f32_16x16x32_bf16 a[24:27], a[96:99], v[80:83], a[24:27]\n"
"s_setprio 2\n"
"v_mfma_f32_16x16x32_bf16 a[24:27], a[104:107], v[84:87], a[24:27]\n"
"v_mul_f32 v113, s44, v113\n"
"v_cvt_pk_bf16_f32 v109, v112, v113\n"
"v_mfma_f32_16x16x32_bf16 a[24:27], a[112:115], v[88:91], a[24:27]\n"
"v_lshlrev_b32 v112, 16, v110\n"
"v_and_b32 v113, s45, v110\n"
"v_mfma_f32_16x16x32_bf16 a[24:27], a[120:123], v[92:95], a[24:27]\n"
"v_mul_f32 v112, s44, v112\n"
"v_mul_f32 v113, s44, v113\n"
"v_mfma_f32_16x16x32_bf16 a[28:31], a[100:103], v[80:83], a[28:31]\n"
"v_cvt_pk_bf16_f32 v110, v112, v113\n"
"v_lshlrev_b32 v112, 16, v111\n"
"v_mfma_f32_16x16x32_bf16 a[28:31], a[108:111], v[84:87], a[28:31]\n"
"v_and_b32 v113, s45, v111\n"
"v_mul_f32 v112, s44, v112\n"
"v_mfma_f32_16x16x32_bf16 a[28:31], a[116:119], v[88:91], a[28:31]\n"
"v_mul_f32 v113, s44, v113\n"
"v_cvt_pk_bf16_f32 v111, v112, v113\n"
"v_mfma_f32_16x16x32_bf16 a[28:31], a[124:127], v[92:95], a[28:31]\n"
"s_setprio 0\n"
"ds_read_b128 v[80:83], v124 offset:32768\n"
"ds_read_b128 v[84:87], v124 offset:33792\n"
"ds_read_b128 v[88:91], v124 offset:34816\n"
"ds_read_b128 v[92:95], v124 offset:35840\n"
"ds_write_b128 v126, v[108:111] offset:45056\n"
"s_waitcnt lgkmcnt(5)\n"
"v_mfma_f32_16x16x32_bf16 v[16:19], v[32:35], v[64:67], 0\n"
"s_setprio 2\n"
"v_mfma_f32_16x16x32_bf16 v[16:19], v[40:43], v[68:71], v[16:19]\n"
"s_waitcnt vmcnt(3)\n"
"v_lshlrev_b32 v112, 16, v96\n"
"v_and_b32 v113, s45, v96\n"
"v_mul_f32 v112, s44, v112\n"
"v_mfma_f32_16x16x32_bf16 v[16:19], v[48:51], v[72:75], v[16:19]\n"
"v_mul_f32 v113, s44, v113\n"
"v_cvt_pk_bf16_f32 v96, v112, v113\n"
"v_lshlrev_b32 v112, 16, v97\n"
"v_mfma_f32_16x16x32_bf16 v[16:19], v[56:59], v[76:79], v[16:19]\n"
"v_and_b32 v113, s45, v97\n"
"v_mul_f32 v112, s44, v112\n"
"v_mul_f32 v113, s44, v113\n"
"v_mfma_f32_16x16x32_bf16 v[20:23], v[36:39], v[64:67], 0\n"
"v_cvt_pk_bf16_f32 v97, v112, v113\n"
"v_lshlrev_b32 v112, 16, v98\n"
"v_mfma_f32_16x16x32_bf16 v[20:23], v[44:47], v[68:71], v[20:23]\n"
"v_and_b32 v113, s45, v98\n"
"v_mul_f32 v112, s44, v112\n"
"v_mfma_f32_16x16x32_bf16 v[20:23], v[52:55], v[72:75], v[20:23]\n"
"v_mul_f32 v113, s44, v113\n"
"v_cvt_pk_bf16_f32 v98, v112, v113\n"
"v_mfma_f32_16x16x32_bf16 v[20:23], v[60:63], v[76:79], v[20:23]\n"
"s_setprio 0\n"
"ds_read_b128 v[64:67], v124 offset:0\n"
"ds_read_b128 v[68:71], v124 offset:1024\n"
"ds_read_b128 v[72:75], v124 offset:2048\n"
"ds_read_b128 v[76:79], v124 offset:3072\n"
"ds_read_b128 v[114:117], v124 offset:39936\n"
"s_waitcnt lgkmcnt(6)\n"
"v_mfma_f32_16x16x32_bf16 v[16:19], a[32:35], v[80:83], v[16:19]\n"
"s_setprio 2\n"
"v_mfma_f32_16x16x32_bf16 v[16:19], a[40:43], v[84:87], v[16:19]\n"
"s_waitcnt lgkmcnt(0)\n"
"v_add_f32 v114, v114, v115\n"
"v_add_f32 v116, v116, v117\n"
"v_add_f32 v114, v114, v116\n"
"v_mfma_f32_16x16x32_bf16 v[16:19], a[48:51], v[88:91], v[16:19]\n"
"v_rcp_f32 v120, v114\n"
"v_subrev_u32 v118, s46, v114\n"
"v_max_u32 v123, v123, v118\n"
"v_mfma_f32_16x16x32_bf16 v[16:19], a[56:59], v[92:95], v[16:19]\n"
"v_mul_f32 v24, v120, v24\n"
"v_mul_f32 v25, v120, v25\n"
"v_mul_f32 v26, v120, v26\n"
"v_mfma_f32_16x16x32_bf16 v[20:23], a[36:39], v[80:83], v[20:23]\n"
"v_mul_f32 v27, v120, v27\n"
"v_mul_f32 v28, v120, v28\n"
"v_mfma_f32_16x16x32_bf16 v[20:23], a[44:47], v[84:87], v[20:23]\n"
"v_mul_f32 v29, v120, v29\n"
"v_mul_f32 v30, v120, v30\n"
"v_mfma_f32_16x16x32_bf16 v[20:23], a[52:55], v[88:91], v[20:23]\n"
"v_mul_f32 v31, v120, v31\n"
"v_fmac_f32 v122, v24, v25\n"
"v_mfma_f32_16x16x32_bf16 v[20:23], a[60:63], v[92:95], v[20:23]\n"
"s_setprio 0\n"
"ds_read_b128 v[80:83], v124 offset:4096\n"
"ds_read_b128 v[84:87], v124 offset:5120\n"
"ds_read_b128 v[88:91], v124 offset:6144\n"
"ds_read_b128 v[92:95], v124 offset:7168\n"
"v_mfma_f32_16x16x32_bf16 v[16:19], a[64:67], v[64:67], v[16:19]\n"
"s_setprio 2\n"
"v_mfma_f32_16x16x32_bf16 v[16:19], a[72:75], v[68:71], v[16:19]\n"
"v_fmac_f32 v122, v26, v27\n"
"v_fmac_f32 v122, v28, v29\n"
"v_mfma_f32_16x16x32_bf16 v[16:19], a[80:83], v[72:75], v[16:19]\n"
"v_fmac_f32 v122, v30, v31\n"
"v_cvt_pk_bf16_f32 v24, v24, v25\n"
"v_mfma_f32_16x16x32_bf16 v[16:19], a[88:91], v[76:79], v[16:19]\n"
"v_cvt_pk_bf16_f32 v25, v26, v27\n"
"v_cvt_pk_bf16_f32 v26, v28, v29\n"
"v_mfma_f32_16x16x32_bf16 v[20:23], a[68:71], v[64:67], v[20:23]\n"
"v_cvt_pk_bf16_f32 v27, v30, v31\n"
"v_lshlrev_b32 v112, 16, v99\n"
"v_mfma_f32_16x16x32_bf16 v[20:23], a[76:79], v[68:71], v[20:23]\n"
"v_and_b32 v113, s45, v99\n"
"v_mul_f32 v112, s44, v112\n"
"v_mfma_f32_16x16x32_bf16 v[20:23], a[84:87], v[72:75], v[20:23]\n"
"v_mul_f32 v113, s44, v113\n"
"v_cvt_pk_bf16_f32 v99, v112, v113\n"
"s_waitcnt lgkmcnt(0)\n"
"s_setprio 0\n"
"v_mfma_f32_16x16x32_bf16 v[20:23], a[92:95], v[76:79], v[20:23]\n"
"s_barrier\n"
"ds_read_b128 v[64:67], v124 offset:8192\n"
"ds_read_b128 v[68:71], v124 offset:9216\n"
"ds_read_b128 v[72:75], v124 offset:10240\n"
"ds_read_b128 v[76:79], v124 offset:11264\n"
"s_add_u32 s41, s52, 0x31000\n"
"buffer_store_dwordx4 v[24:27], v127, s[64:67], s41 offen sc1\n"
"ds_write_b128 v126, v[96:99] offset:32768\n"
"s_add_u32 s40, s51, 0x3e000\n"
"buffer_load_dwordx4 v[108:111], v124, s[20:23], s40 offen\n"
"v_mfma_f32_16x16x32_bf16 v[16:19], a[96:99], v[80:83], v[16:19]\n"
"s_setprio 2\n"
"v_mfma_f32_16x16x32_bf16 v[16:19], a[104:107], v[84:87], v[16:19]\n"
"v_mfma_f32_16x16x32_bf16 v[16:19], a[112:115], v[88:91], v[16:19]\n"
"v_mfma_f32_16x16x32_bf16 v[16:19], a[120:123], v[92:95], v[16:19]\n"
"v_mfma_f32_16x16x32_bf16 v[20:23], a[100:103], v[80:83], v[20:23]\n"
"v_mfma_f32_16x16x32_bf16 v[20:23], a[108:111], v[84:87], v[20:23]\n"
"v_mfma_f32_16x16x32_bf16 v[20:23], a[116:119], v[88:91], v[20:23]\n"
"v_mfma_f32_16x16x32_bf16 v[20:23], a[124:127], v[92:95], v[20:23]\n"
"s_setprio 0\n"
"ds_read_b128 v[80:83], v124 offset:12288\n"
"ds_read_b128 v[84:87], v124 offset:13312\n"
"ds_read_b128 v[88:91], v124 offset:14336\n"
"ds_read_b128 v[92:95], v124 offset:15360\n"
"ds_read_b128 v[114:117], v124 offset:46080\n"
"s_waitcnt lgkmcnt(6)\n"
"v_mfma_f32_16x16x32_bf16 v[24:27], v[32:35], v[64:67], 0\n"
"s_setprio 2\n"
"v_mfma_f32_16x16x32_bf16 v[24:27], v[40:43], v[68:71], v[24:27]\n"
"s_waitcnt lgkmcnt(0)\n"
"v_add_f32 v114, v114, v115\n"
"v_add_f32 v116, v116, v117\n"
"v_add_f32 v114, v114, v116\n"
"v_mfma_f32_16x16x32_bf16 v[24:27], v[48:51], v[72:75], v[24:27]\n"
"v_rcp_f32 v119, v114\n"
"v_subrev_u32 v118, s46, v114\n"
"v_max_u32 v123, v123, v118\n"
"v_mfma_f32_16x16x32_bf16 v[24:27], v[56:59], v[76:79], v[24:27]\n"
"v_mul_f32 v16, v119, v16\n"
"v_mul_f32 v17, v119, v17\n"
"v_mul_f32 v18, v119, v18\n"
"v_mfma_f32_16x16x32_bf16 v[28:31], v[36:39], v[64:67], 0\n"
"v_mul_f32 v19, v119, v19\n"
"v_mul_f32 v20, v119, v20\n"
"v_mul_f32 v21, v119, v21\n"
"v_mfma_f32_16x16x32_bf16 v[28:31], v[44:47], v[68:71], v[28:31]\n"
"v_mul_f32 v22, v119, v22\n"
"v_mul_f32 v23, v119, v23\n"
"v_mfma_f32_16x16x32_bf16 v[28:31], v[52:55], v[72:75], v[28:31]\n"
"v_fmac_f32 v122, v16, v17\n"
"v_fmac_f32 v122, v18, v19\n"
"v_mfma_f32_16x16x32_bf16 v[28:31], v[60:63], v[76:79], v[28:31]\n"
"s_setprio 0\n"
"ds_read_b128 v[64:67], v124 offset:16384\n"
"ds_read_b128 v[68:71], v124 offset:17408\n"
"ds_read_b128 v[72:75], v124 offset:18432\n"
"ds_read_b128 v[76:79], v124 offset:19456\n"
"ds_read_b128 v[114:117], v124 offset:36864\n"
"v_mfma_f32_16x16x32_bf16 v[24:27], a[32:35], v[80:83], v[24:27]\n"
"s_setprio 2\n"
"v_mfma_f32_16x16x32_bf16 v[24:27], a[40:43], v[84:87], v[24:27]\n"
"s_waitcnt lgkmcnt(0)\n"
"v_add_f32 v114, v114, v115\n"
"v_add_f32 v116, v116, v117\n"
"v_add_f32 v114, v114, v116\n"
"v_mfma_f32_16x16x32_bf16 v[24:27], a[48:51], v[88:91], v[24:27]\n"
"v_rcp_f32 v120, v114\n"
"v_subrev_u32 v118, s46, v114\n"
"v_max_u32 v123, v123, v118\n"
"v_mfma_f32_16x16x32_bf16 v[24:27], a[56:59], v[92:95], v[24:27]\n"
"v_fmac_f32 v122, v20, v21\n"
"v_fmac_f32 v122, v22, v23\n"
"v_cvt_pk_bf16_f32 v16, v16, v17\n"
"v_mfma_f32_16x16x32_bf16 v[28:31], a[36:39], v[80:83], v[28:31]\n"
"v_cvt_pk_bf16_f32 v17, v18, v19\n"
"v_cvt_pk_bf16_f32 v18, v20, v21\n"
"v_cvt_pk_bf16_f32 v19, v22, v23\n"
"v_mfma_f32_16x16x32_bf16 v[28:31], a[44:47], v[84:87], v[28:31]\n"
"v_mul_f32 v0, v120, v0\n"
"v_mul_f32 v1, v120, v1\n"
"v_mfma_f32_16x16x32_bf16 v[28:31], a[52:55], v[88:91], v[28:31]\n"
"v_mul_f32 v2, v120, v2\n"
"v_mul_f32 v3, v120, v3\n"
"v_mfma_f32_16x16x32_bf16 v[28:31], a[60:63], v[92:95], v[28:31]\n"
"s_setprio 0\n"
"ds_read_b128 v[80:83], v124 offset:20480\n"
"ds_read_b128 v[84:87], v124 offset:21504\n"
"ds_read_b128 v[88:91], v124 offset:22528\n"
"ds_read_b128 v[92:95], v124 offset:23552\n"
"s_add_u32 s41, s52, 0x36000\n"
"buffer_store_dwordx4 v[16:19], v127, s[64:67], s41 offen sc1\n"
"v_mfma_f32_16x16x32_bf16 v[24:27], a[64:67], v[64:67], v[24:27]\n"
"s_setprio 2\n"
"v_mfma_f32_16x16x32_bf16 v[24:27], a[72:75], v[68:71], v[24:27]\n"
"v_mul_f32 v4, v120, v4\n"
"v_mul_f32 v5, v120, v5\n"
"v_mul_f32 v6, v120, v6\n"
"v_mfma_f32_16x16x32_bf16 v[24:27], a[80:83], v[72:75], v[24:27]\n"
"v_mul_f32 v7, v120, v7\n"
"v_fmac_f32 v122, v0, v1\n"
"v_fmac_f32 v122, v2, v3\n"
"v_mfma_f32_16x16x32_bf16 v[24:27], a[88:91], v[76:79], v[24:27]\n"
"v_fmac_f32 v122, v4, v5\n"
"v_fmac_f32 v122, v6, v7\n"
"v_cvt_pk_bf16_f32 v0, v0, v1\n"
"v_mfma_f32_16x16x32_bf16 v[28:31], a[68:71], v[64:67], v[28:31]\n"
"v_cvt_pk_bf16_f32 v1, v2, v3\n"
"v_cvt_pk_bf16_f32 v2, v4, v5\n"
"v_cvt_pk_bf16_f32 v3, v6, v7\n"
"v_mfma_f32_16x16x32_bf16 v[28:31], a[76:79], v[68:71], v[28:31]\n"
"s_waitcnt vmcnt(5)\n"
"v_lshlrev_b32 v112, 16, v100\n"
"v_and_b32 v113, s45, v100\n"
"v_mfma_f32_16x16x32_bf16 v[28:31], a[84:87], v[72:75], v[28:31]\n"
"v_mul_f32 v112, s44, v112\n"
"v_mul_f32 v113, s44, v113\n"
"s_waitcnt lgkmcnt(0)\n"
"s_setprio 0\n"
"v_mfma_f32_16x16x32_bf16 v[28:31], a[92:95], v[76:79], v[28:31]\n"
"s_barrier\n"
"ds_read_b128 v[64:67], v124 offset:24576\n"
"ds_read_b128 v[68:71], v124 offset:25600\n"
"ds_read_b128 v[72:75], v124 offset:26624\n"
"ds_read_b128 v[76:79], v124 offset:27648\n"
"s_add_u32 s41, s52, 0x2e000\n"
"buffer_store_dwordx4 v[0:3], v127, s[64:67], s41 offen sc1\n"
"s_add_u32 s40, s51, 0x3f000\n"
"buffer_load_dwordx4 v[96:99], v124, s[20:23], s40 offen\n"
"v_mfma_f32_16x16x32_bf16 v[24:27], a[96:99], v[80:83], v[24:27]\n"
"s_setprio 2\n"
"v_mfma_f32_16x16x32_bf16 v[24:27], a[104:107], v[84:87], v[24:27]\n"
"v_cvt_pk_bf16_f32 v100, v112, v113\n"
"v_lshlrev_b32 v112, 16, v101\n"
"v_and_b32 v113, s45, v101\n"
"v_mfma_f32_16x16x32_bf16 v[24:27], a[112:115], v[88:91], v[24:27]\n"
"v_mul_f32 v112, s44, v112\n"
"v_mul_f32 v113, s44, v113\n"
"v_cvt_pk_bf16_f32 v101, v112, v113\n"
"v_mfma_f32_16x16x32_bf16 v[24:27], a[120:123], v[92:95], v[24:27]\n"
"v_lshlrev_b32 v112, 16, v102\n"
"v_and_b32 v113, s45, v102\n"
"v_mul_f32 v112, s44, v112\n"
"v_mfma_f32_16x16x32_bf16 v[28:31], a[100:103], v[80:83], v[28:31]\n"
"v_mul_f32 v113, s44, v113\n"
"v_cvt_pk_bf16_f32 v102, v112, v113\n"
"v_lshlrev_b32 v112, 16, v103\n"
"v_mfma_f32_16x16x32_bf16 v[28:31], a[108:111], v[84:87], v[28:31]\n"
"v_and_b32 v113, s45, v103\n"
"v_mul_f32 v112, s44, v112\n"
"v_mfma_f32_16x16x32_bf16 v[28:31], a[116:119], v[88:91], v[28:31]\n"
"v_mul_f32 v113, s44, v113\n"
"v_cvt_pk_bf16_f32 v103, v112, v113\n"
"v_mfma_f32_16x16x32_bf16 v[28:31], a[124:127], v[92:95], v[28:31]\n"
"s_setprio 0\n"
"ds_read_b128 v[80:83], v124 offset:28672\n"
"ds_read_b128 v[84:87], v124 offset:29696\n"
"ds_read_b128 v[88:91], v124 offset:30720\n"
"ds_read_b128 v[92:95], v124 offset:31744\n"
"ds_read_b128 v[114:117], v124 offset:47104\n"
"ds_write_b128 v126, v[100:103] offset:36864\n"
"s_waitcnt lgkmcnt(6)\n"
"v_mfma_f32_16x16x32_bf16 v[16:19], v[32:35], v[64:67], 0\n"
"s_setprio 2\n"
"v_mfma_f32_16x16x32_bf16 v[16:19], v[40:43], v[68:71], v[16:19]\n"
"s_waitcnt lgkmcnt(1)\n"
"v_add_f32 v114, v114, v115\n"
"v_add_f32 v116, v116, v117\n"
"v_add_f32 v114, v114, v116\n"
"v_mfma_f32_16x16x32_bf16 v[16:19], v[48:51], v[72:75], v[16:19]\n"
"v_rcp_f32 v119, v114\n"
"v_subrev_u32 v118, s46, v114\n"
"v_max_u32 v123, v123, v118\n"
"v_mfma_f32_16x16x32_bf16 v[16:19], v[56:59], v[76:79], v[16:19]\n"
"v_mul_f32 v24, v119, v24\n"
"v_mul_f32 v25, v119, v25\n"
"v_mul_f32 v26, v119, v26\n"
"v_mfma_f32_16x16x32_bf16 v[20:23], v[36:39], v[64:67], 0\n"
"v_mul_f32 v27, v119, v27\n"
"v_mul_f32 v28, v119, v28\n"
"v_mul_f32 v29, v119, v29\n"
"v_mfma_f32_16x16x32_bf16 v[20:23], v[44:47], v[68:71], v[20:23]\n"
"v_mul_f32 v30, v119, v30\n"
"v_mul_f32 v31, v119, v31\n"
"v_mfma_f32_16x16x32_bf16 v[20:23], v[52:55], v[72:75], v[20:23]\n"
"v_fmac_f32 v122, v24, v25\n"
"v_fmac_f32 v122, v26, v27\n"
"v_mfma_f32_16x16x32_bf16 v[20:23], v[60:63], v[76:79], v[20:23]\n"
"s_setprio 0\n"
"ds_read_b128 v[64:67], v124 offset:32768\n"
"ds_read_b128 v[68:71], v124 offset:33792\n"
"ds_read_b128 v[72:75], v124 offset:34816\n"
"ds_read_b128 v[76:79], v124 offset:35840\n"
"ds_read_b128 v[114:117], v124 offset:37888\n"
"v_mfma_f32_16x16x32_bf16 v[16:19], a[32:35], v[80:83], v[16:19]\n"
"s_setprio 2\n"
"v_mfma_f32_16x16x32_bf16 v[16:19], a[40:43], v[84:87], v[16:19]\n"
"s_waitcnt lgkmcnt(0)\n"
"v_add_f32 v114, v114, v115\n"
"v_add_f32 v116, v116, v117\n"
"v_add_f32 v114, v114, v116\n"
"v_mfma_f32_16x16x32_bf16 v[16:19], a[48:51], v[88:91], v[16:19]\n"
"v_rcp_f32 v120, v114\n"
"v_subrev_u32 v118, s46, v114\n"
"v_max_u32 v123, v123, v118\n"
"v_mfma_f32_16x16x32_bf16 v[16:19], a[56:59], v[92:95], v[16:19]\n"
"v_fmac_f32 v122, v28, v29\n"
"v_fmac_f32 v122, v30, v31\n"
"v_cvt_pk_bf16_f32 v24, v24, v25\n"
"v_mfma_f32_16x16x32_bf16 v[20:23], a[36:39], v[80:83], v[20:23]\n"
"v_cvt_pk_bf16_f32 v25, v26, v27\n"
"v_cvt_pk_bf16_f32 v26, v28, v29\n"
"v_cvt_pk_bf16_f32 v27, v30, v31\n"
"v_mfma_f32_16x16x32_bf16 v[20:23], a[44:47], v[84:87], v[20:23]\n"
"v_mul_f32 v8, v120, v8\n"
"v_mul_f32 v9, v120, v9\n"
"v_mfma_f32_16x16x32_bf16 v[20:23], a[52:55], v[88:91], v[20:23]\n"
"v_mul_f32 v10, v120, v10\n"
"v_mul_f32 v11, v120, v11\n"
"v_mfma_f32_16x16x32_bf16 v[20:23], a[60:63], v[92:95], v[20:23]\n"
"s_setprio 0\n"
"ds_read_b128 v[80:83], v124 offset:0\n"
"ds_read_b128 v[84:87], v124 offset:1024\n"
"ds_read_b128 v[88:91], v124 offset:2048\n"
"ds_read_b128 v[92:95], v124 offset:3072\n"
"s_add_u32 s41, s52, 0x37000\n"
"buffer_store_dwordx4 v[24:27], v127, s[64:67], s41 offen sc1\n"
"v_mfma_f32_16x16x32_bf16 v[16:19], a[64:67], v[64:67], v[16:19]\n"
"s_setprio 2\n"
"v_mfma_f32_16x16x32_bf16 v[16:19], a[72:75], v[68:71], v[16:19]\n"
"v_mul_f32 v12, v120, v12\n"
"v_mul_f32 v13, v120, v13\n"
"v_mul_f32 v14, v120, v14\n"
"v_mfma_f32_16x16x32_bf16 v[16:19], a[80:83], v[72:75], v[16:19]\n"
"v_mul_f32 v15, v120, v15\n"
"v_fmac_f32 v122, v8, v9\n"
"v_fmac_f32 v122, v10, v11\n"
"v_mfma_f32_16x16x32_bf16 v[16:19], a[88:91], v[76:79], v[16:19]\n"
"v_fmac_f32 v122, v12, v13\n"
"v_fmac_f32 v122, v14, v15\n"
"v_cvt_pk_bf16_f32 v8, v8, v9\n"
"v_mfma_f32_16x16x32_bf16 v[20:23], a[68:71], v[64:67], v[20:23]\n"
"v_cvt_pk_bf16_f32 v9, v10, v11\n"
"v_cvt_pk_bf16_f32 v10, v12, v13\n"
"v_cvt_pk_bf16_f32 v11, v14, v15\n"
"v_mfma_f32_16x16x32_bf16 v[20:23], a[76:79], v[68:71], v[20:23]\n"
"s_waitcnt vmcnt(6)\n"
"v_lshlrev_b32 v112, 16, v104\n"
"v_and_b32 v113, s45, v104\n"
"v_mfma_f32_16x16x32_bf16 v[20:23], a[84:87], v[72:75], v[20:23]\n"
"v_mul_f32 v112, s44, v112\n"
"v_mul_f32 v113, s44, v113\n"
"s_waitcnt lgkmcnt(0)\n"
"s_setprio 0\n"
"v_mfma_f32_16x16x32_bf16 v[20:23], a[92:95], v[76:79], v[20:23]\n"
"s_barrier\n"
"ds_read_b128 v[64:67], v124 offset:4096\n"
"ds_read_b128 v[68:71], v124 offset:5120\n"
"ds_read_b128 v[72:75], v124 offset:6144\n"
"ds_read_b128 v[76:79], v124 offset:7168\n"
"s_add_u32 s41, s52, 0x2f000\n"
"buffer_store_dwordx4 v[8:11], v127, s[64:67], s41 offen sc1\n"
"s_add_u32 s40, s51, 0x40000\n"
"buffer_load_dwordx4 v[100:103], v124, s[20:23], s40 offen\n"
"v_mfma_f32_16x16x32_bf16 v[16:19], a[96:99], v[80:83], v[16:19]\n"
"s_setprio 2\n"
"v_mfma_f32_16x16x32_bf16 v[16:19], a[104:107], v[84:87], v[16:19]\n"
"v_cvt_pk_bf16_f32 v104, v112, v113\n"
"v_lshlrev_b32 v112, 16, v105\n"
"v_and_b32 v113, s45, v105\n"
"v_mfma_f32_16x16x32_bf16 v[16:19], a[112:115], v[88:91], v[16:19]\n"
"v_mul_f32 v112, s44, v112\n"
"v_mul_f32 v113, s44, v113\n"
"v_cvt_pk_bf16_f32 v105, v112, v113\n"
"v_mfma_f32_16x16x32_bf16 v[16:19], a[120:123], v[92:95], v[16:19]\n"
"v_lshlrev_b32 v112, 16, v106\n"
"v_and_b32 v113, s45, v106\n"
"v_mul_f32 v112, s44, v112\n"
"v_mfma_f32_16x16x32_bf16 v[20:23], a[100:103], v[80:83], v[20:23]\n"
"v_mul_f32 v113, s44, v113\n"
"v_cvt_pk_bf16_f32 v106, v112, v113\n"
"v_lshlrev_b32 v112, 16, v107\n"
"v_mfma_f32_16x16x32_bf16 v[20:23], a[108:111], v[84:87], v[20:23]\n"
"v_and_b32 v113, s45, v107\n"
"v_mul_f32 v112, s44, v112\n"
"v_mfma_f32_16x16x32_bf16 v[20:23], a[116:119], v[88:91], v[20:23]\n"
"v_mul_f32 v113, s44, v113\n"
"v_cvt_pk_bf16_f32 v107, v112, v113\n"
"v_mfma_f32_16x16x32_bf16 v[20:23], a[124:127], v[92:95], v[20:23]\n"
"s_setprio 0\n"
"ds_read_b128 v[80:83], v124 offset:8192\n"
"ds_read_b128 v[84:87], v124 offset:9216\n"
"ds_read_b128 v[88:91], v124 offset:10240\n"
"ds_read_b128 v[92:95], v124 offset:11264\n"
"ds_read_b128 v[114:117], v124 offset:45056\n"
"ds_write_b128 v126, v[104:107] offset:40960\n"
"s_waitcnt lgkmcnt(6)\n"
"v_mfma_f32_16x16x32_bf16 v[24:27], v[32:35], v[64:67], 0\n"
"s_setprio 2\n"
"v_mfma_f32_16x16x32_bf16 v[24:27], v[40:43], v[68:71], v[24:27]\n"
"s_waitcnt lgkmcnt(1)\n"
"v_add_f32 v114, v114, v115\n"
"v_add_f32 v116, v116, v117\n"
"v_add_f32 v114, v114, v116\n"
"v_mfma_f32_16x16x32_bf16 v[24:27], v[48:51], v[72:75], v[24:27]\n"
"v_rcp_f32 v119, v114\n"
"v_subrev_u32 v118, s46, v114\n"
"v_max_u32 v123, v123, v118\n"
"v_mfma_f32_16x16x32_bf16 v[24:27], v[56:59], v[76:79], v[24:27]\n"
"v_mul_f32 v16, v119, v16\n"
"v_mul_f32 v17, v119, v17\n"
"v_mul_f32 v18, v119, v18\n"
"v_mfma_f32_16x16x32_bf16 v[28:31], v[36:39], v[64:67], 0\n"
"v_mul_f32 v19, v119, v19\n"
"v_mul_f32 v20, v119, v20\n"
"v_mul_f32 v21, v119, v21\n"
"v_mfma_f32_16x16x32_bf16 v[28:31], v[44:47], v[68:71], v[28:31]\n"
"v_mul_f32 v22, v119, v22\n"
"v_mul_f32 v23, v119, v23\n"
"v_mfma_f32_16x16x32_bf16 v[28:31], v[52:55], v[72:75], v[28:31]\n"
"v_fmac_f32 v122, v16, v17\n"
"v_fmac_f32 v122, v18, v19\n"
"v_mfma_f32_16x16x32_bf16 v[28:31], v[60:63], v[76:79], v[28:31]\n"
"s_setprio 0\n"
"ds_read_b128 v[64:67], v124 offset:12288\n"
"ds_read_b128 v[68:71], v124 offset:13312\n"
"ds_read_b128 v[72:75], v124 offset:14336\n"
"ds_read_b128 v[76:79], v124 offset:15360\n"
"ds_read_b128 v[114:117], v124 offset:40960\n"
"v_mfma_f32_16x16x32_bf16 v[24:27], a[32:35], v[80:83], v[24:27]\n"
"s_setprio 2\n"
"v_mfma_f32_16x16x32_bf16 v[24:27], a[40:43], v[84:87], v[24:27]\n"
"s_waitcnt lgkmcnt(0)\n"
"v_add_f32 v114, v114, v115\n"
"v_add_f32 v116, v116, v117\n"
"v_add_f32 v114, v114, v116\n"
"v_mfma_f32_16x16x32_bf16 v[24:27], a[48:51], v[88:91], v[24:27]\n"
"v_rcp_f32 v120, v114\n"
"v_subrev_u32 v118, s46, v114\n"
"v_max_u32 v123, v123, v118\n"
"v_mfma_f32_16x16x32_bf16 v[24:27], a[56:59], v[92:95], v[24:27]\n"
"v_fmac_f32 v122, v20, v21\n"
"v_fmac_f32 v122, v22, v23\n"
"v_cvt_pk_bf16_f32 v16, v16, v17\n"
"v_mfma_f32_16x16x32_bf16 v[28:31], a[36:39], v[80:83], v[28:31]\n"
"v_cvt_pk_bf16_f32 v17, v18, v19\n"
"v_cvt_pk_bf16_f32 v18, v20, v21\n"
"v_cvt_pk_bf16_f32 v19, v22, v23\n"
"v_mfma_f32_16x16x32_bf16 v[28:31], a[44:47], v[84:87], v[28:31]\n"
"v_accvgpr_read_b32 v0, a0\n"
"v_accvgpr_read_b32 v1, a1\n"
"v_mfma_f32_16x16x32_bf16 v[28:31], a[52:55], v[88:91], v[28:31]\n"
"v_accvgpr_read_b32 v2, a2\n"
"v_accvgpr_read_b32 v3, a3\n"
"v_mfma_f32_16x16x32_bf16 v[28:31], a[60:63], v[92:95], v[28:31]\n"
"s_setprio 0\n"
"ds_read_b128 v[80:83], v124 offset:16384\n"
"ds_read_b128 v[84:87], v124 offset:17408\n"
"ds_read_b128 v[88:91], v124 offset:18432\n"
"ds_read_b128 v[92:95], v124 offset:19456\n"
"s_add_u32 s41, s52, 0x38000\n"
"buffer_store_dwordx4 v[16:19], v127, s[64:67], s41 offen sc1\n"
"v_mfma_f32_16x16x32_bf16 v[24:27], a[64:67], v[64:67], v[24:27]\n"
"s_setprio 2\n"
"v_mfma_f32_16x16x32_bf16 v[24:27], a[72:75], v[68:71], v[24:27]\n"
"v_accvgpr_read_b32 v4, a4\n"
"v_accvgpr_read_b32 v5, a5\n"
"v_accvgpr_read_b32 v6, a6\n"
"v_mfma_f32_16x16x32_bf16 v[24:27], a[80:83], v[72:75], v[24:27]\n"
"v_accvgpr_read_b32 v7, a7\n"
"v_mul_f32 v0, v120, v0\n"
"v_mul_f32 v1, v120, v1\n"
"v_mfma_f32_16x16x32_bf16 v[24:27], a[88:91], v[76:79], v[24:27]\n"
"v_mul_f32 v2, v120, v2\n"
"v_mul_f32 v3, v120, v3\n"
"v_mul_f32 v4, v120, v4\n"
"v_mfma_f32_16x16x32_bf16 v[28:31], a[68:71], v[64:67], v[28:31]\n"
"v_mul_f32 v5, v120, v5\n"
"v_mul_f32 v6, v120, v6\n"
"v_mul_f32 v7, v120, v7\n"
"v_mfma_f32_16x16x32_bf16 v[28:31], a[76:79], v[68:71], v[28:31]\n"
"v_fmac_f32 v122, v0, v1\n"
"v_fmac_f32 v122, v2, v3\n"
"v_mfma_f32_16x16x32_bf16 v[28:31], a[84:87], v[72:75], v[28:31]\n"
"v_fmac_f32 v122, v4, v5\n"
"v_fmac_f32 v122, v6, v7\n"
"s_waitcnt lgkmcnt(0)\n"
"s_setprio 0\n"
"v_mfma_f32_16x16x32_bf16 v[28:31], a[92:95], v[76:79], v[28:31]\n"
"s_barrier\n"
"ds_read_b128 v[64:67], v124 offset:20480\n"
"ds_read_b128 v[68:71], v124 offset:21504\n"
"ds_read_b128 v[72:75], v124 offset:22528\n"
"ds_read_b128 v[76:79], v124 offset:23552\n"
"s_add_u32 s40, s51, 0x41000\n"
"buffer_load_dwordx4 v[104:107], v124, s[20:23], s40 offen\n"
"v_mfma_f32_16x16x32_bf16 v[24:27], a[96:99], v[80:83], v[24:27]\n"
"s_setprio 2\n"
"v_mfma_f32_16x16x32_bf16 v[24:27], a[104:107], v[84:87], v[24:27]\n"
"v_cvt_pk_bf16_f32 v0, v0, v1\n"
"v_cvt_pk_bf16_f32 v1, v2, v3\n"
"v_cvt_pk_bf16_f32 v2, v4, v5\n"
"v_mfma_f32_16x16x32_bf16 v[24:27], a[112:115], v[88:91], v[24:27]\n"
"v_cvt_pk_bf16_f32 v3, v6, v7\n"
"s_waitcnt vmcnt(8)\n"
"v_lshlrev_b32 v112, 16, v108\n"
"v_and_b32 v113, s45, v108\n"
"v_mfma_f32_16x16x32_bf16 v[24:27], a[120:123], v[92:95], v[24:27]\n"
"v_mul_f32 v112, s44, v112\n"
"v_mul_f32 v113, s44, v113\n"
"v_cvt_pk_bf16_f32 v108, v112, v113\n"
"v_mfma_f32_16x16x32_bf16 v[28:31], a[100:103], v[80:83], v[28:31]\n"
"v_lshlrev_b32 v112, 16, v109\n"
"v_and_b32 v113, s45, v109\n"
"v_mul_f32 v112, s44, v112\n"
"v_mfma_f32_16x16x32_bf16 v[28:31], a[108:111], v[84:87], v[28:31]\n"
"v_mul_f32 v113, s44, v113\n"
"v_cvt_pk_bf16_f32 v109, v112, v113\n"
"v_mfma_f32_16x16x32_bf16 v[28:31], a[116:119], v[88:91], v[28:31]\n"
"v_lshlrev_b32 v112, 16, v110\n"
"v_and_b32 v113, s45, v110\n"
"v_mfma_f32_16x16x32_bf16 v[28:31], a[124:127], v[92:95], v[28:31]\n"
"s_setprio 0\n"
"ds_read_b128 v[80:83], v124 offset:24576\n"
"ds_read_b128 v[84:87], v124 offset:25600\n"
"ds_read_b128 v[88:91], v124 offset:26624\n"
"ds_read_b128 v[92:95], v124 offset:27648\n"
"ds_read_b128 v[114:117], v124 offset:46080\n"
"s_add_u32 s41, s52, 0x32000\n"
"buffer_store_dwordx4 v[0:3], v127, s[64:67], s41 offen sc1\n"
"s_waitcnt lgkmcnt(5)\n"
"v_mfma_f32_16x16x32_bf16 v[16:19], v[32:35], v[64:67], 0\n"
"s_setprio 2\n"
"v_mfma_f32_16x16x32_bf16 v[16:19], v[40:43], v[68:71], v[16:19]\n"
"s_waitcnt lgkmcnt(0)\n"
"v_add_f32 v114, v114, v115\n"
"v_add_f32 v116, v116, v117\n"
"v_add_f32 v114, v114, v116\n"
"v_mfma_f32_16x16x32_bf16 v[16:19], v[48:51], v[72:75], v[16:19]\n"
"v_rcp_f32 v119, v114\n"
"v_subrev_u32 v118, s46, v114\n"
"v_max_u32 v123, v123, v118\n"
"v_mfma_f32_16x16x32_bf16 v[16:19], v[56:59], v[76:79], v[16:19]\n"
"v_mul_f32 v24, v119, v24\n"
"v_mul_f32 v25, v119, v25\n"
"v_mul_f32 v26, v119, v26\n"
"v_mfma_f32_16x16x32_bf16 v[20:23], v[36:39], v[64:67], 0\n"
"v_mul_f32 v27, v119, v27\n"
"v_mul_f32 v28, v119, v28\n"
"v_mul_f32 v29, v119, v29\n"
"v_mfma_f32_16x16x32_bf16 v[20:23], v[44:47], v[68:71], v[20:23]\n"
"v_mul_f32 v30, v119, v30\n"
"v_mul_f32 v31, v119, v31\n"
"v_mfma_f32_16x16x32_bf16 v[20:23], v[52:55], v[72:75], v[20:23]\n"
"v_fmac_f32 v122, v24, v25\n"
"v_fmac_f32 v122, v26, v27\n"
"v_mfma_f32_16x16x32_bf16 v[20:23], v[60:63], v[76:79], v[20:23]\n"
"s_setprio 0\n"
"ds_read_b128 v[64:67], v124 offset:28672\n"
"ds_read_b128 v[68:71], v124 offset:29696\n"
"ds_read_b128 v[72:75], v124 offset:30720\n"
"ds_read_b128 v[76:79], v124 offset:31744\n"
"ds_read_b128 v[114:117], v124 offset:41984\n"
"v_mfma_f32_16x16x32_bf16 v[16:19], a[32:35], v[80:83], v[16:19]\n"
"s_setprio 2\n"
"v_mfma_f32_16x16x32_bf16 v[16:19], a[40:43], v[84:87], v[16:19]\n"
"s_waitcnt lgkmcnt(0)\n"
"v_add_f32 v114, v114, v115\n"
"v_add_f32 v116, v116, v117\n"
"v_add_f32 v114, v114, v116\n"
"v_mfma_f32_16x16x32_bf16 v[16:19], a[48:51], v[88:91], v[16:19]\n"
"v_rcp_f32 v120, v114\n"
"v_subrev_u32 v118, s46, v114\n"
"v_max_u32 v123, v123, v118\n"
"v_mfma_f32_16x16x32_bf16 v[16:19], a[56:59], v[92:95], v[16:19]\n"
"v_fmac_f32 v122, v28, v29\n"
"v_fmac_f32 v122, v30, v31\n"
"v_cvt_pk_bf16_f32 v24, v24, v25\n"
"v_mfma_f32_16x16x32_bf16 v[20:23], a[36:39], v[80:83], v[20:23]\n"
"v_cvt_pk_bf16_f32 v25, v26, v27\n"
"v_cvt_pk_bf16_f32 v26, v28, v29\n"
"v_cvt_pk_bf16_f32 v27, v30, v31\n"
"v_mfma_f32_16x16x32_bf16 v[20:23], a[44:47], v[84:87], v[20:23]\n"
"v_accvgpr_read_b32 v0, a8\n"
"v_accvgpr_read_b32 v1, a9\n"
"v_mfma_f32_16x16x32_bf16 v[20:23], a[52:55], v[88:91], v[20:23]\n"
"v_accvgpr_read_b32 v2, a10\n"
"v_accvgpr_read_b32 v3, a11\n"
"v_mfma_f32_16x16x32_bf16 v[20:23], a[60:63], v[92:95], v[20:23]\n"
"s_setprio 0\n"
"ds_read_b128 v[80:83], v124 offset:32768\n"
"ds_read_b128 v[84:87], v124 offset:33792\n"
"ds_read_b128 v[88:91], v124 offset:34816\n"
"ds_read_b128 v[92:95], v124 offset:35840\n"
"s_add_u32 s41, s52, 0x39000\n"
"buffer_store_dwordx4 v[24:27], v127, s[64:67], s41 offen sc1\n"
"v_mfma_f32_16x16x32_bf16 v[16:19], a[64:67], v[64:67], v[16:19]\n"
"s_setprio 2\n"
"v_mfma_f32_16x16x32_bf16 v[16:19], a[72:75], v[68:71], v[16:19]\n"
"v_accvgpr_read_b32 v4, a12\n"
"v_accvgpr_read_b32 v5, a13\n"
"v_accvgpr_read_b32 v6, a14\n"
"v_mfma_f32_16x16x32_bf16 v[16:19], a[80:83], v[72:75], v[16:19]\n"
"v_accvgpr_read_b32 v7, a15\n"
"v_mul_f32 v0, v120, v0\n"
"v_mul_f32 v1, v120, v1\n"
"v_mfma_f32_16x16x32_bf16 v[16:19], a[88:91], v[76:79], v[16:19]\n"
"v_mul_f32 v2, v120, v2\n"
"v_mul_f32 v3, v120, v3\n"
"v_mul_f32 v4, v120, v4\n"
"v_mfma_f32_16x16x32_bf16 v[20:23], a[68:71], v[64:67], v[20:23]\n"
"v_mul_f32 v5, v120, v5\n"
"v_mul_f32 v6, v120, v6\n"
"v_mul_f32 v7, v120, v7\n"
"v_mfma_f32_16x16x32_bf16 v[20:23], a[76:79], v[68:71], v[20:23]\n"
"v_fmac_f32 v122, v0, v1\n"
"v_fmac_f32 v122, v2, v3\n"
"v_mfma_f32_16x16x32_bf16 v[20:23], a[84:87], v[72:75], v[20:23]\n"
"v_fmac_f32 v122, v4, v5\n"
"v_fmac_f32 v122, v6, v7\n"
"s_waitcnt lgkmcnt(0)\n"
"s_setprio 0\n"
"v_mfma_f32_16x16x32_bf16 v[20:23], a[92:95], v[76:79], v[20:23]\n"
"s_barrier\n"
"ds_read_b128 v[64:67], v124 offset:0\n"
"ds_read_b128 v[68:71], v124 offset:1024\n"
"ds_read_b128 v[72:75], v124 offset:2048\n"
"ds_read_b128 v[76:79], v124 offset:3072\n"
"v_mfma_f32_16x16x32_bf16 v[16:19], a[96:99], v[80:83], v[16:19]\n"
"s_setprio 2\n"
"v_mfma_f32_16x16x32_bf16 v[16:19], a[104:107], v[84:87], v[16:19]\n"
"v_cvt_pk_bf16_f32 v0, v0, v1\n"
"v_cvt_pk_bf16_f32 v1, v2, v3\n"
"v_cvt_pk_bf16_f32 v2, v4, v5\n"
"v_mfma_f32_16x16x32_bf16 v[16:19], a[112:115], v[88:91], v[16:19]\n"
"v_cvt_pk_bf16_f32 v3, v6, v7\n"
"v_mul_f32 v112, s44, v112\n"
"v_mul_f32 v113, s44, v113\n"
"v_mfma_f32_16x16x32_bf16 v[16:19], a[120:123], v[92:95], v[16:19]\n"
"v_cvt_pk_bf16_f32 v110, v112, v113\n"
"v_lshlrev_b32 v112, 16, v111\n"
"v_and_b32 v113, s45, v111\n"
"v_mfma_f32_16x16x32_bf16 v[20:23], a[100:103], v[80:83], v[20:23]\n"
"v_mul_f32 v112, s44, v112\n"
"v_mul_f32 v113, s44, v113\n"
"v_cvt_pk_bf16_f32 v111, v112, v113\n"
"v_mfma_f32_16x16x32_bf16 v[20:23], a[108:111], v[84:87], v[20:23]\n"
"s_waitcnt vmcnt(7)\n"
"v_lshlrev_b32 v112, 16, v96\n"
"v_and_b32 v113, s45, v96\n"
"v_mfma_f32_16x16x32_bf16 v[20:23], a[116:119], v[88:91], v[20:23]\n"
"v_mul_f32 v112, s44, v112\n"
"v_mul_f32 v113, s44, v113\n"
"v_mfma_f32_16x16x32_bf16 v[20:23], a[124:127], v[92:95], v[20:23]\n"
"s_setprio 0\n"
"ds_read_b128 v[80:83], v124 offset:4096\n"
"ds_read_b128 v[84:87], v124 offset:5120\n"
"ds_read_b128 v[88:91], v124 offset:6144\n"
"ds_read_b128 v[92:95], v124 offset:7168\n"
"ds_read_b128 v[114:117], v124 offset:47104\n"
"s_add_u32 s41, s52, 0x33000\n"
"buffer_store_dwordx4 v[0:3], v127, s[64:67], s41 offen sc1\n"
"ds_write_b128 v126, v[108:111] offset:45056\n"
"s_waitcnt lgkmcnt(6)\n"
"v_mfma_f32_16x16x32_bf16 v[24:27], v[32:35], v[64:67], 0\n"
"s_setprio 2\n"
"v_mfma_f32_16x16x32_bf16 v[24:27], v[40:43], v[68:71], v[24:27]\n"
"s_waitcnt lgkmcnt(1)\n"
"v_add_f32 v114, v114, v115\n"
"v_add_f32 v116, v116, v117\n"
"v_add_f32 v114, v114, v116\n"
"v_mfma_f32_16x16x32_bf16 v[24:27], v[48:51], v[72:75], v[24:27]\n"
"v_rcp_f32 v119, v114\n"
"v_subrev_u32 v118, s46, v114\n"
"v_max_u32 v123, v123, v118\n"
"v_mfma_f32_16x16x32_bf16 v[24:27], v[56:59], v[76:79], v[24:27]\n"
"v_mul_f32 v16, v119, v16\n"
"v_mul_f32 v17, v119, v17\n"
"v_mul_f32 v18, v119, v18\n"
"v_mfma_f32_16x16x32_bf16 v[28:31], v[36:39], v[64:67], 0\n"
"v_mul_f32 v19, v119, v19\n"
"v_mul_f32 v20, v119, v20\n"
"v_mfma_f32_16x16x32_bf16 v[28:31], v[44:47], v[68:71], v[28:31]\n"
"v_mul_f32 v21, v119, v21\n"
"v_mul_f32 v22, v119, v22\n"
"v_mfma_f32_16x16x32_bf16 v[28:31], v[52:55], v[72:75], v[28:31]\n"
"v_mul_f32 v23, v119, v23\n"
"v_fmac_f32 v122, v16, v17\n"
"v_mfma_f32_16x16x32_bf16 v[28:31], v[60:63], v[76:79], v[28:31]\n"
"s_setprio 0\n"
"ds_read_b128 v[64:67], v124 offset:8192\n"
"ds_read_b128 v[68:71], v124 offset:9216\n"
"ds_read_b128 v[72:75], v124 offset:10240\n"
"ds_read_b128 v[76:79], v124 offset:11264\n"
"ds_read_b128 v[114:117], v124 offset:43008\n"
"s_add_u32 s40, s51, 0x42000\n"
"buffer_load_dwordx4 v[108:111], v124, s[20:23], s40 offen\n"
"v_mfma_f32_16x16x32_bf16 v[24:27], a[32:35], v[80:83], v[24:27]\n"
"s_setprio 2\n"
"v_mfma_f32_16x16x32_bf16 v[24:27], a[40:43], v[84:87], v[24:27]\n"
"s_waitcnt lgkmcnt(0)\n"
"v_add_f32 v114, v114, v115\n"
"v_add_f32 v116, v116, v117\n"
"v_add_f32 v114, v114, v116\n"
"v_mfma_f32_16x16x32_bf16 v[24:27], a[48:51], v[88:91], v[24:27]\n"
"v_rcp_f32 v120, v114\n"
"v_subrev_u32 v118, s46, v114\n"
"v_max_u32 v123, v123, v118\n"
"v_mfma_f32_16x16x32_bf16 v[24:27], a[56:59], v[92:95], v[24:27]\n"
"v_fmac_f32 v122, v18, v19\n"
"v_fmac_f32 v122, v20, v21\n"
"v_fmac_f32 v122, v22, v23\n"
"v_mfma_f32_16x16x32_bf16 v[28:31], a[36:39], v[80:83], v[28:31]\n"
"v_cvt_pk_bf16_f32 v16, v16, v17\n"
"v_cvt_pk_bf16_f32 v17, v18, v19\n"
"v_mfma_f32_16x16x32_bf16 v[28:31], a[44:47], v[84:87], v[28:31]\n"
"v_cvt_pk_bf16_f32 v18, v20, v21\n"
"v_cvt_pk_bf16_f32 v19, v22, v23\n"
"v_mfma_f32_16x16x32_bf16 v[28:31], a[52:55], v[88:91], v[28:31]\n"
"v_accvgpr_read_b32 v0, a16\n"
"v_accvgpr_read_b32 v1, a17\n"
"v_mfma_f32_16x16x32_bf16 v[28:31], a[60:63], v[92:95], v[28:31]\n"
"s_setprio 0\n"
"ds_read_b128 v[80:83], v124 offset:12288\n"
"ds_read_b128 v[84:87], v124 offset:13312\n"
"ds_read_b128 v[88:91], v124 offset:14336\n"
"ds_read_b128 v[92:95], v124 offset:15360\n"
"s_add_u32 s41, s52, 0x3a000\n"
"buffer_store_dwordx4 v[16:19], v127, s[64:67], s41 offen sc1\n"
"v_mfma_f32_16x16x32_bf16 v[24:27], a[64:67], v[64:67], v[24:27]\n"
"s_setprio 2\n"
"v_mfma_f32_16x16x32_bf16 v[24:27], a[72:75], v[68:71], v[24:27]\n"
"v_accvgpr_read_b32 v2, a18\n"
"v_accvgpr_read_b32 v3, a19\n"
"v_accvgpr_read_b32 v4, a20\n"
"v_mfma_f32_16x16x32_bf16 v[24:27], a[80:83], v[72:75], v[24:27]\n"
"v_accvgpr_read_b32 v5, a21\n"
"v_accvgpr_read_b32 v6, a22\n"
"v_accvgpr_read_b32 v7, a23\n"
"v_mfma_f32_16x16x32_bf16 v[24:27], a[88:91], v[76:79], v[24:27]\n"
"v_mul_f32 v0, v120, v0\n"
"v_mul_f32 v1, v120, v1\n"
"v_mul_f32 v2, v120, v2\n"
"v_mfma_f32_16x16x32_bf16 v[28:31], a[68:71], v[64:67], v[28:31]\n"
"v_mul_f32 v3, v120, v3\n"
"v_mul_f32 v4, v120, v4\n"
"v_mfma_f32_16x16x32_bf16 v[28:31], a[76:79], v[68:71], v[28:31]\n"
"v_mul_f32 v5, v120, v5\n"
"v_mul_f32 v6, v120, v6\n"
"v_mfma_f32_16x16x32_bf16 v[28:31], a[84:87], v[72:75], v[28:31]\n"
"v_mul_f32 v7, v120, v7\n"
"v_fmac_f32 v122, v0, v1\n"
"s_waitcnt lgkmcnt(0)\n"
"s_setprio 0\n"
"v_mfma_f32_16x16x32_bf16 v[28:31], a[92:95], v[76:79], v[28:31]\n"
"s_barrier\n"
"ds_read_b128 v[64:67], v124 offset:16384\n"
"ds_read_b128 v[68:71], v124 offset:17408\n"
"ds_read_b128 v[72:75], v124 offset:18432\n"
"ds_read_b128 v[76:79], v124 offset:19456\n"
"v_mfma_f32_16x16x32_bf16 v[24:27], a[96:99], v[80:83], v[24:27]\n"
"s_setprio 2\n"
"v_mfma_f32_16x16x32_bf16 v[24:27], a[104:107], v[84:87], v[24:27]\n"
"v_fmac_f32 v122, v2, v3\n"
"v_fmac_f32 v122, v4, v5\n"
"v_fmac_f32 v122, v6, v7\n"
"v_mfma_f32_16x16x32_bf16 v[24:27], a[112:115], v[88:91], v[24:27]\n"
"v_cvt_pk_bf16_f32 v0, v0, v1\n"
"v_cvt_pk_bf16_f32 v1, v2, v3\n"
"v_cvt_pk_bf16_f32 v2, v4, v5\n"
"v_mfma_f32_16x16x32_bf16 v[24:27], a[120:123], v[92:95], v[24:27]\n"
"v_cvt_pk_bf16_f32 v3, v6, v7\n"
"v_cvt_pk_bf16_f32 v96, v112, v113\n"
"v_lshlrev_b32 v112, 16, v97\n"
"v_mfma_f32_16x16x32_bf16 v[28:31], a[100:103], v[80:83], v[28:31]\n"
"v_and_b32 v113, s45, v97\n"
"v_mul_f32 v112, s44, v112\n"
"v_mfma_f32_16x16x32_bf16 v[28:31], a[108:111], v[84:87], v[28:31]\n"
"v_mul_f32 v113, s44, v113\n"
"v_cvt_pk_bf16_f32 v97, v112, v113\n"
"v_mfma_f32_16x16x32_bf16 v[28:31], a[116:119], v[88:91], v[28:31]\n"
"v_lshlrev_b32 v112, 16, v98\n"
"v_and_b32 v113, s45, v98\n"
"v_mfma_f32_16x16x32_bf16 v[28:31], a[124:127], v[92:95], v[28:31]\n"
"s_setprio 0\n"
"ds_read_b128 v[80:83], v124 offset:20480\n"
"ds_read_b128 v[84:87], v124 offset:21504\n"
"ds_read_b128 v[88:91], v124 offset:22528\n"
"ds_read_b128 v[92:95], v124 offset:23552\n"
"ds_read_b128 v[114:117], v124 offset:45056\n"
"s_add_u32 s41, s52, 0x34000\n"
"buffer_store_dwordx4 v[0:3], v127, s[64:67], s41 offen sc1\n"
"s_waitcnt lgkmcnt(5)\n"
"v_mfma_f32_16x16x32_bf16 v[16:19], v[32:35], v[64:67], 0\n"
"s_setprio 2\n"
"v_mfma_f32_16x16x32_bf16 v[16:19], v[40:43], v[68:71], v[16:19]\n"
"s_waitcnt lgkmcnt(0)\n"
"v_add_f32 v114, v114, v115\n"
"v_add_f32 v116, v116, v117\n"
"v_add_f32 v114, v114, v116\n"
"v_mfma_f32_16x16x32_bf16 v[16:19], v[48:51], v[72:75], v[16:19]\n"
"v_rcp_f32 v119, v114\n"
"v_subrev_u32 v118, s46, v114\n"
"v_max_u32 v123, v123, v118\n"
"v_mfma_f32_16x16x32_bf16 v[16:19], v[56:59], v[76:79], v[16:19]\n"
"v_mul_f32 v112, s44, v112\n"
"v_mul_f32 v113, s44, v113\n"
"v_cvt_pk_bf16_f32 v98, v112, v113\n"
"v_mfma_f32_16x16x32_bf16 v[20:23], v[36:39], v[64:67], 0\n"
"v_lshlrev_b32 v112, 16, v99\n"
"v_and_b32 v113, s45, v99\n"
"v_mfma_f32_16x16x32_bf16 v[20:23], v[44:47], v[68:71], v[20:23]\n"
"v_mul_f32 v112, s44, v112\n"
"v_mul_f32 v113, s44, v113\n"
"v_mfma_f32_16x16x32_bf16 v[20:23], v[52:55], v[72:75], v[20:23]\n"
"v_cvt_pk_bf16_f32 v99, v112, v113\n"
"v_mul_f32 v24, v119, v24\n"
"v_mfma_f32_16x16x32_bf16 v[20:23], v[60:63], v[76:79], v[20:23]\n"
"s_setprio 0\n"
"ds_read_b128 v[64:67], v124 offset:24576\n"
"ds_read_b128 v[68:71], v124 offset:25600\n"
"ds_read_b128 v[72:75], v124 offset:26624\n"
"ds_read_b128 v[76:79], v124 offset:27648\n"
"ds_read_b128 v[114:117], v124 offset:44032\n"
"ds_write_b128 v126, v[96:99] offset:32768\n"
"v_mfma_f32_16x16x32_bf16 v[16:19], a[32:35], v[80:83], v[16:19]\n"
"s_setprio 2\n"
"v_mfma_f32_16x16x32_bf16 v[16:19], a[40:43], v[84:87], v[16:19]\n"
"s_waitcnt lgkmcnt(1)\n"
"v_add_f32 v114, v114, v115\n"
"v_add_f32 v116, v116, v117\n"
"v_add_f32 v114, v114, v116\n"
"v_mfma_f32_16x16x32_bf16 v[16:19], a[48:51], v[88:91], v[16:19]\n"
"v_rcp_f32 v120, v114\n"
"v_subrev_u32 v118, s46, v114\n"
"v_max_u32 v123, v123, v118\n"
"v_mfma_f32_16x16x32_bf16 v[16:19], a[56:59], v[92:95], v[16:19]\n"
"v_mul_f32 v25, v119, v25\n"
"v_mul_f32 v26, v119, v26\n"
"v_mul_f32 v27, v119, v27\n"
"v_mfma_f32_16x16x32_bf16 v[20:23], a[36:39], v[80:83], v[20:23]\n"
"v_mul_f32 v28, v119, v28\n"
"v_mul_f32 v29, v119, v29\n"
"v_mfma_f32_16x16x32_bf16 v[20:23], a[44:47], v[84:87], v[20:23]\n"
"v_mul_f32 v30, v119, v30\n"
"v_mul_f32 v31, v119, v31\n"
"v_mfma_f32_16x16x32_bf16 v[20:23], a[52:55], v[88:91], v[20:23]\n"
"v_fmac_f32 v122, v24, v25\n"
"v_fmac_f32 v122, v26, v27\n"
"v_mfma_f32_16x16x32_bf16 v[20:23], a[60:63], v[92:95], v[20:23]\n"
"s_setprio 0\n"
"ds_read_b128 v[80:83], v124 offset:28672\n"
"ds_read_b128 v[84:87], v124 offset:29696\n"
"ds_read_b128 v[88:91], v124 offset:30720\n"
"ds_read_b128 v[92:95], v124 offset:31744\n"
"s_add_u32 s40, s51, 0x43000\n"
"buffer_load_dwordx4 v[96:99], v124, s[20:23], s40 offen\n"
"v_mfma_f32_16x16x32_bf16 v[16:19], a[64:67], v[64:67], v[16:19]\n"
"s_setprio 2\n"
"v_mfma_f32_16x16x32_bf16 v[16:19], a[72:75], v[68:71], v[16:19]\n"
"v_fmac_f32 v122, v28, v29\n"
"v_fmac_f32 v122, v30, v31\n"
"v_cvt_pk_bf16_f32 v24, v24, v25\n"
"v_mfma_f32_16x16x32_bf16 v[16:19], a[80:83], v[72:75], v[16:19]\n"
"v_cvt_pk_bf16_f32 v25, v26, v27\n"
"v_cvt_pk_bf16_f32 v26, v28, v29\n"
"v_cvt_pk_bf16_f32 v27, v30, v31\n"
"v_mfma_f32_16x16x32_bf16 v[16:19], a[88:91], v[76:79], v[16:19]\n"
"v_accvgpr_read_b32 v0, a24\n"
"v_accvgpr_read_b32 v1, a25\n"
"v_accvgpr_read_b32 v2, a26\n"
"v_mfma_f32_16x16x32_bf16 v[20:23], a[68:71], v[64:67], v[20:23]\n"
"v_accvgpr_read_b32 v3, a27\n"
"v_accvgpr_read_b32 v4, a28\n"
"v_mfma_f32_16x16x32_bf16 v[20:23], a[76:79], v[68:71], v[20:23]\n"
"v_accvgpr_read_b32 v5, a29\n"
"v_accvgpr_read_b32 v6, a30\n"
"v_mfma_f32_16x16x32_bf16 v[20:23], a[84:87], v[72:75], v[20:23]\n"
"v_accvgpr_read_b32 v7, a31\n"
"v_mul_f32 v0, v120, v0\n"
"s_waitcnt lgkmcnt(0)\n"
"s_setprio 0\n"
"v_mfma_f32_16x16x32_bf16 v[20:23], a[92:95], v[76:79], v[20:23]\n"
"s_barrier\n"
"ds_read_b128 v[64:67], v124 offset:32768\n"
"ds_read_b128 v[68:71], v124 offset:33792\n"
"ds_read_b128 v[72:75], v124 offset:34816\n"
"ds_read_b128 v[76:79], v124 offset:35840\n"
"s_add_u32 s41, s52, 0x3b000\n"
"buffer_store_dwordx4 v[24:27], v127, s[64:67], s41 offen sc1\n"
"v_mfma_f32_16x16x32_bf16 v[16:19], a[96:99], v[80:83], v[16:19]\n"
"s_setprio 2\n"
"v_mfma_f32_16x16x32_bf16 v[16:19], a[104:107], v[84:87], v[16:19]\n"
"v_mul_f32 v1, v120, v1\n"
"v_mul_f32 v2, v120, v2\n"
"v_mul_f32 v3, v120, v3\n"
"v_mfma_f32_16x16x32_bf16 v[16:19], a[112:115], v[88:91], v[16:19]\n"
"v_mul_f32 v4, v120, v4\n"
"v_mul_f32 v5, v120, v5\n"
"v_mul_f32 v6, v120, v6\n"
"v_mfma_f32_16x16x32_bf16 v[16:19], a[120:123], v[92:95], v[16:19]\n"
"v_mul_f32 v7, v120, v7\n"
"v_fmac_f32 v122, v0, v1\n"
"v_fmac_f32 v122, v2, v3\n"
"v_mfma_f32_16x16x32_bf16 v[20:23], a[100:103], v[80:83], v[20:23]\n"
"v_fmac_f32 v122, v4, v5\n"
"v_fmac_f32 v122, v6, v7\n"
"v_mfma_f32_16x16x32_bf16 v[20:23], a[108:111], v[84:87], v[20:23]\n"
"v_cvt_pk_bf16_f32 v0, v0, v1\n"
"v_cvt_pk_bf16_f32 v1, v2, v3\n"
"v_mfma_f32_16x16x32_bf16 v[20:23], a[116:119], v[88:91], v[20:23]\n"
"v_cvt_pk_bf16_f32 v2, v4, v5\n"
"v_cvt_pk_bf16_f32 v3, v6, v7\n"
"v_mfma_f32_16x16x32_bf16 v[20:23], a[124:127], v[92:95], v[20:23]\n"
"s_setprio 0\n"
"ds_read_b128 v[80:83], v124 offset:0\n"
"ds_read_b128 v[84:87], v124 offset:1024\n"
"ds_read_b128 v[88:91], v124 offset:2048\n"
"ds_read_b128 v[92:95], v124 offset:3072\n"
"ds_read_b128 v[114:117], v124 offset:46080\n"
"s_add_u32 s41, s52, 0x35000\n"
"buffer_store_dwordx4 v[0:3], v127, s[64:67], s41 offen sc1\n"
"s_waitcnt lgkmcnt(5)\n"
"v_mfma_f32_16x16x32_bf16 v[24:27], v[32:35], v[64:67], 0\n"
"s_setprio 2\n"
"v_mfma_f32_16x16x32_bf16 v[24:27], v[40:43], v[68:71], v[24:27]\n"
"s_waitcnt lgkmcnt(0)\n"
"v_add_f32 v114, v114, v115\n"
"v_add_f32 v116, v116, v117\n"
"v_mfma_f32_16x16x32_bf16 v[24:27], v[48:51], v[72:75], v[24:27]\n"
"v_add_f32 v114, v114, v116\n"
"v_rcp_f32 v119, v114\n"
"v_mfma_f32_16x16x32_bf16 v[24:27], v[56:59], v[76:79], v[24:27]\n"
"v_subrev_u32 v118, s46, v114\n"
"v_max_u32 v123, v123, v118\n"
"v_mfma_f32_16x16x32_bf16 v[28:31], v[36:39], v[64:67], 0\n"
"s_waitcnt vmcnt(11)\n"
"v_lshlrev_b32 v112, 16, v100\n"
"v_and_b32 v113, s45, v100\n"
"v_mfma_f32_16x16x32_bf16 v[28:31], v[44:47], v[68:71], v[28:31]\n"
"v_mul_f32 v112, s44, v112\n"
"v_mul_f32 v113, s44, v113\n"
"v_mfma_f32_16x16x32_bf16 v[28:31], v[52:55], v[72:75], v[28:31]\n"
"v_cvt_pk_bf16_f32 v100, v112, v113\n"
"v_lshlrev_b32 v112, 16, v101\n"
"v_mfma_f32_16x16x32_bf16 v[28:31], v[60:63], v[76:79], v[28:31]\n"
"s_setprio 0\n"
"ds_read_b128 v[64:67], v124 offset:4096\n"
"ds_read_b128 v[68:71], v124 offset:5120\n"
"ds_read_b128 v[72:75], v124 offset:6144\n"
"ds_read_b128 v[76:79], v124 offset:7168\n"
"v_mfma_f32_16x16x32_bf16 v[24:27], a[32:35], v[80:83], v[24:27]\n"
"s_setprio 2\n"
"v_mfma_f32_16x16x32_bf16 v[24:27], a[40:43], v[84:87], v[24:27]\n"
"v_and_b32 v113, s45, v101\n"
"v_mul_f32 v112, s44, v112\n"
"v_mul_f32 v113, s44, v113\n"
"v_mfma_f32_16x16x32_bf16 v[24:27], a[48:51], v[88:91], v[24:27]\n"
"v_cvt_pk_bf16_f32 v101, v112, v113\n"
"v_lshlrev_b32 v112, 16, v102\n"
"v_and_b32 v113, s45, v102\n"
"v_mfma_f32_16x16x32_bf16 v[24:27], a[56:59], v[92:95], v[24:27]\n"
"v_mul_f32 v112, s44, v112\n"
"v_mul_f32 v113, s44, v113\n"
"v_mfma_f32_16x16x32_bf16 v[28:31], a[36:39], v[80:83], v[28:31]\n"
"v_cvt_pk_bf16_f32 v102, v112, v113\n"
"v_lshlrev_b32 v112, 16, v103\n"
"v_mfma_f32_16x16x32_bf16 v[28:31], a[44:47], v[84:87], v[28:31]\n"
"v_and_b32 v113, s45, v103\n"
"v_mul_f32 v112, s44, v112\n"
"v_mfma_f32_16x16x32_bf16 v[28:31], a[52:55], v[88:91], v[28:31]\n"
"v_mul_f32 v113, s44, v113\n"
"v_cvt_pk_bf16_f32 v103, v112, v113\n"
"v_mfma_f32_16x16x32_bf16 v[28:31], a[60:63], v[92:95], v[28:31]\n"
"s_setprio 0\n"
"ds_read_b128 v[80:83], v124 offset:8192\n"
"ds_read_b128 v[84:87], v124 offset:9216\n"
"ds_read_b128 v[88:91], v124 offset:10240\n"
"ds_read_b128 v[92:95], v124 offset:11264\n"
"ds_write_b128 v126, v[100:103] offset:36864\n"
"s_waitcnt lgkmcnt(5)\n"
"v_mfma_f32_16x16x32_bf16 v[24:27], a[64:67], v[64:67], v[24:27]\n"
"s_setprio 2\n"
"v_mfma_f32_16x16x32_bf16 v[24:27], a[72:75], v[68:71], v[24:27]\n"
"v_mul_f32 v16, v119, v16\n"
"v_mul_f32 v17, v119, v17\n"
"v_mfma_f32_16x16x32_bf16 v[24:27], a[80:83], v[72:75], v[24:27]\n"
"v_mul_f32 v18, v119, v18\n"
"v_mul_f32 v19, v119, v19\n"
"v_mfma_f32_16x16x32_bf16 v[24:27], a[88:91], v[76:79], v[24:27]\n"
"v_mul_f32 v20, v119, v20\n"
"v_mul_f32 v21, v119, v21\n"
"v_mfma_f32_16x16x32_bf16 v[28:31], a[68:71], v[64:67], v[28:31]\n"
"v_mul_f32 v22, v119, v22\n"
"v_mul_f32 v23, v119, v23\n"
"v_mfma_f32_16x16x32_bf16 v[28:31], a[76:79], v[68:71], v[28:31]\n"
"v_fmac_f32 v122, v16, v17\n"
"v_fmac_f32 v122, v18, v19\n"
"v_mfma_f32_16x16x32_bf16 v[28:31], a[84:87], v[72:75], v[28:31]\n"
"v_fmac_f32 v122, v20, v21\n"
"v_fmac_f32 v122, v22, v23\n"
"s_waitcnt lgkmcnt(0)\n"
"s_setprio 0\n"
"v_mfma_f32_16x16x32_bf16 v[28:31], a[92:95], v[76:79], v[28:31]\n"
"s_barrier\n"
"ds_read_b128 v[64:67], v124 offset:12288\n"
"ds_read_b128 v[68:71], v124 offset:13312\n"
"ds_read_b128 v[72:75], v124 offset:14336\n"
"ds_read_b128 v[76:79], v124 offset:15360\n"
"s_add_u32 s40, s51, 0x44000\n"
"buffer_load_dwordx4 v[100:103], v124, s[20:23], s40 offen\n"
"v_mfma_f32_16x16x32_bf16 v[24:27], a[96:99], v[80:83], v[24:27]\n"
"s_setprio 2\n"
"v_mfma_f32_16x16x32_bf16 v[24:27], a[104:107], v[84:87], v[24:27]\n"
"v_cvt_pk_bf16_f32 v16, v16, v17\n"
"v_cvt_pk_bf16_f32 v17, v18, v19\n"
"v_mfma_f32_16x16x32_bf16 v[24:27], a[112:115], v[88:91], v[24:27]\n"
"v_cvt_pk_bf16_f32 v18, v20, v21\n"
"v_cvt_pk_bf16_f32 v19, v22, v23\n"
"v_mfma_f32_16x16x32_bf16 v[24:27], a[120:123], v[92:95], v[24:27]\n"
"s_waitcnt vmcnt(10)\n"
"v_lshlrev_b32 v112, 16, v104\n"
"v_and_b32 v113, s45, v104\n"
"v_mfma_f32_16x16x32_bf16 v[28:31], a[100:103], v[80:83], v[28:31]\n"
"v_mul_f32 v112, s44, v112\n"
"v_mul_f32 v113, s44, v113\n"
"v_mfma_f32_16x16x32_bf16 v[28:31], a[108:111], v[84:87], v[28:31]\n"
"v_cvt_pk_bf16_f32 v104, v112, v113\n"
"v_lshlrev_b32 v112, 16, v105\n"
"v_mfma_f32_16x16x32_bf16 v[28:31], a[116:119], v[88:91], v[28:31]\n"
"v_and_b32 v113, s45, v105\n"
"v_mul_f32 v112, s44, v112\n"
"v_mfma_f32_16x16x32_bf16 v[28:31], a[124:127], v[92:95], v[28:31]\n"
"s_setprio 0\n"
"ds_read_b128 v[80:83], v124 offset:16384\n"
"ds_read_b128 v[84:87], v124 offset:17408\n"
"ds_read_b128 v[88:91], v124 offset:18432\n"
"ds_read_b128 v[92:95], v124 offset:19456\n"
"ds_read_b128 v[114:117], v124 offset:47104\n"
"s_add_u32 s41, s52, 0x3c000\n"
"buffer_store_dwordx4 v[16:19], v127, s[64:67], s41 offen sc1\n"
"s_waitcnt lgkmcnt(5)\n"
"v_mfma_f32_16x16x32_bf16 v[16:19], v[32:35], v[64:67], 0\n"
"s_setprio 2\n"
"v_mfma_f32_16x16x32_bf16 v[16:19], v[40:43], v[68:71], v[16:19]\n"
"s_waitcnt lgkmcnt(0)\n"
"v_add_f32 v114, v114, v115\n"
"v_add_f32 v116, v116, v117\n"
"v_mfma_f32_16x16x32_bf16 v[16:19], v[48:51], v[72:75], v[16:19]\n"
"v_add_f32 v114, v114, v116\n"
"v_rcp_f32 v119, v114\n"
"v_mfma_f32_16x16x32_bf16 v[16:19], v[56:59], v[76:79], v[16:19]\n"
"v_subrev_u32 v118, s46, v114\n"
"v_max_u32 v123, v123, v118\n"
"v_mfma_f32_16x16x32_bf16 v[20:23], v[36:39], v[64:67], 0\n"
"v_mul_f32 v113, s44, v113\n"
"v_cvt_pk_bf16_f32 v105, v112, v113\n"
"v_mfma_f32_16x16x32_bf16 v[20:23], v[44:47], v[68:71], v[20:23]\n"
"v_lshlrev_b32 v112, 16, v106\n"
"v_and_b32 v113, s45, v106\n"
"v_mfma_f32_16x16x32_bf16 v[20:23], v[52:55], v[72:75], v[20:23]\n"
"v_mul_f32 v112, s44, v112\n"
"v_mul_f32 v113, s44, v113\n"
"v_mfma_f32_16x16x32_bf16 v[20:23], v[60:63], v[76:79], v[20:23]\n"
"s_setprio 0\n"
"ds_read_b128 v[64:67], v124 offset:20480\n"
"ds_read_b128 v[68:71], v124 offset:21504\n"
"ds_read_b128 v[72:75], v124 offset:22528\n"
"ds_read_b128 v[76:79], v124 offset:23552\n"
"v_mfma_f32_16x16x32_bf16 v[16:19], a[32:35], v[80:83], v[16:19]\n"
"s_setprio 2\n"
"v_mfma_f32_16x16x32_bf16 v[16:19], a[40:43], v[84:87], v[16:19]\n"
"v_cvt_pk_bf16_f32 v106, v112, v113\n"
"v_lshlrev_b32 v112, 16, v107\n"
"v_mfma_f32_16x16x32_bf16 v[16:19], a[48:51], v[88:91], v[16:19]\n"
"v_and_b32 v113, s45, v107\n"
"v_mul_f32 v112, s44, v112\n"
"v_mfma_f32_16x16x32_bf16 v[16:19], a[56:59], v[92:95], v[16:19]\n"
"v_mul_f32 v113, s44, v113\n"
"v_cvt_pk_bf16_f32 v107, v112, v113\n"
"v_mfma_f32_16x16x32_bf16 v[20:23], a[36:39], v[80:83], v[20:23]\n"
"v_mul_f32 v24, v119, v24\n"
"v_mul_f32 v25, v119, v25\n"
"v_mfma_f32_16x16x32_bf16 v[20:23], a[44:47], v[84:87], v[20:23]\n"
"v_mul_f32 v26, v119, v26\n"
"v_mul_f32 v27, v119, v27\n"
"v_mfma_f32_16x16x32_bf16 v[20:23], a[52:55], v[88:91], v[20:23]\n"
"v_mul_f32 v28, v119, v28\n"
"v_mul_f32 v29, v119, v29\n"
"v_mfma_f32_16x16x32_bf16 v[20:23], a[60:63], v[92:95], v[20:23]\n"
"s_setprio 0\n"
"ds_read_b128 v[80:83], v124 offset:24576\n"
"ds_read_b128 v[84:87], v124 offset:25600\n"
"ds_read_b128 v[88:91], v124 offset:26624\n"
"ds_read_b128 v[92:95], v124 offset:27648\n"
"ds_write_b128 v126, v[104:107] offset:40960\n"
"s_waitcnt lgkmcnt(5)\n"
"v_mfma_f32_16x16x32_bf16 v[16:19], a[64:67], v[64:67], v[16:19]\n"
"s_setprio 2\n"
"v_mfma_f32_16x16x32_bf16 v[16:19], a[72:75], v[68:71], v[16:19]\n"
"v_mul_f32 v30, v119, v30\n"
"v_mul_f32 v31, v119, v31\n"
"v_mfma_f32_16x16x32_bf16 v[16:19], a[80:83], v[72:75], v[16:19]\n"
"v_fmac_f32 v122, v24, v25\n"
"v_fmac_f32 v122, v26, v27\n"
"v_mfma_f32_16x16x32_bf16 v[16:19], a[88:91], v[76:79], v[16:19]\n"
"v_fmac_f32 v122, v28, v29\n"
"v_fmac_f32 v122, v30, v31\n"
"v_mfma_f32_16x16x32_bf16 v[20:23], a[68:71], v[64:67], v[20:23]\n"
"v_cvt_pk_bf16_f32 v24, v24, v25\n"
"v_cvt_pk_bf16_f32 v25, v26, v27\n"
"v_mfma_f32_16x16x32_bf16 v[20:23], a[76:79], v[68:71], v[20:23]\n"
"v_cvt_pk_bf16_f32 v26, v28, v29\n"
"v_cvt_pk_bf16_f32 v27, v30, v31\n"
"v_mfma_f32_16x16x32_bf16 v[20:23], a[84:87], v[72:75], v[20:23]\n"
"s_waitcnt vmcnt(7)\n"
"v_lshlrev_b32 v112, 16, v108\n"
"v_and_b32 v113, s45, v108\n"
"s_waitcnt lgkmcnt(0)\n"
"s_setprio 0\n"
"v_mfma_f32_16x16x32_bf16 v[20:23], a[92:95], v[76:79], v[20:23]\n"
"s_barrier\n"
"ds_read_b128 v[64:67], v124 offset:28672\n"
"ds_read_b128 v[68:71], v124 offset:29696\n"
"ds_read_b128 v[72:75], v124 offset:30720\n"
"ds_read_b128 v[76:79], v124 offset:31744\n"
"s_add_u32 s41, s52, 0x3d000\n"
"buffer_store_dwordx4 v[24:27], v127, s[64:67], s41 offen sc1\n"
"v_mfma_f32_16x16x32_bf16 v[16:19], a[96:99], v[80:83], v[16:19]\n"
"s_setprio 2\n"
"v_mfma_f32_16x16x32_bf16 v[16:19], a[104:107], v[84:87], v[16:19]\n"
"v_mul_f32 v112, s44, v112\n"
"v_mul_f32 v113, s44, v113\n"
"v_mfma_f32_16x16x32_bf16 v[16:19], a[112:115], v[88:91], v[16:19]\n"
"v_cvt_pk_bf16_f32 v108, v112, v113\n"
"v_lshlrev_b32 v112, 16, v109\n"
"v_mfma_f32_16x16x32_bf16 v[16:19], a[120:123], v[92:95], v[16:19]\n"
"v_and_b32 v113, s45, v109\n"
"v_mul_f32 v112, s44, v112\n"
"v_mfma_f32_16x16x32_bf16 v[20:23], a[100:103], v[80:83], v[20:23]\n"
"v_mul_f32 v113, s44, v113\n"
"v_cvt_pk_bf16_f32 v109, v112, v113\n"
"v_mfma_f32_16x16x32_bf16 v[20:23], a[108:111], v[84:87], v[20:23]\n"
"v_lshlrev_b32 v112, 16, v110\n"
"v_and_b32 v113, s45, v110\n"
"v_mfma_f32_16x16x32_bf16 v[20:23], a[116:119], v[88:91], v[20:23]\n"
"v_mul_f32 v112, s44, v112\n"
"v_mul_f32 v113, s44, v113\n"
"v_mfma_f32_16x16x32_bf16 v[20:23], a[124:127], v[92:95], v[20:23]\n"
"s_setprio 0\n"
"ds_read_b128 v[80:83], v124 offset:32768\n"
"ds_read_b128 v[84:87], v124 offset:33792\n"
"ds_read_b128 v[88:91], v124 offset:34816\n"
"ds_read_b128 v[92:95], v124 offset:35840\n"
"ds_read_b128 v[114:117], v124 offset:45056\n"
"s_waitcnt lgkmcnt(5)\n"
"v_mfma_f32_16x16x32_bf16 v[24:27], v[32:35], v[64:67], 0\n"
"s_setprio 2\n"
"v_mfma_f32_16x16x32_bf16 v[24:27], v[40:43], v[68:71], v[24:27]\n"
"s_waitcnt lgkmcnt(0)\n"
"v_add_f32 v114, v114, v115\n"
"v_add_f32 v116, v116, v117\n"
"v_mfma_f32_16x16x32_bf16 v[24:27], v[48:51], v[72:75], v[24:27]\n"
"v_add_f32 v114, v114, v116\n"
"v_rcp_f32 v119, v114\n"
"v_mfma_f32_16x16x32_bf16 v[24:27], v[56:59], v[76:79], v[24:27]\n"
"v_subrev_u32 v118, s46, v114\n"
"v_max_u32 v123, v123, v118\n"
"v_mfma_f32_16x16x32_bf16 v[28:31], v[36:39], v[64:67], 0\n"
"v_cvt_pk_bf16_f32 v110, v112, v113\n"
"v_lshlrev_b32 v112, 16, v111\n"
"v_mfma_f32_16x16x32_bf16 v[28:31], v[44:47], v[68:71], v[28:31]\n"
"v_and_b32 v113, s45, v111\n"
"v_mul_f32 v112, s44, v112\n"
"v_mfma_f32_16x16x32_bf16 v[28:31], v[52:55], v[72:75], v[28:31]\n"
"v_mul_f32 v113, s44, v113\n"
"v_cvt_pk_bf16_f32 v111, v112, v113\n"
"v_mfma_f32_16x16x32_bf16 v[28:31], v[60:63], v[76:79], v[28:31]\n"
"s_setprio 0\n"
"ds_read_b128 v[64:67], v124 offset:0\n"
"ds_read_b128 v[68:71], v124 offset:1024\n"
"ds_read_b128 v[72:75], v124 offset:2048\n"
"ds_read_b128 v[76:79], v124 offset:3072\n"
"ds_write_b128 v126, v[108:111] offset:45056\n"
"v_mfma_f32_16x16x32_bf16 v[24:27], a[32:35], v[80:83], v[24:27]\n"
"s_setprio 2\n"
"v_mfma_f32_16x16x32_bf16 v[24:27], a[40:43], v[84:87], v[24:27]\n"
"v_mul_f32 v16, v119, v16\n"
"v_mul_f32 v17, v119, v17\n"
"v_mfma_f32_16x16x32_bf16 v[24:27], a[48:51], v[88:91], v[24:27]\n"
"v_mul_f32 v18, v119, v18\n"
"v_mul_f32 v19, v119, v19\n"
"v_mfma_f32_16x16x32_bf16 v[24:27], a[56:59], v[92:95], v[24:27]\n"
"v_mul_f32 v20, v119, v20\n"
"v_mul_f32 v21, v119, v21\n"
"v_mfma_f32_16x16x32_bf16 v[28:31], a[36:39], v[80:83], v[28:31]\n"
"v_mul_f32 v22, v119, v22\n"
"v_mul_f32 v23, v119, v23\n"
"v_mfma_f32_16x16x32_bf16 v[28:31], a[44:47], v[84:87], v[28:31]\n"
"v_fmac_f32 v122, v16, v17\n"
"v_fmac_f32 v122, v18, v19\n"
"v_mfma_f32_16x16x32_bf16 v[28:31], a[52:55], v[88:91], v[28:31]\n"
"v_fmac_f32 v122, v20, v21\n"
"v_fmac_f32 v122, v22, v23\n"
"v_mfma_f32_16x16x32_bf16 v[28:31], a[60:63], v[92:95], v[28:31]\n"
"s_setprio 0\n"
"ds_read_b128 v[80:83], v124 offset:4096\n"
"ds_read_b128 v[84:87], v124 offset:5120\n"
"ds_read_b128 v[88:91], v124 offset:6144\n"
"ds_read_b128 v[92:95], v124 offset:7168\n"
"s_waitcnt lgkmcnt(5)\n"
"v_mfma_f32_16x16x32_bf16 v[24:27], a[64:67], v[64:67], v[24:27]\n"
"s_setprio 2\n"
"v_mfma_f32_16x16x32_bf16 v[24:27], a[72:75], v[68:71], v[24:27]\n"
"v_cvt_pk_bf16_f32 v16, v16, v17\n"
"v_cvt_pk_bf16_f32 v17, v18, v19\n"
"v_mfma_f32_16x16x32_bf16 v[24:27], a[80:83], v[72:75], v[24:27]\n"
"v_cvt_pk_bf16_f32 v18, v20, v21\n"
"v_cvt_pk_bf16_f32 v19, v22, v23\n"
"v_mfma_f32_16x16x32_bf16 v[24:27], a[88:91], v[76:79], v[24:27]\n"
"s_waitcnt vmcnt(5)\n"
"v_lshlrev_b32 v112, 16, v96\n"
"v_and_b32 v113, s45, v96\n"
"v_mfma_f32_16x16x32_bf16 v[28:31], a[68:71], v[64:67], v[28:31]\n"
"v_mul_f32 v112, s44, v112\n"
"v_mul_f32 v113, s44, v113\n"
"v_mfma_f32_16x16x32_bf16 v[28:31], a[76:79], v[68:71], v[28:31]\n"
"v_cvt_pk_bf16_f32 v96, v112, v113\n"
"v_lshlrev_b32 v112, 16, v97\n"
"v_mfma_f32_16x16x32_bf16 v[28:31], a[84:87], v[72:75], v[28:31]\n"
"v_and_b32 v113, s45, v97\n"
"v_mul_f32 v112, s44, v112\n"
"s_waitcnt lgkmcnt(0)\n"
"s_setprio 0\n"
"v_mfma_f32_16x16x32_bf16 v[28:31], a[92:95], v[76:79], v[28:31]\n"
"s_barrier\n"
"ds_read_b128 v[64:67], v124 offset:8192\n"
"ds_read_b128 v[68:71], v124 offset:9216\n"
"ds_read_b128 v[72:75], v124 offset:10240\n"
"ds_read_b128 v[76:79], v124 offset:11264\n"
"s_add_u32 s41, s52, 0x3e000\n"
"buffer_store_dwordx4 v[16:19], v127, s[64:67], s41 offen sc1\n"
"v_mfma_f32_16x16x32_bf16 v[24:27], a[96:99], v[80:83], v[24:27]\n"
"s_setprio 2\n"
"v_mfma_f32_16x16x32_bf16 v[24:27], a[104:107], v[84:87], v[24:27]\n"
"v_mul_f32 v113, s44, v113\n"
"v_cvt_pk_bf16_f32 v97, v112, v113\n"
"v_mfma_f32_16x16x32_bf16 v[24:27], a[112:115], v[88:91], v[24:27]\n"
"v_lshlrev_b32 v112, 16, v98\n"
"v_and_b32 v113, s45, v98\n"
"v_mfma_f32_16x16x32_bf16 v[24:27], a[120:123], v[92:95], v[24:27]\n"
"v_mul_f32 v112, s44, v112\n"
"v_mul_f32 v113, s44, v113\n"
"v_mfma_f32_16x16x32_bf16 v[28:31], a[100:103], v[80:83], v[28:31]\n"
"v_cvt_pk_bf16_f32 v98, v112, v113\n"
"v_lshlrev_b32 v112, 16, v99\n"
"v_mfma_f32_16x16x32_bf16 v[28:31], a[108:111], v[84:87], v[28:31]\n"
"v_and_b32 v113, s45, v99\n"
"v_mul_f32 v112, s44, v112\n"
"v_mfma_f32_16x16x32_bf16 v[28:31], a[116:119], v[88:91], v[28:31]\n"
"v_mul_f32 v113, s44, v113\n"
"v_cvt_pk_bf16_f32 v99, v112, v113\n"
"v_mfma_f32_16x16x32_bf16 v[28:31], a[124:127], v[92:95], v[28:31]\n"
"s_setprio 0\n"
"ds_read_b128 v[80:83], v124 offset:12288\n"
"ds_read_b128 v[84:87], v124 offset:13312\n"
"ds_read_b128 v[88:91], v124 offset:14336\n"
"ds_read_b128 v[92:95], v124 offset:15360\n"
"ds_read_b128 v[114:117], v124 offset:46080\n"
"ds_write_b128 v126, v[96:99] offset:32768\n"
"s_waitcnt lgkmcnt(6)\n"
"v_mfma_f32_16x16x32_bf16 v[16:19], v[32:35], v[64:67], 0\n"
"s_setprio 2\n"
"v_mfma_f32_16x16x32_bf16 v[16:19], v[40:43], v[68:71], v[16:19]\n"
"s_waitcnt lgkmcnt(1)\n"
"v_add_f32 v114, v114, v115\n"
"v_add_f32 v116, v116, v117\n"
"v_mfma_f32_16x16x32_bf16 v[16:19], v[48:51], v[72:75], v[16:19]\n"
"v_add_f32 v114, v114, v116\n"
"v_rcp_f32 v119, v114\n"
"v_mfma_f32_16x16x32_bf16 v[16:19], v[56:59], v[76:79], v[16:19]\n"
"v_subrev_u32 v118, s46, v114\n"
"v_max_u32 v123, v123, v118\n"
"v_mfma_f32_16x16x32_bf16 v[20:23], v[36:39], v[64:67], 0\n"
"v_mul_f32 v24, v119, v24\n"
"v_mul_f32 v25, v119, v25\n"
"v_mfma_f32_16x16x32_bf16 v[20:23], v[44:47], v[68:71], v[20:23]\n"
"v_mul_f32 v26, v119, v26\n"
"v_mul_f32 v27, v119, v27\n"
"v_mfma_f32_16x16x32_bf16 v[20:23], v[52:55], v[72:75], v[20:23]\n"
"v_mul_f32 v28, v119, v28\n"
"v_mul_f32 v29, v119, v29\n"
"v_mfma_f32_16x16x32_bf16 v[20:23], v[60:63], v[76:79], v[20:23]\n"
"s_setprio 0\n"
"ds_read_b128 v[64:67], v124 offset:16384\n"
"ds_read_b128 v[68:71], v124 offset:17408\n"
"ds_read_b128 v[72:75], v124 offset:18432\n"
"ds_read_b128 v[76:79], v124 offset:19456\n"
"v_mfma_f32_16x16x32_bf16 v[16:19], a[32:35], v[80:83], v[16:19]\n"
"s_setprio 2\n"
"v_mfma_f32_16x16x32_bf16 v[16:19], a[40:43], v[84:87], v[16:19]\n"
"v_mul_f32 v30, v119, v30\n"
"v_mul_f32 v31, v119, v31\n"
"v_mfma_f32_16x16x32_bf16 v[16:19], a[48:51], v[88:91], v[16:19]\n"
"v_fmac_f32 v122, v24, v25\n"
"v_fmac_f32 v122, v26, v27\n"
"v_mfma_f32_16x16x32_bf16 v[16:19], a[56:59], v[92:95], v[16:19]\n"
"v_fmac_f32 v122, v28, v29\n"
"v_fmac_f32 v122, v30, v31\n"
"v_mfma_f32_16x16x32_bf16 v[20:23], a[36:39], v[80:83], v[20:23]\n"
"v_cvt_pk_bf16_f32 v24, v24, v25\n"
"v_cvt_pk_bf16_f32 v25, v26, v27\n"
"v_mfma_f32_16x16x32_bf16 v[20:23], a[44:47], v[84:87], v[20:23]\n"
"v_cvt_pk_bf16_f32 v26, v28, v29\n"
"v_cvt_pk_bf16_f32 v27, v30, v31\n"
"v_mfma_f32_16x16x32_bf16 v[20:23], a[52:55], v[88:91], v[20:23]\n"
"s_waitcnt vmcnt(3)\n"
"v_lshlrev_b32 v112, 16, v100\n"
"v_and_b32 v113, s45, v100\n"
"v_mfma_f32_16x16x32_bf16 v[20:23], a[60:63], v[92:95], v[20:23]\n"
"s_setprio 0\n"
"ds_read_b128 v[80:83], v124 offset:20480\n"
"ds_read_b128 v[84:87], v124 offset:21504\n"
"ds_read_b128 v[88:91], v124 offset:22528\n"
"ds_read_b128 v[92:95], v124 offset:23552\n"
"s_add_u32 s41, s52, 0x3f000\n"
"buffer_store_dwordx4 v[24:27], v127, s[64:67], s41 offen sc1\n"
"s_waitcnt lgkmcnt(4)\n"
"v_mfma_f32_16x16x32_bf16 v[16:19], a[64:67], v[64:67], v[16:19]\n"
"s_setprio 2\n"
"v_mfma_f32_16x16x32_bf16 v[16:19], a[72:75], v[68:71], v[16:19]\n"
"v_mul_f32 v112, s44, v112\n"
"v_mul_f32 v113, s44, v113\n"
"v_mfma_f32_16x16x32_bf16 v[16:19], a[80:83], v[72:75], v[16:19]\n"
"v_cvt_pk_bf16_f32 v100, v112, v113\n"
"v_lshlrev_b32 v112, 16, v101\n"
"v_mfma_f32_16x16x32_bf16 v[16:19], a[88:91], v[76:79], v[16:19]\n"
"v_and_b32 v113, s45, v101\n"
"v_mul_f32 v112, s44, v112\n"
"v_mfma_f32_16x16x32_bf16 v[20:23], a[68:71], v[64:67], v[20:23]\n"
"v_mul_f32 v113, s44, v113\n"
"v_cvt_pk_bf16_f32 v101, v112, v113\n"
"v_mfma_f32_16x16x32_bf16 v[20:23], a[76:79], v[68:71], v[20:23]\n"
"v_lshlrev_b32 v112, 16, v102\n"
"v_and_b32 v113, s45, v102\n"
"v_mfma_f32_16x16x32_bf16 v[20:23], a[84:87], v[72:75], v[20:23]\n"
"v_mul_f32 v112, s44, v112\n"
"v_mul_f32 v113, s44, v113\n"
"s_waitcnt lgkmcnt(0)\n"
"s_setprio 0\n"
"v_mfma_f32_16x16x32_bf16 v[20:23], a[92:95], v[76:79], v[20:23]\n"
"s_barrier\n"
"ds_read_b128 v[64:67], v124 offset:24576\n"
"ds_read_b128 v[68:71], v124 offset:25600\n"
"ds_read_b128 v[72:75], v124 offset:26624\n"
"ds_read_b128 v[76:79], v124 offset:27648\n"
"v_mfma_f32_16x16x32_bf16 v[16:19], a[96:99], v[80:83], v[16:19]\n"
"s_setprio 2\n"
"v_mfma_f32_16x16x32_bf16 v[16:19], a[104:107], v[84:87], v[16:19]\n"
"v_cvt_pk_bf16_f32 v102, v112, v113\n"
"v_lshlrev_b32 v112, 16, v103\n"
"v_mfma_f32_16x16x32_bf16 v[16:19], a[112:115], v[88:91], v[16:19]\n"
"v_and_b32 v113, s45, v103\n"
"v_mfma_f32_16x16x32_bf16 v[16:19], a[120:123], v[92:95], v[16:19]\n"
"v_mul_f32 v112, s44, v112\n"
"v_mfma_f32_16x16x32_bf16 v[20:23], a[100:103], v[80:83], v[20:23]\n"
"v_mul_f32 v113, s44, v113\n"
"v_mfma_f32_16x16x32_bf16 v[20:23], a[108:111], v[84:87], v[20:23]\n"
"v_cvt_pk_bf16_f32 v103, v112, v113\n"
"v_mfma_f32_16x16x32_bf16 v[20:23], a[116:119], v[88:91], v[20:23]\n"
"v_mfma_f32_16x16x32_bf16 v[20:23], a[124:127], v[92:95], v[20:23]\n"
"s_setprio 0\n"
"ds_read_b128 v[80:83], v124 offset:28672\n"
"ds_read_b128 v[84:87], v124 offset:29696\n"
"ds_read_b128 v[88:91], v124 offset:30720\n"
"ds_read_b128 v[92:95], v124 offset:31744\n"
"ds_read_b128 v[114:117], v124 offset:47104\n"
"ds_write_b128 v126, v[100:103] offset:36864\n"
"s_waitcnt lgkmcnt(6)\n"
"v_mfma_f32_16x16x32_bf16 v[24:27], v[32:35], v[64:67], 0\n"
"s_setprio 2\n"
"v_mfma_f32_16x16x32_bf16 v[24:27], v[40:43], v[68:71], v[24:27]\n"
"s_waitcnt lgkmcnt(1)\n"
"v_add_f32 v114, v114, v115\n"
"v_add_f32 v116, v116, v117\n"
"v_mfma_f32_16x16x32_bf16 v[24:27], v[48:51], v[72:75], v[24:27]\n"
"v_add_f32 v114, v114, v116\n"
"v_rcp_f32 v119, v114\n"
"v_mfma_f32_16x16x32_bf16 v[24:27], v[56:59], v[76:79], v[24:27]\n"
"v_subrev_u32 v118, s46, v114\n"
"v_max_u32 v123, v123, v118\n"
"v_mfma_f32_16x16x32_bf16 v[28:31], v[36:39], v[64:67], 0\n"
"v_mul_f32 v16, v119, v16\n"
"v_mul_f32 v17, v119, v17\n"
"v_mfma_f32_16x16x32_bf16 v[28:31], v[44:47], v[68:71], v[28:31]\n"
"v_mul_f32 v18, v119, v18\n"
"v_mul_f32 v19, v119, v19\n"
"v_mfma_f32_16x16x32_bf16 v[28:31], v[52:55], v[72:75], v[28:31]\n"
"v_mul_f32 v20, v119, v20\n"
"v_mul_f32 v21, v119, v21\n"
"v_mfma_f32_16x16x32_bf16 v[28:31], v[60:63], v[76:79], v[28:31]\n"
"s_setprio 0\n"
"ds_read_b128 v[64:67], v124 offset:32768\n"
"ds_read_b128 v[68:71], v124 offset:33792\n"
"ds_read_b128 v[72:75], v124 offset:34816\n"
"ds_read_b128 v[76:79], v124 offset:35840\n"
"v_mfma_f32_16x16x32_bf16 v[24:27], a[32:35], v[80:83], v[24:27]\n"
"s_setprio 2\n"
"v_mfma_f32_16x16x32_bf16 v[24:27], a[40:43], v[84:87], v[24:27]\n"
"v_mul_f32 v22, v119, v22\n"
"v_mul_f32 v23, v119, v23\n"
"v_mfma_f32_16x16x32_bf16 v[24:27], a[48:51], v[88:91], v[24:27]\n"
"v_fmac_f32 v122, v16, v17\n"
"v_fmac_f32 v122, v18, v19\n"
"v_mfma_f32_16x16x32_bf16 v[24:27], a[56:59], v[92:95], v[24:27]\n"
"v_fmac_f32 v122, v20, v21\n"
"v_fmac_f32 v122, v22, v23\n"
"v_mfma_f32_16x16x32_bf16 v[28:31], a[36:39], v[80:83], v[28:31]\n"
"v_cvt_pk_bf16_f32 v16, v16, v17\n"
"v_cvt_pk_bf16_f32 v17, v18, v19\n"
"v_mfma_f32_16x16x32_bf16 v[28:31], a[44:47], v[84:87], v[28:31]\n"
"v_cvt_pk_bf16_f32 v18, v20, v21\n"
"v_cvt_pk_bf16_f32 v19, v22, v23\n"
"v_mfma_f32_16x16x32_bf16 v[28:31], a[52:55], v[88:91], v[28:31]\n"
"v_mfma_f32_16x16x32_bf16 v[28:31], a[60:63], v[92:95], v[28:31]\n"
"s_setprio 0\n"
"ds_read_b128 v[80:83], v124 offset:0\n"
"ds_read_b128 v[84:87], v124 offset:1024\n"
"ds_read_b128 v[88:91], v124 offset:2048\n"
"ds_read_b128 v[92:95], v124 offset:3072\n"
"s_add_u32 s41, s52, 0x40000\n"
"buffer_store_dwordx4 v[16:19], v127, s[64:67], s41 offen sc1\n"
"s_waitcnt lgkmcnt(4)\n"
"v_mfma_f32_16x16x32_bf16 v[24:27], a[64:67], v[64:67], v[24:27]\n"
"s_setprio 2\n"
"v_mfma_f32_16x16x32_bf16 v[24:27], a[72:75], v[68:71], v[24:27]\n"
"v_mfma_f32_16x16x32_bf16 v[24:27], a[80:83], v[72:75], v[24:27]\n"
"v_mfma_f32_16x16x32_bf16 v[24:27], a[88:91], v[76:79], v[24:27]\n"
"v_mfma_f32_16x16x32_bf16 v[28:31], a[68:71], v[64:67], v[28:31]\n"
"v_mfma_f32_16x16x32_bf16 v[28:31], a[76:79], v[68:71], v[28:31]\n"
"v_mfma_f32_16x16x32_bf16 v[28:31], a[84:87], v[72:75], v[28:31]\n"
"s_waitcnt lgkmcnt(0)\n"
"s_setprio 0\n"
"v_mfma_f32_16x16x32_bf16 v[28:31], a[92:95], v[76:79], v[28:31]\n"
"s_barrier\n"
"ds_read_b128 v[64:67], v124 offset:4096\n"
"ds_read_b128 v[68:71], v124 offset:5120\n"
"ds_read_b128 v[72:75], v124 offset:6144\n"
"ds_read_b128 v[76:79], v124 offset:7168\n"
"v_mfma_f32_16x16x32_bf16 v[24:27], a[96:99], v[80:83], v[24:27]\n"
"s_setprio 2\n"
"v_mfma_f32_16x16x32_bf16 v[24:27], a[104:107], v[84:87], v[24:27]\n"
"v_mfma_f32_16x16x32_bf16 v[24:27], a[112:115], v[88:91], v[24:27]\n"
"v_mfma_f32_16x16x32_bf16 v[24:27], a[120:123], v[92:95], v[24:27]\n"
"v_mfma_f32_16x16x32_bf16 v[28:31], a[100:103], v[80:83], v[28:31]\n"
"v_mfma_f32_16x16x32_bf16 v[28:31], a[108:111], v[84:87], v[28:31]\n"
"v_mfma_f32_16x16x32_bf16 v[28:31], a[116:119], v[88:91], v[28:31]\n"
"v_mfma_f32_16x16x32_bf16 v[28:31], a[124:127], v[92:95], v[28:31]\n"
"s_setprio 0\n"
"ds_read_b128 v[80:83], v124 offset:8192\n"
"ds_read_b128 v[84:87], v124 offset:9216\n"
"ds_read_b128 v[88:91], v124 offset:10240\n"
"ds_read_b128 v[92:95], v124 offset:11264\n"
"ds_read_b128 v[114:117], v124 offset:45056\n"
"s_waitcnt lgkmcnt(5)\n"
"v_mfma_f32_16x16x32_bf16 v[16:19], v[32:35], v[64:67], 0\n"
"s_setprio 2\n"
"v_mfma_f32_16x16x32_bf16 v[16:19], v[40:43], v[68:71], v[16:19]\n"
"s_waitcnt lgkmcnt(0)\n"
"v_add_f32 v114, v114, v115\n"
"v_add_f32 v116, v116, v117\n"
"v_mfma_f32_16x16x32_bf16 v[16:19], v[48:51], v[72:75], v[16:19]\n"
"v_add_f32 v114, v114, v116\n"
"v_rcp_f32 v119, v114\n"
"v_mfma_f32_16x16x32_bf16 v[16:19], v[56:59], v[76:79], v[16:19]\n"
"v_subrev_u32 v118, s46, v114\n"
"v_max_u32 v123, v123, v118\n"
"v_mfma_f32_16x16x32_bf16 v[20:23], v[36:39], v[64:67], 0\n"
"v_mul_f32 v24, v119, v24\n"
"v_mul_f32 v25, v119, v25\n"
"v_mfma_f32_16x16x32_bf16 v[20:23], v[44:47], v[68:71], v[20:23]\n"
"v_mul_f32 v26, v119, v26\n"
"v_mul_f32 v27, v119, v27\n"
"v_mfma_f32_16x16x32_bf16 v[20:23], v[52:55], v[72:75], v[20:23]\n"
"v_mul_f32 v28, v119, v28\n"
"v_mul_f32 v29, v119, v29\n"
"v_mfma_f32_16x16x32_bf16 v[20:23], v[60:63], v[76:79], v[20:23]\n"
"s_setprio 0\n"
"ds_read_b128 v[64:67], v124 offset:12288\n"
"ds_read_b128 v[68:71], v124 offset:13312\n"
"ds_read_b128 v[72:75], v124 offset:14336\n"
"ds_read_b128 v[76:79], v124 offset:15360\n"
"v_mfma_f32_16x16x32_bf16 v[16:19], a[32:35], v[80:83], v[16:19]\n"
"s_setprio 2\n"
"v_mfma_f32_16x16x32_bf16 v[16:19], a[40:43], v[84:87], v[16:19]\n"
"v_mul_f32 v30, v119, v30\n"
"v_mul_f32 v31, v119, v31\n"
"v_mfma_f32_16x16x32_bf16 v[16:19], a[48:51], v[88:91], v[16:19]\n"
"v_fmac_f32 v122, v24, v25\n"
"v_fmac_f32 v122, v26, v27\n"
"v_mfma_f32_16x16x32_bf16 v[16:19], a[56:59], v[92:95], v[16:19]\n"
"v_fmac_f32 v122, v28, v29\n"
"v_fmac_f32 v122, v30, v31\n"
"v_mfma_f32_16x16x32_bf16 v[20:23], a[36:39], v[80:83], v[20:23]\n"
"v_cvt_pk_bf16_f32 v24, v24, v25\n"
"v_cvt_pk_bf16_f32 v25, v26, v27\n"
"v_mfma_f32_16x16x32_bf16 v[20:23], a[44:47], v[84:87], v[20:23]\n"
"v_cvt_pk_bf16_f32 v26, v28, v29\n"
"v_cvt_pk_bf16_f32 v27, v30, v31\n"
"v_mfma_f32_16x16x32_bf16 v[20:23], a[52:55], v[88:91], v[20:23]\n"
"v_mfma_f32_16x16x32_bf16 v[20:23], a[60:63], v[92:95], v[20:23]\n"
"s_setprio 0\n"
"ds_read_b128 v[80:83], v124 offset:16384\n"
"ds_read_b128 v[84:87], v124 offset:17408\n"
"ds_read_b128 v[88:91], v124 offset:18432\n"
"ds_read_b128 v[92:95], v124 offset:19456\n"
"s_add_u32 s41, s52, 0x41000\n"
"buffer_store_dwordx4 v[24:27], v127, s[64:67], s41 offen sc1\n"
"s_waitcnt lgkmcnt(4)\n"
"v_mfma_f32_16x16x32_bf16 v[16:19], a[64:67], v[64:67], v[16:19]\n"
"s_setprio 2\n"
"v_mfma_f32_16x16x32_bf16 v[16:19], a[72:75], v[68:71], v[16:19]\n"
"v_mfma_f32_16x16x32_bf16 v[16:19], a[80:83], v[72:75], v[16:19]\n"
"v_mfma_f32_16x16x32_bf16 v[16:19], a[88:91], v[76:79], v[16:19]\n"
"v_mfma_f32_16x16x32_bf16 v[20:23], a[68:71], v[64:67], v[20:23]\n"
"v_mfma_f32_16x16x32_bf16 v[20:23], a[76:79], v[68:71], v[20:23]\n"
"v_mfma_f32_16x16x32_bf16 v[20:23], a[84:87], v[72:75], v[20:23]\n"
"s_waitcnt lgkmcnt(0)\n"
"s_setprio 0\n"
"v_mfma_f32_16x16x32_bf16 v[20:23], a[92:95], v[76:79], v[20:23]\n"
"s_barrier\n"
"ds_read_b128 v[64:67], v124 offset:20480\n"
"ds_read_b128 v[68:71], v124 offset:21504\n"
"ds_read_b128 v[72:75], v124 offset:22528\n"
"ds_read_b128 v[76:79], v124 offset:23552\n"
"v_mfma_f32_16x16x32_bf16 v[16:19], a[96:99], v[80:83], v[16:19]\n"
"s_setprio 2\n"
"v_mfma_f32_16x16x32_bf16 v[16:19], a[104:107], v[84:87], v[16:19]\n"
"v_mfma_f32_16x16x32_bf16 v[16:19], a[112:115], v[88:91], v[16:19]\n"
"v_mfma_f32_16x16x32_bf16 v[16:19], a[120:123], v[92:95], v[16:19]\n"
"v_mfma_f32_16x16x32_bf16 v[20:23], a[100:103], v[80:83], v[20:23]\n"
"v_mfma_f32_16x16x32_bf16 v[20:23], a[108:111], v[84:87], v[20:23]\n"
"v_mfma_f32_16x16x32_bf16 v[20:23], a[116:119], v[88:91], v[20:23]\n"
"v_mfma_f32_16x16x32_bf16 v[20:23], a[124:127], v[92:95], v[20:23]\n"
"s_setprio 0\n"
"ds_read_b128 v[80:83], v124 offset:24576\n"
"ds_read_b128 v[84:87], v124 offset:25600\n"
"ds_read_b128 v[88:91], v124 offset:26624\n"
"ds_read_b128 v[92:95], v124 offset:27648\n"
"ds_read_b128 v[114:117], v124 offset:46080\n"
"s_waitcnt lgkmcnt(5)\n"
"v_mfma_f32_16x16x32_bf16 v[24:27], v[32:35], v[64:67], 0\n"
"s_setprio 2\n"
"v_mfma_f32_16x16x32_bf16 v[24:27], v[40:43], v[68:71], v[24:27]\n"
"s_waitcnt lgkmcnt(0)\n"
"v_add_f32 v114, v114, v115\n"
"v_add_f32 v116, v116, v117\n"
"v_mfma_f32_16x16x32_bf16 v[24:27], v[48:51], v[72:75], v[24:27]\n"
"v_add_f32 v114, v114, v116\n"
"v_rcp_f32 v119, v114\n"
"v_mfma_f32_16x16x32_bf16 v[24:27], v[56:59], v[76:79], v[24:27]\n"
"v_subrev_u32 v118, s46, v114\n"
"v_max_u32 v123, v123, v118\n"
"v_mfma_f32_16x16x32_bf16 v[28:31], v[36:39], v[64:67], 0\n"
"v_mul_f32 v16, v119, v16\n"
"v_mul_f32 v17, v119, v17\n"
"v_mfma_f32_16x16x32_bf16 v[28:31], v[44:47], v[68:71], v[28:31]\n"
"v_mul_f32 v18, v119, v18\n"
"v_mul_f32 v19, v119, v19\n"
"v_mfma_f32_16x16x32_bf16 v[28:31], v[52:55], v[72:75], v[28:31]\n"
"v_mul_f32 v20, v119, v20\n"
"v_mul_f32 v21, v119, v21\n"
"v_mfma_f32_16x16x32_bf16 v[28:31], v[60:63], v[76:79], v[28:31]\n"
"s_setprio 0\n"
"ds_read_b128 v[64:67], v124 offset:28672\n"
"ds_read_b128 v[68:71], v124 offset:29696\n"
"ds_read_b128 v[72:75], v124 offset:30720\n"
"ds_read_b128 v[76:79], v124 offset:31744\n"
"v_mfma_f32_16x16x32_bf16 v[24:27], a[32:35], v[80:83], v[24:27]\n"
"s_setprio 2\n"
"v_mfma_f32_16x16x32_bf16 v[24:27], a[40:43], v[84:87], v[24:27]\n"
"v_mul_f32 v22, v119, v22\n"
"v_mul_f32 v23, v119, v23\n"
"v_mfma_f32_16x16x32_bf16 v[24:27], a[48:51], v[88:91], v[24:27]\n"
"v_fmac_f32 v122, v16, v17\n"
"v_fmac_f32 v122, v18, v19\n"
"v_mfma_f32_16x16x32_bf16 v[24:27], a[56:59], v[92:95], v[24:27]\n"
"v_fmac_f32 v122, v20, v21\n"
"v_fmac_f32 v122, v22, v23\n"
"v_mfma_f32_16x16x32_bf16 v[28:31], a[36:39], v[80:83], v[28:31]\n"
"v_cvt_pk_bf16_f32 v16, v16, v17\n"
"v_cvt_pk_bf16_f32 v17, v18, v19\n"
"v_mfma_f32_16x16x32_bf16 v[28:31], a[44:47], v[84:87], v[28:31]\n"
"v_cvt_pk_bf16_f32 v18, v20, v21\n"
"v_cvt_pk_bf16_f32 v19, v22, v23\n"
"v_mfma_f32_16x16x32_bf16 v[28:31], a[52:55], v[88:91], v[28:31]\n"
"v_mfma_f32_16x16x32_bf16 v[28:31], a[60:63], v[92:95], v[28:31]\n"
"s_setprio 0\n"
"ds_read_b128 v[80:83], v124 offset:32768\n"
"ds_read_b128 v[84:87], v124 offset:33792\n"
"ds_read_b128 v[88:91], v124 offset:34816\n"
"ds_read_b128 v[92:95], v124 offset:35840\n"
"s_add_u32 s41, s52, 0x42000\n"
"buffer_store_dwordx4 v[16:19], v127, s[64:67], s41 offen sc1\n"
"s_waitcnt lgkmcnt(4)\n"
"v_mfma_f32_16x16x32_bf16 v[24:27], a[64:67], v[64:67], v[24:27]\n"
"s_setprio 2\n"
"v_mfma_f32_16x16x32_bf16 v[24:27], a[72:75], v[68:71], v[24:27]\n"
"v_mfma_f32_16x16x32_bf16 v[24:27], a[80:83], v[72:75], v[24:27]\n"
"v_mfma_f32_16x16x32_bf16 v[24:27], a[88:91], v[76:79], v[24:27]\n"
"v_mfma_f32_16x16x32_bf16 v[28:31], a[68:71], v[64:67], v[28:31]\n"
"v_mfma_f32_16x16x32_bf16 v[28:31], a[76:79], v[68:71], v[28:31]\n"
"v_mfma_f32_16x16x32_bf16 v[28:31], a[84:87], v[72:75], v[28:31]\n"
"s_waitcnt lgkmcnt(0)\n"
"s_setprio 0\n"
"v_mfma_f32_16x16x32_bf16 v[28:31], a[92:95], v[76:79], v[28:31]\n"
"s_barrier\n"
"ds_read_b128 v[64:67], v124 offset:0\n"
"ds_read_b128 v[68:71], v124 offset:1024\n"
"ds_read_b128 v[72:75], v124 offset:2048\n"
"ds_read_b128 v[76:79], v124 offset:3072\n"
"v_mfma_f32_16x16x32_bf16 v[24:27], a[96:99], v[80:83], v[24:27]\n"
"s_setprio 2\n"
"v_mfma_f32_16x16x32_bf16 v[24:27], a[104:107], v[84:87], v[24:27]\n"
"v_mfma_f32_16x16x32_bf16 v[24:27], a[112:115], v[88:91], v[24:27]\n"
"v_mfma_f32_16x16x32_bf16 v[24:27], a[120:123], v[92:95], v[24:27]\n"
"v_mfma_f32_16x16x32_bf16 v[28:31], a[100:103], v[80:83], v[28:31]\n"
"v_mfma_f32_16x16x32_bf16 v[28:31], a[108:111], v[84:87], v[28:31]\n"
"v_mfma_f32_16x16x32_bf16 v[28:31], a[116:119], v[88:91], v[28:31]\n"
"v_mfma_f32_16x16x32_bf16 v[28:31], a[124:127], v[92:95], v[28:31]\n"
"s_setprio 0\n"
"ds_read_b128 v[80:83], v124 offset:4096\n"
"ds_read_b128 v[84:87], v124 offset:5120\n"
"ds_read_b128 v[88:91], v124 offset:6144\n"
"ds_read_b128 v[92:95], v124 offset:7168\n"
"ds_read_b128 v[114:117], v124 offset:47104\n"
"s_waitcnt lgkmcnt(5)\n"
"v_mfma_f32_16x16x32_bf16 v[16:19], v[32:35], v[64:67], 0\n"
"s_setprio 2\n"
"v_mfma_f32_16x16x32_bf16 v[16:19], v[40:43], v[68:71], v[16:19]\n"
"s_waitcnt lgkmcnt(0)\n"
"v_add_f32 v114, v114, v115\n"
"v_add_f32 v116, v116, v117\n"
"v_mfma_f32_16x16x32_bf16 v[16:19], v[48:51], v[72:75], v[16:19]\n"
"v_add_f32 v114, v114, v116\n"
"v_rcp_f32 v119, v114\n"
"v_mfma_f32_16x16x32_bf16 v[16:19], v[56:59], v[76:79], v[16:19]\n"
"v_subrev_u32 v118, s46, v114\n"
"v_max_u32 v123, v123, v118\n"
"v_mfma_f32_16x16x32_bf16 v[20:23], v[36:39], v[64:67], 0\n"
"v_mul_f32 v24, v119, v24\n"
"v_mul_f32 v25, v119, v25\n"
"v_mfma_f32_16x16x32_bf16 v[20:23], v[44:47], v[68:71], v[20:23]\n"
"v_mul_f32 v26, v119, v26\n"
"v_mul_f32 v27, v119, v27\n"
"v_mfma_f32_16x16x32_bf16 v[20:23], v[52:55], v[72:75], v[20:23]\n"
"v_mul_f32 v28, v119, v28\n"
"v_mul_f32 v29, v119, v29\n"
"v_mfma_f32_16x16x32_bf16 v[20:23], v[60:63], v[76:79], v[20:23]\n"
"s_setprio 0\n"
"ds_read_b128 v[64:67], v124 offset:8192\n"
"ds_read_b128 v[68:71], v124 offset:9216\n"
"ds_read_b128 v[72:75], v124 offset:10240\n"
"ds_read_b128 v[76:79], v124 offset:11264\n"
"v_mfma_f32_16x16x32_bf16 v[16:19], a[32:35], v[80:83], v[16:19]\n"
"s_setprio 2\n"
"v_mfma_f32_16x16x32_bf16 v[16:19], a[40:43], v[84:87], v[16:19]\n"
"v_mul_f32 v30, v119, v30\n"
"v_mul_f32 v31, v119, v31\n"
"v_mfma_f32_16x16x32_bf16 v[16:19], a[48:51], v[88:91], v[16:19]\n"
"v_fmac_f32 v122, v24, v25\n"
"v_fmac_f32 v122, v26, v27\n"
"v_mfma_f32_16x16x32_bf16 v[16:19], a[56:59], v[92:95], v[16:19]\n"
"v_fmac_f32 v122, v28, v29\n"
"v_fmac_f32 v122, v30, v31\n"
"v_mfma_f32_16x16x32_bf16 v[20:23], a[36:39], v[80:83], v[20:23]\n"
"v_cvt_pk_bf16_f32 v24, v24, v25\n"
"v_cvt_pk_bf16_f32 v25, v26, v27\n"
"v_mfma_f32_16x16x32_bf16 v[20:23], a[44:47], v[84:87], v[20:23]\n"
"v_cvt_pk_bf16_f32 v26, v28, v29\n"
"v_cvt_pk_bf16_f32 v27, v30, v31\n"
"v_mfma_f32_16x16x32_bf16 v[20:23], a[52:55], v[88:91], v[20:23]\n"
"v_mfma_f32_16x16x32_bf16 v[20:23], a[60:63], v[92:95], v[20:23]\n"
"s_setprio 0\n"
"ds_read_b128 v[80:83], v124 offset:12288\n"
"ds_read_b128 v[84:87], v124 offset:13312\n"
"ds_read_b128 v[88:91], v124 offset:14336\n"
"ds_read_b128 v[92:95], v124 offset:15360\n"
"s_add_u32 s41, s52, 0x43000\n"
"buffer_store_dwordx4 v[24:27], v127, s[64:67], s41 offen sc1\n"
"s_waitcnt lgkmcnt(4)\n"
"v_mfma_f32_16x16x32_bf16 v[16:19], a[64:67], v[64:67], v[16:19]\n"
"s_setprio 2\n"
"v_mfma_f32_16x16x32_bf16 v[16:19], a[72:75], v[68:71], v[16:19]\n"
"v_mfma_f32_16x16x32_bf16 v[16:19], a[80:83], v[72:75], v[16:19]\n"
"v_mfma_f32_16x16x32_bf16 v[16:19], a[88:91], v[76:79], v[16:19]\n"
"v_mfma_f32_16x16x32_bf16 v[20:23], a[68:71], v[64:67], v[20:23]\n"
"v_mfma_f32_16x16x32_bf16 v[20:23], a[76:79], v[68:71], v[20:23]\n"
"v_mfma_f32_16x16x32_bf16 v[20:23], a[84:87], v[72:75], v[20:23]\n"
"v_mfma_f32_16x16x32_bf16 v[20:23], a[92:95], v[76:79], v[20:23]\n"
"s_setprio 0\n"
"s_waitcnt lgkmcnt(0)\n"
"v_mfma_f32_16x16x32_bf16 v[16:19], a[96:99], v[80:83], v[16:19]\n"
"s_setprio 2\n"
"v_mfma_f32_16x16x32_bf16 v[16:19], a[104:107], v[84:87], v[16:19]\n"
"v_mfma_f32_16x16x32_bf16 v[16:19], a[112:115], v[88:91], v[16:19]\n"
"v_mfma_f32_16x16x32_bf16 v[16:19], a[120:123], v[92:95], v[16:19]\n"
"v_mfma_f32_16x16x32_bf16 v[20:23], a[100:103], v[80:83], v[20:23]\n"
"v_mfma_f32_16x16x32_bf16 v[20:23], a[108:111], v[84:87], v[20:23]\n"
"v_mfma_f32_16x16x32_bf16 v[20:23], a[116:119], v[88:91], v[20:23]\n"
"v_mfma_f32_16x16x32_bf16 v[20:23], a[124:127], v[92:95], v[20:23]\n"
"s_setprio 0\n"
"ds_read_b128 v[114:117], v124 offset:36864\n"
"s_waitcnt lgkmcnt(0)\n"
"v_add_f32 v114, v114, v115\n"
"v_add_f32 v116, v116, v117\n"
"v_add_f32 v114, v114, v116\n"
"v_rcp_f32 v119, v114\n"
"v_subrev_u32 v118, s46, v114\n"
"v_max_u32 v123, v123, v118\n"
"v_mul_f32 v16, v119, v16\n"
"v_mul_f32 v17, v119, v17\n"
"v_mul_f32 v18, v119, v18\n"
"v_mul_f32 v19, v119, v19\n"
"v_mul_f32 v20, v119, v20\n"
"v_mul_f32 v21, v119, v21\n"
"v_mul_f32 v22, v119, v22\n"
"v_mul_f32 v23, v119, v23\n"
"v_fmac_f32 v122, v16, v17\n"
"v_fmac_f32 v122, v18, v19\n"
"v_fmac_f32 v122, v20, v21\n"
"v_fmac_f32 v122, v22, v23\n"
"v_cvt_pk_bf16_f32 v16, v16, v17\n"
"v_cvt_pk_bf16_f32 v17, v18, v19\n"
"v_cvt_pk_bf16_f32 v18, v20, v21\n"
"v_cvt_pk_bf16_f32 v19, v22, v23\n"
"s_add_u32 s41, s52, 0x44000\n"
"buffer_store_dwordx4 v[16:19], v127, s[64:67], s41 offen sc1\n"
"v_and_b32 v122, 0x7fffffff, v122\n"
"v_lshrrev_b32 v122, 23, v122\n"
"v_add_u32 v122, 1, v122\n"
"v_lshrrev_b32 v122, 8, v122\n"
"v_lshlrev_b32 v122, 31, v122\n"
"v_or_b32 v123, v123, v122\n"
"v_cmp_lt_u32 vcc, s47, v123\n"
"s_cmp_lg_u64 vcc, 0\n"
"s_cselect_b32 s48, 1, 0\n"
"s_lshl_b32 s49, s38, 2\n"
"s_add_u32 s49, s49, 0xbc00\n"
"v_mov_b32 v112, s49\n"
"v_mov_b32 v113, s48\n"
"ds_write_b32 v112, v113 offset:0\n"
"s_waitcnt lgkmcnt(0)\n"
"s_barrier\n"
"v_mov_b32 v112, 0xbc00\n"
"ds_read_b128 v[114:117], v112 offset:0\n"
"s_waitcnt lgkmcnt(0)\n"
"v_or_b32 v114, v114, v115\n"
"v_or_b32 v116, v116, v117\n"
"v_or_b32 v114, v114, v116\n"
"s_nop 0\n"
"v_readfirstlane_b32 s58, v114\n"
"s_nop 3\n"
"L_done:\n"
"s_mov_b32 %0, s58\n"
      : "=s"(flag)
      : "s"(q), "s"(k), "s"(v), "s"(o), "s"(wave), "s"(blk), "s"((unsigned)(size_t)lds)
      : "v0", "v1", "v2", "v3", "v4", "v5", "v6", "v7", "v8", "v9", "v10", "v11", "v12", "v13", "v14", "v15", "v16", "v17", "v18", "v19", "v20", "v21", "v22", "v23", "v24", "v25", "v26", "v27", "v28", "v29", "v30", "v31", "v32", "v33", "v34", "v35", "v36", "v37", "v38", "v39", "v40", "v41", "v42", "v43", "v44", "v45", "v46", "v47", "v48", "v49", "v50", "v51", "v52", "v53", "v54", "v55", "v56", "v57", "v58", "v59", "v60", "v61", "v62", "v63", "v64", "v65", "v66", "v67", "v68", "v69", "v70", "v71", "v72", "v73", "v74", "v75", "v76", "v77", "v78", "v79", "v80", "v81", "v82", "v83", "v84", "v85", "v86", "v87", "v88", "v89", "v90", "v91", "v92", "v93", "v94", "v95", "v96", "v97", "v98", "v99", "v100", "v101", "v102", "v103", "v104", "v105", "v106", "v107", "v108", "v109", "v110", "v111", "v112", "v113", "v114", "v115", "v116", "v117", "v118", "v119", "v120", "v121", "v122", "v123", "v124", "v125", "v126", "v127", "a0", "a1", "a2", "a3", "a4", "a5", "a6", "a7", "a8", "a9", "a10", "a11", "a12", "a13", "a14", "a15", "a16", "a17", "a18", "a19", "a20", "a21", "a22", "a23", "a24", "a25", "a26", "a27", "a28", "a29", "a30", "a31", "a32", "a33", "a34", "a35", "a36", "a37", "a38", "a39", "a40", "a41", "a42", "a43", "a44", "a45", "a46", "a47", "a48", "a49", "a50", "a51", "a52", "a53", "a54", "a55", "a56", "a57", "a58", "a59", "a60", "a61", "a62", "a63", "a64", "a65", "a66", "a67", "a68", "a69", "a70", "a71", "a72", "a73", "a74", "a75", "a76", "a77", "a78", "a79", "a80", "a81", "a82", "a83", "a84", "a85", "a86", "a87", "a88", "a89", "a90", "a91", "a92", "a93", "a94", "a95", "a96", "a97", "a98", "a99", "a100", "a101", "a102", "a103", "a104", "a105", "a106", "a107", "a108", "a109", "a110", "a111", "a112", "a113", "a114", "a115", "a116", "a117", "a118", "a119", "a120", "a121", "a122", "a123", "a124", "a125", "a126", "a127", "s20", "s21", "s22", "s23", "s24", "s25", "s26", "s27", "s28", "s29", "s30", "s31", "s36", "s37", "s38", "s39", "s40", "s41", "s42", "s43", "s44", "s45", "s46", "s47", "s48", "s49", "s50", "s51", "s52", "s53", "s54", "s55", "s56", "s57", "s58", "s59", "s60", "s61", "s62", "s63", "s64", "s65", "s66", "s67", "s68", "s69", "s70", "s71", "s72", "s73", "s74", "s75", "vcc", "scc", "memory");
  if (flag) {
    asm volatile("s_waitcnt vmcnt(0)\n\ts_barrier" ::: "memory");
    const int lane = (int)__builtin_amdgcn_mbcnt_hi(~0u, __builtin_amdgcn_mbcnt_lo(~0u, 0u));
    safe_rows((const unsigned*)q, (const unsigned*)k, (const unsigned*)v, (unsigned*)o, lds, (int)((blk & 7) * 16 + (blk >> 4)), (int)((blk >> 3) & 1), (int)(wave & 3), lane);
  }
}

extern "C" void kernel_launch(void* const* d_in, const int* in_sizes, int n_in, void* d_out, int out_size, void* d_ws, size_t ws_size, hipStream_t stream) {
  (void)in_sizes; (void)n_in; (void)out_size; (void)ws_size; (void)d_ws;
  mha_kvs_kernel<<<256, 512, 0, stream>>>(d_in[0], d_in[1], d_in[2], d_out);
}
```
